```python
import torch

def _softmax(x, axis=-1):
    return torch.softmax(x, dim=axis)
def _gelu(x):
    return torch.nn.functional.gelu(x, approximate="tanh")
def _silu(x):
    return torch.nn.functional.silu(x)
def _sigmoid(x):
    return torch.sigmoid(x)
def _layernorm(x, axis=-1, eps=1e-5):
    m = x.mean(dim=axis, keepdim=True)
    v = x.var(dim=axis, keepdim=True, unbiased=False)
    return (x - m) / torch.sqrt(v + eps)
def _scatter_add(tgt, idx, src, axis=0):
    return tgt.clone().index_add_(axis, idx, src)
def _segment_sum(src, seg, n):
    out = torch.zeros((n,) + src.shape[1:], dtype=src.dtype)
    return out.index_add_(0, seg, src)
def _pad(x, pw):
    flat = tuple(v for pair in reversed(pw) for v in pair)
    return torch.nn.functional.pad(x, flat)

kernel_name = 'gated_mix__sliding_block__linear_attn_block__prompt'

def setup_inputs():

    _w = (lambda a: (lambda t: t.to(torch.bfloat16) if t.is_floating_point() else t)(torch.as_tensor(a)))
    def _A_setup():
        B, S, D = 1, 2048, 640
        return {
            "x": _w((lambda *s: torch.randn(*s, dtype=torch.bfloat16) * 0.05)(B, S, D)),
            "wqkv": _w((lambda *s: torch.randn(*s, dtype=torch.bfloat16) * 0.8)(D, 3 * D)),
            "wo": _w((lambda *s: torch.randn(*s, dtype=torch.bfloat16) * 0.05)(D, D)),
        }
    _A_in = _A_setup()

    def _B_setup():
        B, S, D = 1, 2048, 640
        return {
            "x": _w((lambda *s: torch.randn(*s, dtype=torch.bfloat16) * 0.05)(B, S, D)),
            "wqkv": _w((lambda *s: torch.randn(*s, dtype=torch.bfloat16) * 1.6)(D, 3 * D)),
            "wo": _w((lambda *s: torch.randn(*s, dtype=torch.bfloat16) * 0.4)(D, D)),
        }
    _B_in = _B_setup()

    return {
        "A_x": _A_in["x"],
        "A_wqkv": _A_in["wqkv"],
        "A_wo": _A_in["wo"],
        "B_x": _B_in["x"],
        "B_wqkv": _B_in["wqkv"],
        "B_wo": _B_in["wo"],
        "gate_w": _w((lambda *s: torch.randn(*s, dtype=torch.bfloat16) * 0.05)(640) * 16.0),
    }

def reference(A_x, A_wqkv, A_wo, B_x, B_wqkv, B_wo, gate_w):
    def _A_ref(x, wqkv, wo):
        B, S, D = x.shape
        H = 32; Dh = D // H; W = 256
        qkv = (lambda x, *d: x.permute(*d))((x @ wqkv).reshape(B, S, 3, H, Dh), 2, 0, 3, 1, 4)
        q, k, v = qkv[0], qkv[1], qkv[2]
        scores = torch.einsum("bhqd,bhkd->bhqk", q, k) * (Dh ** -0.5)
        idx = torch.arange(S)
        mask = (idx[:, None] - idx[None, :] >= 0) & (idx[:, None] - idx[None, :] < W)
        scores = torch.where(mask[None, None], scores, -1e9)
        p = _softmax(scores, axis=-1)
        o = torch.einsum("bhqk,bhkd->bhqd", p, v)
        return (lambda x, *d: x.permute(*d))(o, 0, 2, 1, 3).reshape(B, S, D) @ wo

    def _B_ref(x, wqkv, wo):
        B, S, D = x.shape
        H = 32; Dh = D // H
        qkv = (lambda x, *d: x.permute(*d))((x @ wqkv).reshape(B, S, 3, H, Dh), 2, 0, 3, 1, 4)
        q, k, v = qkv[0], qkv[1], qkv[2]
        phi = lambda t: torch.where(t > 0, t + 1, torch.exp(t)) + 1e-6
        pq, pk = phi(q), phi(k)
        kv = torch.einsum("bhsd,bhse->bhde", pk, v)
        z = torch.einsum("bhsd->bhd", pk)
        num = torch.einsum("bhqd,bhde->bhqe", pq, kv)
        den = torch.einsum("bhqd,bhd->bhq", pq, z)[..., None] + 1e-6
        o = num / den
        return (lambda x, *d: x.permute(*d))(o, 0, 2, 1, 3).reshape(B, S, D) @ wo

    x = A_x
    g = _sigmoid((x * gate_w).sum(-1, keepdims=True))
    a = _A_ref(x, A_wqkv, A_wo)
    b = _B_ref(x, B_wqkv, B_wo)
    return g * a + (1 - g) * b
```

```cpp
#include <hip/hip_runtime.h>
#include <hip/hip_bf16.h>
#include <stdint.h>

typedef unsigned short u16;
__device__ __forceinline__ float b2f(u16 v) { return __uint_as_float(((unsigned)v) << 16); }
__device__ __forceinline__ u16 f2b(float f) { __hip_bfloat16 h = __float2bfloat16(f); return *(u16*)&h; }

constexpr int S = 2048, D = 640, H = 32, DH = 20, W = 256;

__global__ void k_qkv(const u16* x, const u16* wa, const u16* wb, u16* qkv) {
    int n = blockIdx.x * 256 + threadIdx.x;
    int s = blockIdx.y;
    const u16* w = n < 1920 ? wa : wb;
    int nn = n < 1920 ? n : n - 1920;
    float acc = 0.f;
    for (int k = 0; k < D; ++k) acc += b2f(x[s * D + k]) * b2f(w[k * 1920 + nn]);
    qkv[s * 3840 + n] = f2b(acc);
}

__global__ void k_attn(const u16* qkv, u16* o) {
    int s = blockIdx.x * 64 + threadIdx.x;
    int h = blockIdx.y;
    float q[DH];
    for (int d = 0; d < DH; ++d) q[d] = b2f(qkv[s * 3840 + h * DH + d]);
    float m = -1e30f;
    int lo = s - W + 1 < 0 ? 0 : s - W + 1;
    const float sc = 0.22360679774997896f;
    for (int j = lo; j <= s; ++j) {
        float a = 0.f;
        for (int d = 0; d < DH; ++d) a += q[d] * b2f(qkv[j * 3840 + 640 + h * DH + d]);
        m = fmaxf(m, a * sc);
    }
    float l = 0.f, acc[DH];
    for (int d = 0; d < DH; ++d) acc[d] = 0.f;
    for (int j = lo; j <= s; ++j) {
        float a = 0.f;
        for (int d = 0; d < DH; ++d) a += q[d] * b2f(qkv[j * 3840 + 640 + h * DH + d]);
        float p = __expf(a * sc - m);
        l += p;
        for (int d = 0; d < DH; ++d) acc[d] += p * b2f(qkv[j * 3840 + 1280 + h * DH + d]);
    }
    for (int d = 0; d < DH; ++d) o[s * 1280 + h * DH + d] = f2b(acc[d] / l);
}

__device__ __forceinline__ float phi(float t) { return (t > 0.f ? t + 1.f : __expf(t)) + 1e-6f; }

__global__ void k_kv(const u16* qkv, float* kv) {
    int h = blockIdx.x;
    int t = threadIdx.x;
    if (t >= 420) return;
    int d = t / 21, e = t % 21;
    float acc = 0.f;
    for (int s = 0; s < S; ++s) {
        float pk = phi(b2f(qkv[s * 3840 + 1920 + 640 + h * DH + d]));
        float v = e < 20 ? b2f(qkv[s * 3840 + 1920 + 1280 + h * DH + e]) : 1.f;
        acc += pk * v;
    }
    kv[h * 420 + t] = acc;
}

__global__ void k_lin(const u16* qkv, const float* kv, u16* o) {
    int s = blockIdx.x * 64 + threadIdx.x;
    int h = blockIdx.y;
    float acc[21];
    for (int e = 0; e < 21; ++e) acc[e] = 0.f;
    for (int d = 0; d < DH; ++d) {
        float pq = phi(b2f(qkv[s * 3840 + 1920 + h * DH + d]));
        for (int e = 0; e < 21; ++e) acc[e] += pq * kv[h * 420 + d * 21 + e];
    }
    float den = acc[20] + 1e-6f;
    for (int e = 0; e < DH; ++e) o[s * 1280 + 640 + h * DH + e] = f2b(acc[e] / den);
}

__global__ void k_out(const u16* x, const u16* gw, const u16* o, const u16* woa, const u16* wob, u16* out) {
    int n = blockIdx.x * 64 + threadIdx.x;
    int s = blockIdx.y;
    float gs = 0.f;
    for (int k = 0; k < D; ++k) gs += b2f(x[s * D + k]) * b2f(gw[k]);
    float g = 1.f / (1.f + __expf(-gs));
    float a = 0.f, b = 0.f;
    for (int k = 0; k < D; ++k) {
        a += b2f(o[s * 1280 + k]) * b2f(woa[k * D + n]);
        b += b2f(o[s * 1280 + 640 + k]) * b2f(wob[k * D + n]);
    }
    out[s * D + n] = f2b(g * a + (1.f - g) * b);
}

extern "C" void kernel_launch(void* const* d_in, const int* in_sizes, int n_in,
                              void* d_out, int out_size, void* d_ws, size_t ws_size,
                              hipStream_t stream) {
    const u16* x = (const u16*)d_in[0];
    const u16* wa = (const u16*)d_in[1];
    const u16* woa = (const u16*)d_in[2];
    const u16* wb = (const u16*)d_in[4];
    const u16* wob = (const u16*)d_in[5];
    const u16* gw = (const u16*)d_in[6];
    u16* qkv = (u16*)d_ws;
    u16* o = qkv + S * 3840;
    float* kv = (float*)(o + S * 1280);
    k_qkv<<<dim3(15, S), 256, 0, stream>>>(x, wa, wb, qkv);
    k_attn<<<dim3(S / 64, H), 64, 0, stream>>>(qkv, o);
    k_kv<<<H, 448, 0, stream>>>(qkv, kv);
    k_lin<<<dim3(S / 64, H), 64, 0, stream>>>(qkv, kv, o);
    k_out<<<dim3(10, S), 64, 0, stream>>>(x, gw, o, woa, wob, (u16*)d_out);
}
```

```cpp
#include <hip/hip_runtime.h>
#include <stdint.h>
#pragma clang diagnostic ignored "-Wint-to-pointer-cast"

typedef unsigned short u16;
typedef unsigned int u32;
typedef unsigned long long u64;
typedef __attribute__((ext_vector_type(8))) __bf16 bf16x8;
typedef __attribute__((ext_vector_type(4))) __bf16 bf16x4;
typedef __attribute__((ext_vector_type(2))) __bf16 bf16x2;
typedef __attribute__((ext_vector_type(2))) float f32x2;
typedef __attribute__((ext_vector_type(4))) float f32x4;
typedef __attribute__((ext_vector_type(16))) float f32x16;
typedef __attribute__((ext_vector_type(4))) u32 u32x4;
typedef __attribute__((ext_vector_type(2))) u32 u32x2;
typedef __attribute__((ext_vector_type(4))) short s16x4;
#define AS3 __attribute__((address_space(3)))
#define AS1 __attribute__((address_space(1)))
typedef AS1 u64 gu64;
#define RLX_AGENT __ATOMIC_RELAXED, __HIP_MEMORY_SCOPE_AGENT
#define DEVI __device__ __forceinline__

constexpr u32 WS_E   = 0;
constexpr u32 WS_FK  = 4096;
constexpr u32 WS_FV  = WS_FK + 1024;
constexpr u32 WS_FKV = WS_FK + 2048;
constexpr u32 WS_FOB = WS_FK + 3072;
constexpr u32 WS_FOA = WS_FK + 5120;
constexpr u32 WS_TK  = 65536;
constexpr u32 WS_TV  = WS_TK + 8 * 64 * 5120;
constexpr u32 WS_KVP = WS_TV + 8 * 64 * 5120;
constexpr u32 KVP_H  = 1792;
constexpr u32 WS_O   = WS_KVP + 8 * 16 * 4 * KVP_H;
constexpr u32 WS_END = WS_O + 128 * 40960;

constexpr u32 L_WR   = 0;
constexpr u32 L_XR   = 81920;
constexpr u32 L_K    = 81920;
constexpr u32 L_PK   = 112640;
constexpr u32 L_VS   = 133120;
constexpr u32 L_V    = 112640;
constexpr u32 VBLK   = 2624;
constexpr u32 L_Q    = 144128 + 256;
constexpr u32 L_QR   = L_Q + 8192;
constexpr u32 L_MISC = 154624;
constexpr u32 L_G    = L_MISC;
constexpr u32 L_GW   = L_MISC + 512;
constexpr u32 L_ONE  = L_MISC + 3072;
constexpr u32 L_TAG  = L_MISC + 3088;
constexpr u32 L_KVI  = L_MISC + 3136;
constexpr u32 L_END  = L_KVI + 4096;
constexpr u32 LDS_BYTES = 163840;
static_assert(L_END <= LDS_BYTES, "lds");
static_assert(L_V + 12 * VBLK <= L_Q, "lds v");
static_assert(L_VS + 20480 <= L_MISC, "lds vs");

constexpr float LOG2E = 1.4426950408889634f;
constexpr float QSCALE = 0.22360679774997896f * 1.4426950408889634f;

DEVI u32 pk2(float a, float b) { f32x2 v = {a, b}; bf16x2 r = __builtin_convertvector(v, bf16x2); return __builtin_bit_cast(u32, r); }
DEVI u32 f2u(float f) { return __builtin_bit_cast(u32, f); }
DEVI float u2f(u32 u) { return __builtin_bit_cast(float, u); }
DEVI bf16x8 asbf8(u32x4 v) { return __builtin_bit_cast(bf16x8, v); }
DEVI f32x4 mfma16(bf16x8 a, bf16x8 b, f32x4 c) { return __builtin_amdgcn_mfma_f32_16x16x32_bf16(a, b, c, 0, 0, 0); }
template <bool FIRST> DEVI void mfma16a(f32x4& c, bf16x8 a, bf16x8 b) {
    if constexpr (FIRST) asm volatile("v_mfma_f32_16x16x32_bf16 %0, %1, %2, 0" : "=&v"(c) : "v"(a), "v"(b));
    else asm volatile("v_mfma_f32_16x16x32_bf16 %0, %1, %2, %0" : "+v"(c) : "v"(a), "v"(b));
}
DEVI void mfma_pad() { asm volatile("s_nop 7\n\ts_nop 3" ::: "memory"); }
DEVI f32x16 mfma32(bf16x8 a, bf16x8 b, f32x16 c) { return __builtin_amdgcn_mfma_f32_32x32x16_bf16(a, b, c, 0, 0, 0); }
DEVI f32x16 mfma32k8(bf16x4 a, bf16x4 b, f32x16 c) {
    return __builtin_amdgcn_mfma_f32_32x32x8bf16_1k(__builtin_bit_cast(s16x4, a), __builtin_bit_cast(s16x4, b), c, 0, 0, 0);
}
DEVI bf16x4 ldtr(u32 a) { return __builtin_amdgcn_ds_read_tr16_b64_v4bf16((AS3 bf16x4*)a); }
DEVI bf16x8 ldtr2(u32 a, u32 b) { bf16x4 x = ldtr(a), y = ldtr(b); return __builtin_shufflevector(x, y, 0, 1, 2, 3, 4, 5, 6, 7); }
DEVI u32x4 lds128(u32 a) { return *(AS3 u32x4*)a; }
DEVI u32x2 lds64(u32 a) { return *(AS3 u32x2*)a; }
DEVI float ldsf(u32 a) { return *(AS3 float*)a; }
DEVI void sts128(u32 a, u32x4 v) { *(AS3 u32x4*)a = v; }
DEVI void sts64(u32 a, u32x2 v) { *(AS3 u32x2*)a = v; }
DEVI void stsf(u32 a, float v) { *(AS3 float*)a = v; }

DEVI void dma16(const void* g, u32 lds) {
    u32 keep;
    asm volatile("s_mov_b32 %0, m0\n\ts_mov_b32 m0, %2\n\ts_nop 0\n\tglobal_load_lds_dwordx4 %1, off\n\ts_mov_b32 m0, %0"
                 : "=&s"(keep) : "v"(g), "s"(lds) : "memory");
}
DEVI void dma16c(const void* g, u32 lds) {
    u32 keep;
    asm volatile("s_mov_b32 %0, m0\n\ts_mov_b32 m0, %2\n\ts_nop 0\n\tglobal_load_lds_dwordx4 %1, off sc1\n\ts_mov_b32 m0, %0"
                 : "=&s"(keep) : "v"(g), "s"(lds) : "memory");
}
template <int N> DEVI void wait_vm() { asm volatile("s_waitcnt vmcnt(%0)" ::"i"(N) : "memory"); }
DEVI void wait_lgkm0() { asm volatile("s_waitcnt lgkmcnt(0)" ::: "memory"); }
DEVI void bar() { asm volatile("s_barrier" ::: "memory"); }
DEVI void sched0() { __builtin_amdgcn_sched_barrier(0); }

DEVI float phi(float t) { float e = __builtin_amdgcn_exp2f(t * LOG2E); return (t > 0.f ? t + 1.f : e) + 1e-6f; }

DEVI u64 mix64(u64 z) {
    z += 0x9E3779B97F4A7C15ull;
    z = (z ^ (z >> 30)) * 0xBF58476D1CE4E5B9ull;
    z = (z ^ (z >> 27)) * 0x94D049BB133111EBull;
    return z ^ (z >> 31);
}

DEVI void poll_flags(const char* p, int n, int lane, u64 tag) {
    gu64* q = (gu64*)(p + (lane < n ? lane : 0) * 8);
    for (u32 spins = 0; spins < (1u << 17); ++spins) {
        u64 v = __hip_atomic_load(q, RLX_AGENT);
        if (__builtin_amdgcn_ballot_w64(v != tag) == 0ull) break;
        __builtin_amdgcn_s_sleep(1);
    }
}

#define FST(v, i)
#define STAMP(i)

template <int N> struct IC { static constexpr int v = N; };
template <int I, int N, class F> DEVI void sfor(F&& f) { if constexpr (I < N) { f(IC<I>{}); sfor<I + 1, N>(f); } }

__global__ __launch_bounds__(512, 2) void fused_kernel(const u16* __restrict__ x, const u16* __restrict__ wa, const u16* __restrict__ woa,
                                                       const u16* __restrict__ wb, const u16* __restrict__ wob, const u16* __restrict__ gw,
                                                       u16* __restrict__ out, char* __restrict__ ws) {
    extern __shared__ __attribute__((aligned(1024))) char smem[];
    const u32 LB = (u32)(uintptr_t)(AS3 char*)smem;
    int lane = threadIdx.x & 63;
    const int wid = __builtin_amdgcn_readfirstlane(threadIdx.x >> 6);
    const int b = blockIdx.x, xcd = b & 7, idx = b >> 3;
    const int rb = (xcd & 1) * 8 + (idx >> 2), cg = (xcd >> 1) * 4 + (idx & 3);
    const int hg = cg & 7, ty = cg >> 3;
    const int rot = (rb + 3 * cg) % 10;
    int l15 = lane & 15, g4 = lane >> 4, l31 = lane & 31, hf = lane >> 5;
    STAMP(0);
    if (wid == 3) {
        u64 pc; u32 len;
        asm volatile("s_getpc_b64 %0\n.Lcode_lo:\n\ts_mov_b32 %1, .Lcode_hi-.Lcode_lo" : "=s"(pc), "=s"(len));
        u32 o = (idx & 15) * 4096 + lane * 64;
        o = o < len ? o : 0;
        u32 keep;
        asm volatile("s_mov_b32 %0, m0\n\ts_mov_b32 m0, %2\n\ts_nop 0\n\tglobal_load_lds_dword %1, off\n\ts_mov_b32 m0, %0"
                     : "=&s"(keep) : "v"(pc + o), "s"(LB + L_END) : "memory");
    }

    __amdgpu_buffer_rsrc_t wsr = __builtin_amdgcn_make_buffer_rsrc(ws, 0, WS_END, 0x00020000);

    u32 pq[2][6];

    if (wid < 4) {
        u64 tag0 = 0;
        if (wid == 0) {
            const u64 e = __hip_atomic_load((gu64*)(ws + WS_E), RLX_AGENT);
            tag0 = mix64(((u64)(u32)__builtin_amdgcn_readfirstlane((u32)(e >> 32)) << 32) | (u32)__builtin_amdgcn_readfirstlane((u32)e));
            if (lane == 0) sts64(LB + L_TAG, u32x2{(u32)tag0, (u32)(tag0 >> 32)});
        } else if (wid == 2) {
#pragma unroll
            for (int i = 0; i < 3; ++i) {
                int ch = i * 64 + lane;
                if (ch < 160) { int c = ch >= 80 ? ch - 80 : ch; sts128(LB + L_GW + ch * 16, *(const u32x4*)(gw + c * 8)); }
            }
        } else if (wid == 1) {
            u32x4 z = {0, 0, 0, 0};
#pragma unroll
            for (int i = 0; i < 4; ++i) sts128(LB + L_KVI + (i * 64 + lane) * 16, z);
            u32x4 o = {0x3F803F80u, 0x3F803F80u, 0x3F803F80u, 0x3F803F80u};
            if (lane == 0) sts128(LB + L_ONE, o);
        }
        wait_lgkm0();

        const u32 wbase0 = LB + L_WR + (16 * (g4 >> 1) + 4 * (g4 & 1) + (l15 >> 2)) * 160 + (lane & 3) * 8;
        const u32 wbase1 = wbase0 + 40960;
        const int xrow = 32 * wid + 16 * (l15 >> 3) + (l15 & 7);
        const u32 xbase0 = LB + L_XR + xrow * 128 + ((g4 ^ (l15 >> 1)) * 16);
        const u32 xbase1 = LB + L_XR + xrow * 128 + (((4 + g4) ^ (l15 >> 1)) * 16);
        const u32 gwbase = LB + L_GW + rot * 128 + g4 * 16;

        bf16x8 xf[10][2][2];
        bf16x8 gf[2];
        f32x4 acc[2][5];
        f32x4 accg[2];
        constexpr int PD = 4;
        bf16x8 wf[PD + 1];
        auto loadw = [&](auto NC) {
            constexpr int n = decltype(NC)::v;
            constexpr int s = n / 10, f = n % 10, ks2 = f / 5, ct = f % 5, slot = s % 8;
            if constexpr (f == 0) {
                if constexpr (s == 15 || s == 25) wait_vm<0>();
                if constexpr (s == 21 || s == 11) wait_lgkm0();
                sched0(); bar(); sched0();
                if constexpr (s == 15 || s == 25) {
                    if (ty == 0 && wid == 0 && lane == 0)
                        __hip_atomic_store((gu64*)(ws + (s == 15 ? WS_FK : WS_FV) + hg * 128 + rb * 8), tag0, RLX_AGENT);
                }
            }
            if constexpr (s < 10 && ct == 0) {
                constexpr u32 xo = (s % 4) * 16384;
                xf[s][ks2][0] = asbf8(lds128((ks2 ? xbase1 : xbase0) + xo));
                xf[s][ks2][1] = asbf8(lds128((ks2 ? xbase1 : xbase0) + xo + 1024));
                gf[ks2] = asbf8(lds128(gwbase + s * 128 + ks2 * 64));
            }
            constexpr u32 off = (slot % 4) * 10240 + ks2 * 5120 + ct * 32;
            const u32 base = slot < 4 ? wbase0 : wbase1;
            wf[n % (PD + 1)] = ldtr2(base + off, base + off + 1280);
        };
        sfor<0, PD>([&](auto NC) { loadw(NC); });
        STAMP(1);
        sfor<0, 300>([&](auto NC) {
            constexpr int n = decltype(NC)::v;
            constexpr int s = n / 10, f = n % 10, ks2 = f / 5, ct = f % 5, kt = s % 10, pass = s / 10;
            if constexpr (n + PD < 300) loadw(IC<n + PD>{});
            const bf16x8 w = wf[n % (PD + 1)];
            constexpr bool first = kt == 0 && ks2 == 0;
            if constexpr (pass < 2) {
                mfma16a<first>(acc[0][ct], xf[kt][ks2][0], w);
                mfma16a<first>(acc[1][ct], xf[kt][ks2][1], w);
            } else {
                mfma16a<first>(acc[0][ct], w, xf[kt][ks2][0]);
                mfma16a<first>(acc[1][ct], w, xf[kt][ks2][1]);
            }
            if constexpr (pass == 0 && ct == 0) {
                mfma16a<first>(accg[0], gf[ks2], xf[kt][ks2][0]);
                mfma16a<first>(accg[1], gf[ks2], xf[kt][ks2][1]);
            }
            if constexpr (n % 100 == 99) mfma_pad();
            sched0();
            if constexpr (n == 99 || n == 199) {
                if constexpr (n == 99) {
                    if (g4 == 0) {
#pragma unroll
                        for (int t = 0; t < 2; ++t) {
                            float gs = accg[t][0];
                            float gv = __builtin_amdgcn_rcpf(1.f + __builtin_amdgcn_exp2f(-gs * LOG2E));
                            stsf(LB + L_G + (xrow + 8 * t) * 4, gv);
                        }
                    }
                }
                if (ty == 0) {
                    const u32 to = (n == 99 ? WS_TK : WS_TV) + (hg * 64 + rb * 4 + wid) * 5120 + l15 * 64 + g4 * 16;
#pragma unroll
                    for (int c = 0; c < 5; ++c) {
                        u32x4 v = {pk2(acc[0][c][0], acc[0][c][1]), pk2(acc[0][c][2], acc[0][c][3]),
                                   pk2(acc[1][c][0], acc[1][c][1]), pk2(acc[1][c][2], acc[1][c][3])};
                        __builtin_amdgcn_raw_buffer_store_b128(v, wsr, to + c * 1024, 0, 16);
                        sched0();
                    }
                } else {
                    const u32 so = LB + (n == 99 ? L_PK : L_VS) + wid * 5120 + l15 * 64 + ((g4 ^ (l15 >> 2)) * 16);
#pragma unroll
                    for (int c = 0; c < 5; ++c) {
                        u32x4 v = {pk2(acc[0][c][0], acc[0][c][1]), pk2(acc[0][c][2], acc[0][c][3]),
                                   pk2(acc[1][c][0], acc[1][c][1]), pk2(acc[1][c][2], acc[1][c][3])};
                        sts128(so + c * 1024, v);
                        sched0();
                    }
                }
                asm volatile("s_nop 1");
                sched0();
                if constexpr (n == 99) { STAMP(2); } else { STAMP(3); }
            }
        });
#pragma unroll
        for (int t = 0; t < 2; ++t) {
            const int row = xrow + 8 * t;
#pragma unroll
            for (int a = 0; a < 3; ++a) {
                const int Q = 4 * a + g4;
                const int hh = Q >= 5 ? 1 : 0, dq = Q - 5 * hh;
                u32x2 v = {pk2(acc[t][a][0] * QSCALE, acc[t][a][1] * QSCALE), pk2(acc[t][a][2] * QSCALE, acc[t][a][3] * QSCALE)};
                const u32 ad = dq < 4 ? LB + L_Q + hh * 4096 + row * 32 + dq * 8 : LB + L_QR + hh * 1024 + row * 8;
                if (a < 2 || g4 < 2) sts64(ad, v);
            }
#pragma unroll
            for (int a = 2; a < 5; ++a) {
                pq[t][2 * (a - 2)] = pk2(acc[t][a][0], acc[t][a][1]);
                pq[t][2 * (a - 2) + 1] = pk2(acc[t][a][2], acc[t][a][3]);
            }
        }
        STAMP(4);
    } else {
        const int pw = wid - 4;
        u32 fa = 0, fb = 0, fc = 0; (void)fa; (void)fb; (void)fc;
        FST(fa, 62);
        auto kvpart = [&]() {
            const int ra = 20 * pw + (l31 < 20 ? l31 : 0);
            const u32 sw = (ra >> 2) & 3;
            f32x16 d;
#pragma unroll
            for (int i = 0; i < 16; ++i) d[i] = 0.f;
#pragma unroll
            for (int w = 0; w < 4; ++w)
#pragma unroll
                for (int ks = 0; ks < 2; ++ks) {
                    const u32 o = w * 5120 + ra * 64 + (((2 * ks + hf) ^ sw) * 16);
                    bf16x8 a = asbf8(lds128(LB + L_PK + o));
                    bf16x8 bb = asbf8(lds128(l31 < 20 ? LB + L_VS + o : LB + L_ONE));
                    d = mfma32(a, bb, d);
                }
            const u32 po = WS_KVP + ((hg * 16 + rb) * 4 + pw) * KVP_H + l31 * 16;
            if (l31 < 21) {
                f32x4 q0 = {d[0], d[1], d[2], d[3]}, q1 = {d[4], d[5], d[6], d[7]}, q2 = {d[8], d[9], d[10], d[11]};
                __builtin_amdgcn_raw_buffer_store_b128(__builtin_bit_cast(u32x4, q0), wsr, po + hf * 336, 0, 16);
                __builtin_amdgcn_raw_buffer_store_b128(__builtin_bit_cast(u32x4, q1), wsr, po + (2 + hf) * 336, 0, 16);
                if (hf == 0) __builtin_amdgcn_raw_buffer_store_b128(__builtin_bit_cast(u32x4, q2), wsr, po + 4 * 336, 0, 16);
            }
        };
        auto phik = [&](int c) {
            const u32 a = LB + L_PK + pw * 5120 + c * 1024 + lane * 16;
            u32x4 v = lds128(a);
#pragma unroll
            for (int i = 0; i < 4; ++i)
                v[i] = pk2(phi(__builtin_bit_cast(float, v[i] << 16)), phi(__builtin_bit_cast(float, v[i] & 0xFFFF0000u)));
            sts128(a, v);
            wait_lgkm0();
        };
        if (pw < 2) {
            const char* wm = (const char*)(ty ? wb : wa);
            u32 off[5];
            const char* p2[5];
#pragma unroll
            for (int i = 0; i < 5; ++i) {
                const int ch = (5 * pw + i) * 64 + lane;
                const int p = ch / 10, c = ch - p * 10;
                const int k = (p & 0x33) | ((p & 4) << 1) | ((p & 8) >> 1);
                off[i] = k * 3840 + hg * 160 + c * 16;
                p2[i] = (const char*)(c < 5 ? wa : wb) + k * 3840 + hg * 160 + ty * 80 + (c < 5 ? c : c - 5) * 16;
            }
            auto issue = [&](auto SC) {
                constexpr int s = decltype(SC)::v;
                constexpr int pass = s / 10, kt = s % 10;
                int kb = kt + rot; if (kb >= 10) kb -= 10;
                const u32 dst = LB + L_WR + (s % 8) * 10240 + pw * 5120;
                if constexpr (pass < 2) {
                    const char* base = wm + kb * (64 * 3840) + (pass + 1) * 1280;
#pragma unroll
                    for (int i = 0; i < 5; ++i) dma16(base + off[i], dst + i * 1024);
                } else {
#pragma unroll
                    for (int i = 0; i < 5; ++i) dma16(p2[i] + kb * (64 * 3840), dst + i * 1024);
                }
            };
            sfor<0, 6>([&](auto SC) { issue(SC); });
            sfor<0, 30>([&](auto SC) {
                constexpr int s = decltype(SC)::v;
                constexpr int last = s + 5 < 29 ? s + 5 : 29;
                FST(fa, s);
                wait_vm<(last - s) * 5>();
                FST(fb, s);
                bar();
                FST(fc, s);
                if constexpr (s + 6 < 30) issue(IC<s + 6>{});
                if constexpr (s >= 11 && s < 16) { if (ty) phik(s - 11); }
                if constexpr (s == 21) { if (ty) kvpart(); }
            });
        } else {
            const int xw = pw - 2;
            u32 off[8];
#pragma unroll
            for (int i = 0; i < 8; ++i) {
                const int pi = 8 * xw + i;
                const int row = 8 * pi + (lane >> 3);
                const int c = (lane & 7) ^ ((lane >> 4) | (((pi >> 1) & 1) << 2));
                off[i] = (rb * 128 + row) * 1280 + c * 16;
            }
            auto issue = [&](auto SC) {
                constexpr int s = decltype(SC)::v;
                int kb = s + rot; if (kb >= 10) kb -= 10;
                const char* base = (const char*)x + kb * 128;
                const u32 dst = LB + L_XR + (s % 4) * 16384 + xw * 8192;
#pragma unroll
                for (int i = 0; i < 8; ++i) dma16(base + off[i], dst + i * 1024);
            };
            sfor<0, 4>([&](auto SC) { issue(SC); });
            sfor<0, 30>([&](auto SC) {
                constexpr int s = decltype(SC)::v;
                FST(fa, s);
                if constexpr (s < 10) {
                    constexpr int last = s == 0 ? 3 : (s + 2 < 9 ? s + 2 : 9);
                    wait_vm<(last - s) * 8>();
                }
                FST(fb, s);
                bar();
                FST(fc, s);
                if constexpr (s >= 1 && s + 3 < 10) issue(IC<s + 3>{});
                if constexpr (s >= 11 && s < 16) { if (ty) phik(s - 11); }
                if constexpr (s == 21) { if (ty) kvpart(); }
            });
        }
        wait_vm<0>();
        STAMP(4);
    }
    wait_lgkm0();
    bar();
    asm volatile("v_mbcnt_lo_u32_b32 %0, -1, 0\n\tv_mbcnt_hi_u32_b32 %0, -1, %0" : "=v"(lane));
    l15 = lane & 15; g4 = lane >> 4; l31 = lane & 31; hf = lane >> 5;
    u64 tag;
    { u32x2 t2 = lds64(LB + L_TAG); tag = ((u64)(u32)__builtin_amdgcn_readfirstlane(t2[1]) << 32) | (u32)__builtin_amdgcn_readfirstlane(t2[0]); }
    if (ty && wid == 4 && lane == 0) __hip_atomic_store((gu64*)(ws + WS_FKV + hg * 128 + rb * 8), tag, RLX_AGENT);
    const int rlo = rb >= 2 ? rb - 2 : 0;
    if (wid == 7) poll_flags(ws + WS_FK + hg * 128 + rlo * 8, rb - rlo + 1, lane, tag);
    if (wid == 6) poll_flags(ws + WS_FV + hg * 128 + rlo * 8, rb - rlo + 1, lane, tag);
    if (wid == 5) poll_flags(ws + WS_FKV + hg * 128, 16, lane, tag);
    if (wid == 4 && lane < 48) {
        u32x4 o = {0x3F803F80u, 0x3F803F80u, 0x3F803F80u, 0x3F803F80u};
        sts128(LB + L_V + (lane >> 2) * VBLK + 2560 + (lane & 3) * 16, o);
    }
    bar();
    STAMP(5);
    {
#pragma unroll
        for (int i = 0; i < 4; ++i) {
            const int pc = wid + 8 * i;
            if (pc < 30) {
                const int ch = pc * 64 + lane;
                const int bl = ch / 160, wi = ch - bl * 160;
                int gb = 4 * rb - 8 + bl; gb = gb < 0 ? 0 : gb;
                dma16c(ws + WS_TK + (hg * 64 + gb) * 5120 + ty * 2560 + wi * 16, LB + L_K + pc * 1024);
            }
        }
#pragma unroll
        for (int i = 0; i < 4; ++i) {
            const int pc = wid + 8 * i;
            if (pc < 31) {
                const int ch = pc * 64 + lane;
                const int bl = ch / 164, wi = ch - bl * 164;
                int gb = 4 * rb - 8 + bl; gb = gb < 0 ? 0 : gb;
                const int row = wi >> 2, c = (wi & 3) ^ ((row >> 2) & 3);
                if (wi < 160 && bl < 12)
                    dma16c(ws + WS_TV + (hg * 64 + gb) * 5120 + ty * 2560 + row * 64 + c * 16, LB + L_V + pc * 1024);
            }
        }
    }
    {
        const int qd = wid * 32 + l31;
        if (qd < 210) {
            const int hb = qd >= 105 ? 1 : 0, qi = qd - 105 * hb;
            const u32 po = WS_KVP + ((hg * 16 + hf * 8) * 4 + 2 * ty + hb) * KVP_H + qi * 16;
            f32x4 s = {0, 0, 0, 0};
            u32x4 v[8];
#pragma unroll
            for (int i = 0; i < 8; ++i) v[i] = __builtin_amdgcn_raw_buffer_load_b128(wsr, po + i * (4 * KVP_H), 0, 16);
#pragma unroll
            for (int i = 0; i < 8; ++i) s += __builtin_bit_cast(f32x4, v[i]);
#pragma unroll
            for (int r = 0; r < 4; ++r) {
                u32 a = f2u(s[r]);
                auto sw = __builtin_amdgcn_permlane32_swap(a, a, false, false);
                s[r] = __builtin_bit_cast(float, (u32)sw[0]) + __builtin_bit_cast(float, (u32)sw[1]);
            }
            const int dq = qi / 21, e = qi - dq * 21;
            const int Q = 10 + 5 * hb + dq, gq = Q & 3, u = (Q >> 2) - (2 + hb);
            u32x2 pv = {pk2(s[0], s[1]), pk2(s[2], s[3])};
            const int fr = e < 16 ? hb : 2 + hb;
            const int ix = e < 16 ? e : (e < 20 ? 4 * hb + e - 16 : 8 + hb + 4 * hf);
            if (hf == 0 || e == 20) sts64(LB + L_KVI + fr * 1024 + (gq * 16 + ix) * 16 + u * 8, pv);
        }
    }
    wait_vm<0>();
    wait_lgkm0();
    bar();
    STAMP(6);
    const int H0 = 4 * hg + 2 * ty;
    const int unit = H0 >> 3, h8 = H0 & 7;
    if (wid < 4) {
        bf16x8 m0 = asbf8(lds128(LB + L_KVI + lane * 16)), m1 = asbf8(lds128(LB + L_KVI + 1024 + lane * 16));
        bf16x8 x0 = asbf8(lds128(LB + L_KVI + 2048 + lane * 16)), x1 = asbf8(lds128(LB + L_KVI + 3072 + lane * 16));
#pragma unroll
        for (int t = 0; t < 2; ++t) {
            const int row = 32 * wid + 16 * (l15 >> 3) + (l15 & 7) + 8 * t;
            const float gv = 1.f - ldsf(LB + L_G + row * 4);
#pragma unroll
            for (int i = 0; i < 6; ++i)
                pq[t][i] = pk2(phi(__builtin_bit_cast(float, pq[t][i] << 16)), phi(__builtin_bit_cast(float, pq[t][i] & 0xFFFF0000u)));
            bf16x8 p23 = asbf8(u32x4{pq[t][0], pq[t][1], pq[t][2], pq[t][3]});
            bf16x8 p34 = asbf8(u32x4{pq[t][2], pq[t][3], pq[t][4], pq[t][5]});
            const f32x4 z = {0, 0, 0, 0};
            f32x4 n0 = mfma16(m0, p23, z), n1 = mfma16(m1, p34, z);
            f32x4 xx = mfma16(x0, p23, z);
            xx = mfma16(x1, p34, xx);
            u32 a0 = f2u(xx[0]), a1 = f2u(xx[1]);
            auto s0 = __builtin_amdgcn_permlane32_swap(a0, a0, false, false);
            auto s1 = __builtin_amdgcn_permlane32_swap(a1, a1, false, false);
            const float r0 = gv * __builtin_amdgcn_rcpf(__builtin_bit_cast(float, (u32)s0[1]) + 1e-6f);
            const float r1 = gv * __builtin_amdgcn_rcpf(__builtin_bit_cast(float, (u32)s1[1]) + 1e-6f);
            const u32 ob = WS_O + (u32)(rb * 8 + (row >> 4)) * 40960 + (row & 15) * 16;
            u32x4 v = {pk2(n0[0] * r0, n0[1] * r0), pk2(n0[2] * r0, n0[3] * r0), pk2(n1[0] * r1, n1[1] * r1), pk2(n1[2] * r1, n1[3] * r1)};
            __builtin_amdgcn_raw_buffer_store_b128(v, wsr, ob + (80 + 20 * unit + 4 * (h8 >> 1) + g4) * 256, 0, 16);
            const float rr = g4 == 0 ? r0 : r1;
            u32x2 v2 = {pk2(xx[0] * rr, xx[1] * rr), pk2(xx[2] * rr, xx[3] * rr)};
            if (g4 < 2) __builtin_amdgcn_raw_buffer_store_b64(v2, wsr, ob + (80 + 20 * unit + 16 + g4 + 2 * (h8 >> 2)) * 256 + ((h8 >> 1) & 1) * 8, 0, 16);
        }
    }
    STAMP(7);
    {
        const int hh = wid >> 2, qt = wid & 3;
        const int qrow = 32 * qt + l31;
        bf16x8 qf = asbf8(lds128(LB + L_Q + hh * 4096 + qrow * 32 + hf * 16));
        u32x2 qr_ = lds64(LB + L_QR + hh * 1024 + qrow * 8);
        if (hf) qr_ = u32x2{0, 0};
        bf16x4 qr = __builtin_bit_cast(bf16x4, qr_);
        const int kh = g4 & 1, r4 = l15 >> 2, c4 = lane & 3;
        const u32 kcol = kh * 32 + (((c4 & 1) << 1) | (c4 >> 1)) * 8;
        const u32 ka = LB + L_K + qt * 2560 + hh * 1280 + (8 * hf + r4) * 64 + kcol;
        const u32 kr = LB + L_K + qt * 2560 + hh * 1280 + (16 + r4) * 64 + kcol;
        f32x16 S[9];
        f32x16 zero;
#pragma unroll
        for (int i = 0; i < 16; ++i) zero[i] = 0.f;
#pragma unroll
        for (int i = 0; i < 9; ++i) {
            bf16x8 kf = ldtr2(ka + i * 2560, ka + i * 2560 + 256);
            bf16x4 k2 = ldtr(kr + i * 2560);
            S[i] = mfma32(kf, qf, zero);
            S[i] = mfma32k8(k2, qr, S[i]);
        }
        const int nskip = 8 - 4 * rb - qt;
        if (nskip > 0) {
#pragma unroll
            for (int i = 0; i < 8; ++i)
                if (i < nskip) {
#pragma unroll
                    for (int r = 0; r < 16; ++r) S[i][r] = -1e30f;
                }
        }
        const int qq = l31 - 4 * hf;
#pragma unroll
        for (int r = 0; r < 16; ++r) S[0][r] = ((r & 3) + 8 * (r >> 2) <= qq) ? S[8][r] : S[0][r];
        float m = S[0][0];
#pragma unroll
        for (int i = 0; i < 8; ++i)
#pragma unroll
            for (int r = 0; r < 16; r += 2) m = fmaxf(fmaxf(m, S[i][r]), S[i][r + 1]);
        {
            u32 a = __builtin_bit_cast(u32, m);
            auto sw = __builtin_amdgcn_permlane32_swap(a, a, false, false);
            m = fmaxf(__builtin_bit_cast(float, (u32)sw[0]), __builtin_bit_cast(float, (u32)sw[1]));
        }
        const int vrow = 20 * hh + l31;
        const u32 vsw = (vrow >> 2) & 3;
        const u32 va0 = LB + L_V + qt * VBLK + (l31 < 20 ? vrow * 64 + ((hf ^ vsw) * 16) : 2560);
        const u32 va1 = LB + L_V + qt * VBLK + (l31 < 20 ? vrow * 64 + (((2 + hf) ^ vsw) * 16) : 2560);
        f32x16 o = zero;
#pragma unroll
        for (int i = 0; i < 8; ++i) {
            u32 p[8];
#pragma unroll
            for (int j = 0; j < 8; ++j)
                p[j] = pk2(__builtin_amdgcn_exp2f(S[i][2 * j] - m), __builtin_amdgcn_exp2f(S[i][2 * j + 1] - m));
            if (i == 0) {
                u32 pd[8];
#pragma unroll
                for (int j = 0; j < 8; ++j) {
                    const int k0 = ((2 * j) & 3) + 8 * ((2 * j) >> 2);
                    const u32 mk = (k0 <= qq ? 0xFFFFu : 0u) | (k0 + 1 <= qq ? 0xFFFF0000u : 0u);
                    pd[j] = p[j] & mk;
                    p[j] = p[j] & ~mk;
                }
                o = mfma32(asbf8(lds128(va0 + 8 * VBLK)), asbf8(u32x4{pd[0], pd[1], pd[2], pd[3]}), o);
                o = mfma32(asbf8(lds128(va1 + 8 * VBLK)), asbf8(u32x4{pd[4], pd[5], pd[6], pd[7]}), o);
            }
            o = mfma32(asbf8(lds128(va0 + i * VBLK)), asbf8(u32x4{p[0], p[1], p[2], p[3]}), o);
            o = mfma32(asbf8(lds128(va1 + i * VBLK)), asbf8(u32x4{p[4], p[5], p[6], p[7]}), o);
        }
        const float sc = ldsf(LB + L_G + qrow * 4) * __builtin_amdgcn_rcpf(o[12]);
        const int hx = h8 + hh;
        const u32 ob = WS_O + (u32)(rb * 8 + (qrow >> 4)) * 40960 + (qrow & 15) * 16;
        u32x4 v = {pk2(o[0] * sc, o[1] * sc), pk2(o[2] * sc, o[3] * sc), pk2(o[4] * sc, o[5] * sc), pk2(o[6] * sc, o[7] * sc)};
        __builtin_amdgcn_raw_buffer_store_b128(v, wsr, ob + (20 * unit + 4 * (hx >> 1) + 2 * (hx & 1) + hf) * 256, 0, 16);
        u32x2 v2 = {pk2(o[8] * sc, o[9] * sc), pk2(o[10] * sc, o[11] * sc)};
        if (hf == 0) __builtin_amdgcn_raw_buffer_store_b64(v2, wsr, ob + (20 * unit + 16 + (hx & 1) + 2 * (hx >> 2)) * 256 + ((hx >> 1) & 1) * 8, 0, 16);
    }
    STAMP(8);
    wait_vm<0>();
    bar();
    if (wid == 0 && lane == 0) __hip_atomic_store((gu64*)(ws + WS_FOA + rb * 128 + cg * 8), tag, RLX_AGENT);
    STAMP(9);
    {
        const int r64 = xcd * 4 + (idx >> 3), c80 = idx & 7;
        const int kh = wid >> 2, rt = wid & 3;
        auto unit_dma = [&](const u16* wo, int un, int slot) {
#pragma unroll
            for (int i = 0; i < 4; ++i) {
                const int pc = wid + 8 * i;
                if (pc < 25) {
                    const int ch = pc * 64 + lane;
                    const int row = ch / 10, c = ch - row * 10;
                    dma16((const char*)wo + (un * 160 + row) * 1280 + c80 * 160 + c * 16, LB + slot * 25600 + pc * 1024);
                }
            }
        };
        unit_dma(wob, 0, 0); unit_dma(wob, 2, 1); unit_dma(wob, 1, 2); unit_dma(wob, 3, 3); unit_dma(woa, 0, 4); unit_dma(woa, 2, 5);
        if (b == 255 && wid == 6) {
            for (u32 spins = 0; spins < (1u << 17); ++spins) {
                u64 v0 = __hip_atomic_load((gu64*)(ws + WS_FK + lane * 8), RLX_AGENT);
                u64 v1 = __hip_atomic_load((gu64*)(ws + WS_FK + 512 + lane * 8), RLX_AGENT);
                u64 v2 = __hip_atomic_load((gu64*)(ws + WS_FKV + lane * 8), RLX_AGENT);
                u64 v3 = __hip_atomic_load((gu64*)(ws + WS_FKV + 512 + lane * 8), RLX_AGENT);
                if (__builtin_amdgcn_ballot_w64(v0 != tag || v1 != tag || v2 != tag || v3 != tag) == 0ull) break;
                __builtin_amdgcn_s_sleep(1);
            }
            if (lane == 0) __hip_atomic_store((gu64*)(ws + WS_E), tag, RLX_AGENT);
        }
        if (wid == 7) poll_flags(ws + WS_FOA + (r64 >> 1) * 128, 16, lane, tag);
        wait_vm<0>();
        bar();
        STAMP(10);
        const u32 oa = WS_O + (u32)(r64 * 4 + rt) * 40960 + lane * 16 + kh * 10240;
        u32x4 of[20];
#pragma unroll
        for (int i = 0; i < 10; ++i) of[i] = __builtin_amdgcn_raw_buffer_load_b128(wsr, oa + 20480 + i * 1024, 0, 16);
#pragma unroll
        for (int i = 0; i < 10; ++i) of[10 + i] = __builtin_amdgcn_raw_buffer_load_b128(wsr, oa + i * 1024, 0, 16);
        const int r4 = l15 >> 2, c4 = lane & 3;
        const u32 aA = LB + kh * 25600 + (20 * (g4 >> 1) + 4 * (g4 & 1) + r4) * 160 + c4 * 8;
        const u32 aB = LB + kh * 25600 + (4 * g4 + r4) * 160 + c4 * 8;
        const u32 aR = LB + kh * 25600 + (20 * (g4 & 1) + 80 * (g4 >> 1) + 16 + r4) * 160 + c4 * 8;
        f32x4 acc[5];
#pragma unroll
        for (int c = 0; c < 5; ++c) acc[c] = f32x4{0, 0, 0, 0};
        auto do_unit = [&](auto BR, u32 so, int fo) {
            constexpr int br = decltype(BR)::v;
#pragma unroll
            for (int ks = 0; ks < 5; ++ks) {
                const bf16x8 ofr = asbf8(of[fo + ks]);
#pragma unroll
                for (int c = 0; c < 5; ++c) {
                    bf16x8 w;
                    if (ks < 4) {
                        if (br == 0) w = ldtr2(aA + so + ks * 6400 + c * 32, aA + so + ks * 6400 + c * 32 + 8 * 160);
                        else w = ldtr2(aB + so + ks * 6400 + c * 32, aB + so + ks * 6400 + c * 32 + 20 * 160);
                    } else w = ldtr2(aR + so + c * 32, aR + so + c * 32 + 40 * 160);
                    acc[c] = mfma16(w, ofr, acc[c]);
                }
            }
        };
        do_unit(IC<1>{}, 0, 0);
        wait_lgkm0(); sched0();
        bar();
        unit_dma(woa, 1, 0); unit_dma(woa, 3, 1);
        do_unit(IC<1>{}, 51200, 5);
        do_unit(IC<0>{}, 102400, 10);
        wait_vm<0>();
        bar();
        do_unit(IC<0>{}, 0, 15);
        STAMP(11);
        const u32 ra = LB + 51200 + rt * 5120 + lane * 16;
        wait_lgkm0(); sched0();
        if (kh == 1) {
#pragma unroll
            for (int c = 0; c < 5; ++c) sts128(ra + c * 1024, __builtin_bit_cast(u32x4, acc[c]));
        }
        wait_lgkm0();
        bar();
        if (kh == 0) {
            u16* op = out + (r64 * 64 + rt * 16 + l15) * 640 + c80 * 80 + g4 * 4;
#pragma unroll
            for (int c = 0; c < 5; ++c) {
                f32x4 o2 = __builtin_bit_cast(f32x4, lds128(ra + c * 1024));
                f32x4 s = acc[c] + o2;
                u32x2 v = {pk2(s[0], s[1]), pk2(s[2], s[3])};
                *(u32x2*)(op + c * 16) = v;
            }
        }
        STAMP(12);
    }
    asm volatile(".Lcode_hi:");
}

extern "C" void kernel_launch(void* const* d_in, const int* in_sizes, int n_in, void* d_out, int out_size, void* d_ws,
                              size_t ws_size, hipStream_t stream) {
    (void)in_sizes; (void)n_in; (void)out_size; (void)ws_size;
    (void)hipFuncSetAttribute((const void*)fused_kernel, hipFuncAttributeMaxDynamicSharedMemorySize, LDS_BYTES);
    fused_kernel<<<256, 512, LDS_BYTES, stream>>>((const u16*)d_in[0], (const u16*)d_in[1], (const u16*)d_in[2], (const u16*)d_in[4],
                                                  (const u16*)d_in[5], (const u16*)d_in[6], (u16*)d_out, (char*)d_ws);
}
```

```cpp
#include <hip/hip_runtime.h>
#include <stdint.h>
#pragma clang diagnostic ignored "-Wint-to-pointer-cast"

typedef unsigned short u16;
typedef unsigned int u32;
typedef unsigned long long u64;
typedef __attribute__((ext_vector_type(8))) __bf16 bf16x8;
typedef __attribute__((ext_vector_type(4))) __bf16 bf16x4;
typedef __attribute__((ext_vector_type(2))) __bf16 bf16x2;
typedef __attribute__((ext_vector_type(2))) float f32x2;
typedef __attribute__((ext_vector_type(4))) float f32x4;
typedef __attribute__((ext_vector_type(16))) float f32x16;
typedef __attribute__((ext_vector_type(4))) u32 u32x4;
typedef __attribute__((ext_vector_type(2))) u32 u32x2;
typedef __attribute__((ext_vector_type(4))) short s16x4;
#define AS3 __attribute__((address_space(3)))
#define DEVI __device__ __forceinline__

constexpr u32 WS_E   = 0;
constexpr u32 WS_FQ  = 4096;
constexpr u32 WS_FT  = 8192;
constexpr u32 WS_FKV = 12288;
constexpr u32 WS_FOA = 16384;
constexpr u32 WS_T   = 65536;
constexpr u32 WS_KVP = WS_T + 8 * 64 * 10240;
constexpr u32 KVP_H  = 1792;
constexpr u32 WS_O   = WS_KVP + 8 * 16 * 4 * KVP_H;
constexpr u32 WS_WQ  = WS_O + 128 * 40960;
constexpr u32 WS_END = WS_WQ + 16 * 102400;

constexpr u32 CELL   = 10240;
constexpr u32 L_WR   = 0;
constexpr u32 L_XR   = 81920;
constexpr u32 L_PK   = 0;
constexpr u32 L_VS   = 13 * CELL;
constexpr u32 L_RAW  = 13 * CELL;
constexpr u32 L_K    = 4 * CELL;
constexpr u32 L_V    = 12 * CELL;
constexpr u32 VBLK   = 2624;
constexpr u32 L_Q    = 3 * CELL;
constexpr u32 L_QR   = L_Q + 8192;
constexpr u32 L_MISC = 155136;
constexpr u32 L_G    = L_MISC;
constexpr u32 L_GW   = L_MISC + 512;
constexpr u32 L_KVI  = L_GW + 3072;
constexpr u32 L_ONE  = L_KVI + 4096;
constexpr u32 L_DUMP = L_ONE + 64;
constexpr u32 L_END  = L_DUMP + 256;
constexpr u32 LDS_BYTES = 163840;
static_assert(L_END <= LDS_BYTES, "lds");
static_assert(L_V + 12 * VBLK <= L_MISC, "lds v");
__device__ constexpr int QCELL[10] = {4, 5, 6, 12, 10, 7, 11, 8, 9, 2};

constexpr float LOG2E = 1.4426950408889634f;
constexpr float QSCALE = 0.22360679774997896f * 1.4426950408889634f;
constexpr u32 TAGC = 0x7F4A7C15u;

DEVI u32 pk2(float a, float b) { f32x2 v = {a, b}; bf16x2 r = __builtin_convertvector(v, bf16x2); return __builtin_bit_cast(u32, r); }
DEVI u32 f2u(float f) { return __builtin_bit_cast(u32, f); }
DEVI float u2f(u32 u) { return __builtin_bit_cast(float, u); }
DEVI bf16x8 asbf8(u32x4 v) { return __builtin_bit_cast(bf16x8, v); }
DEVI f32x4 mfma16(bf16x8 a, bf16x8 b, f32x4 c) { return __builtin_amdgcn_mfma_f32_16x16x32_bf16(a, b, c, 0, 0, 0); }
template <bool FIRST> DEVI void mfma16a(f32x4& c, bf16x8 a, bf16x8 b) {
    if constexpr (FIRST) asm volatile("v_mfma_f32_16x16x32_bf16 %0, %1, %2, 0" : "=&v"(c) : "v"(a), "v"(b));
    else asm volatile("v_mfma_f32_16x16x32_bf16 %0, %1, %2, %0" : "+v"(c) : "v"(a), "v"(b));
}
DEVI void mfma_pad() { asm volatile("s_nop 7\n\ts_nop 3" ::: "memory"); }
DEVI f32x16 mfma32(bf16x8 a, bf16x8 b, f32x16 c) { return __builtin_amdgcn_mfma_f32_32x32x16_bf16(a, b, c, 0, 0, 0); }
DEVI f32x16 mfma32k8(bf16x4 a, bf16x4 b, f32x16 c) {
    return __builtin_amdgcn_mfma_f32_32x32x8bf16_1k(__builtin_bit_cast(s16x4, a), __builtin_bit_cast(s16x4, b), c, 0, 0, 0);
}
DEVI bf16x4 ldtr(u32 a) { return __builtin_amdgcn_ds_read_tr16_b64_v4bf16((AS3 bf16x4*)a); }
DEVI bf16x8 ldtr2(u32 a, u32 b) { bf16x4 x = ldtr(a), y = ldtr(b); return __builtin_shufflevector(x, y, 0, 1, 2, 3, 4, 5, 6, 7); }
DEVI u32x4 lds128(u32 a) { return *(AS3 u32x4*)a; }
DEVI u32x2 lds64(u32 a) { return *(AS3 u32x2*)a; }
DEVI float ldsf(u32 a) { return *(AS3 float*)a; }
DEVI void sts128(u32 a, u32x4 v) { *(AS3 u32x4*)a = v; }
DEVI void sts64(u32 a, u32x2 v) { *(AS3 u32x2*)a = v; }
DEVI void stsf(u32 a, float v) { *(AS3 float*)a = v; }
DEVI u32 rfl(u32 v) { return __builtin_amdgcn_readfirstlane(v); }
DEVI u32 opaque(u32 v) { asm volatile("" : "+v"(v)); return v; }
DEVI u32 opaque_s(u32 v) { asm volatile("" : "+s"(v)); return v; }

DEVI u32x4 mkrs(const void* p, u32 bytes) { return u32x4{(u32)(uintptr_t)p, (u32)((uintptr_t)p >> 32) & 0xFFFFu, bytes, 0x00020000u}; }
template <int MO, int OFF = 0> DEVI void dma(u32 voff, u32x4 rs, u32 soff, u32 mb) {
    asm volatile("s_add_u32 m0, %3, %5\n\ts_nop 0\n\tbuffer_load_dwordx4 %0, %1, %2 offen offset:%4 lds"
                 :: "v"(voff), "s"(rs), "s"(soff), "s"(mb), "i"(OFF), "i"(MO) : "memory", "scc");
}
template <int MO, int OFF = 0> DEVI void dmac(u32 voff, u32x4 rs, u32 soff, u32 mb) {
    asm volatile("s_add_u32 m0, %3, %5\n\ts_nop 0\n\tbuffer_load_dwordx4 %0, %1, %2 offen offset:%4 sc1 lds"
                 :: "v"(voff), "s"(rs), "s"(soff), "s"(mb), "i"(OFF), "i"(MO) : "memory", "scc");
}
DEVI u32 ldflag(u32 voff, u32x4 rs) {
    u32 v;
    asm volatile("buffer_load_dword %0, %1, %2, 0 offen sc1" : "=v"(v) : "v"(voff), "s"(rs) : "memory");
    return v;
}
DEVI void stflag(u32 val, u32 soff, u32x4 rs) {
    asm volatile("buffer_store_dword %0, off, %1, %2 sc1" :: "v"(val), "s"(rs), "s"(soff) : "memory");
}
template <int N> DEVI void wait_vm() { asm volatile("s_waitcnt vmcnt(%0)" ::"i"(N) : "memory"); }
template <int N> DEVI u32 wait_rfl(u32 v) { u32 r; asm volatile("s_waitcnt vmcnt(%2)\n\tv_readfirstlane_b32 %0, %1" : "=s"(r) : "v"(v), "i"(N) : "memory"); return r; }
template <int N> DEVI bool wait_eq(u32 v, u32 tag) {
    u64 m; asm volatile("s_waitcnt vmcnt(%3)\n\tv_cmp_ne_u32_e64 %0, %2, %1" : "=s"(m) : "v"(v), "s"(tag), "i"(N) : "memory");
    return m == 0ull;
}
template <int N> DEVI void wait_lgkm() { asm volatile("s_waitcnt lgkmcnt(%0)" ::"i"(N) : "memory"); }
DEVI void bar() { asm volatile("s_barrier" ::: "memory"); }
DEVI void sched0() { __builtin_amdgcn_sched_barrier(0); }
DEVI void spin(u32 voff, u32x4 rs, u32 tag) {
#pragma nounroll
    for (u32 i = 0; i < (1u << 17); ++i) {
        u32 v = ldflag(voff, rs);
        if (wait_eq<0>(v, tag)) break;
        __builtin_amdgcn_s_sleep(1);
    }
}

DEVI float phi(float t) {
    float e = __builtin_amdgcn_fmed3f(__builtin_amdgcn_exp2f(t * LOG2E), 0.f, 1.f);
    return e + __builtin_fmaxf(t + 1e-6f, 1e-6f);
}

#define STAMP(i)

template <int N> struct IC { static constexpr int v = N; };
template <int I, int N, class F> DEVI void sfor(F&& f) { if constexpr (I < N) { f(IC<I>{}); sfor<I + 1, N>(f); } }

__global__ __launch_bounds__(512, 2) void fused_kernel(const u16* __restrict__ x, const u16* __restrict__ wa, const u16* __restrict__ woa,
                                                       const u16* __restrict__ wb, const u16* __restrict__ wob, const u16* __restrict__ gw,
                                                       u16* __restrict__ out, char* __restrict__ ws) {
    u32 lane = threadIdx.x & 63;
    const u32 wid = rfl(threadIdx.x >> 6);
    const u32 b = blockIdx.x, xcd = b & 7, idx = b >> 3;
    STAMP(0);
    if (wid == 3) {
        u64 pc; u32 len;
        asm volatile("s_getpc_b64 %0\n.Lcode_lo:\n\ts_mov_b32 %1, .Lcode_hi-.Lcode_lo" : "=s"(pc), "=s"(len));
        u32 o = (idx & 15) * 4096 + lane * 64;
        o = o < len ? o : 0;
        asm volatile("s_mov_b32 m0, %2\n\ts_nop 0\n\tglobal_load_lds_dword %0, %1" :: "v"(o), "s"(pc), "i"(L_DUMP) : "memory");
    }
    const u32 rb = (xcd & 1) * 8 + (idx >> 2), cg = (xcd >> 1) * 4 + (idx & 3);
    const u32 hg = cg & 7, ty = cg >> 3;
    const u32 rot = (rb + 3 * cg) % 10;
    const u32 wr = wid & 3;
    const u32x4 rsx = mkrs(x, 2048 * 1280), rsw = mkrs(ty ? wb : wa, 640 * 3840), rws = mkrs(ws, WS_END);
    auto kbof = [&](int s) { u32 k = rot + s; return k >= 10 ? k - 10 : k; };

    u32 vo[5];
    u32 ev;
    if (wid >= 4) {
#pragma unroll
        for (int i = 0; i < 5; ++i) {
            const u32 ch = 64 * i + lane, pr = (ch * 205) >> 12, j = ch - pr * 20;
            const u32 k = (pr & 3) | ((pr & 4) << 1) | ((pr & 8) >> 1);
            const u32 js = j ^ ((pr & 4) >> 1);
            vo[i] = (16 * wr + k) * 3840 + (ty ? 1280 + 160 * hg + (js >= 10 ? 1120 : 0) : 1280 + 320 * hg) + js * 16;
        }
        sfor<0, 5>([&](auto I) { dma<L_WR + I.v * 1024>(vo[I.v], rsw, kbof(0) * 245760, wr * 5120); });
        ev = ldflag(0, rws);
        sfor<0, 5>([&](auto I) { dma<L_WR + 20480 + I.v * 1024>(vo[I.v], rsw, kbof(1) * 245760, wr * 5120); });
    } else {
        vo[0] = (rb * 128 + 32 * wr + (lane >> 3)) * 1280 + (((lane & 7) ^ ((lane >> 3) & 6)) * 16);
        sfor<0, 4>([&](auto I) { dma<L_XR + I.v * 1024>(vo[0], rsx, kbof(0) * 128 + I.v * 10240, wr * 4096); });
        ev = ldflag(0, rws);
        if (wr < 3) {
            u32 ch = 64 * wr + lane; ch = ch >= 160 ? ch - 160 : (ch >= 80 ? ch - 80 : ch);
            dma<L_GW>(ch * 16, mkrs(gw, 1280), 0, wr * 1024);
        }
#pragma unroll
        for (int i = 0; i < 2; ++i) {
            const u32 ch = 64 * (wr + 4 * i) + lane;
            u32 row = (ch * 205) >> 11; const u32 c = ch - row * 10; row = row < 39 ? row : 39;
            dma<L_RAW>(row * 3840 + c * 16, rsw, rb * (40 * 3840) + hg * 160, (wr + 4 * i) * 1024);
        }
        sfor<0, 4>([&](auto I) { dma<L_XR + 16384 + I.v * 1024>(vo[0], rsx, kbof(1) * 128 + I.v * 10240, wr * 4096); });
        vo[1] = vo[2] = vo[3] = vo[4] = 0;
    }
    u32 l15 = lane & 15, g4 = lane >> 4, l31 = lane & 31, hf = lane >> 5;
    __amdgpu_buffer_rsrc_t wsr = __builtin_amdgcn_make_buffer_rsrc(ws, 0, WS_END, 0x00020000);
    if (wid >= 4) {
        sts128(L_KVI + (wr * 64 + lane) * 16, u32x4{0, 0, 0, 0});
        if (wid == 4 && lane == 0) sts128(L_ONE, u32x4{0x3F803F80u, 0x3F803F80u, 0x3F803F80u, 0x3F803F80u});
    }
    u32 rpk_src, rpk_dst;
    {
        const u32 id0 = wid * 4 + g4, id = id0 < 24 ? id0 : 24;
        const u32 o = (id * 205) >> 10, t = id - 5 * o;
        rpk_src = L_RAW + (8 * o + (l15 >> 2)) * 160 + t * 32 + (lane & 3) * 8;
        const u32 col = 16 * t + l15, tq = col >= 40 ? 1 : 0, cc = col - 40 * tq + 40 * ty, oct = 5 * rb + o;
        rpk_dst = WS_WQ + (tq * 8 + hg) * 102400 + (oct >> 2) * 5120 + (cc >> 4) * 1024 + ((oct & 3) * 16 + (cc & 15)) * 16;
        rpk_dst = id0 < 25 ? rpk_dst : 0xFFFFFFF0u;
    }
    const u32 xrow = 32 * wr + 16 * (l15 >> 3) + (l15 & 7);
    u32 tag = 0;
    u32 pq[2][6];
    bool kvdone = false;
    auto kvflag_off = [&](u32 G) {
        int gb = (int)(4 * rb + 3 * wr + (lane < 2 ? lane : 2)) - 8; gb = gb < 0 ? 0 : gb;
        return WS_FT + (((4 * G + (hg >> 1)) * 2 + (hg & 1)) * 64 + gb) * 4;
    };
    auto kvissue = [&](auto GC) {
        constexpr int G = decltype(GC)::v;
        const u32 v = G ? (lane >> 2) * 64 + (((lane & 3) ^ (lane >> 4)) * 16) : lane * 16;
#pragma unroll
        for (int i = 0; i < 3; ++i) {
            int gb = (int)(4 * rb + 3 * wr + i) - 8; gb = gb < 0 ? 0 : gb;
            const u32 so = WS_T + ((4 * G + (hg >> 1)) * 64 + gb) * 10240 + ((hg & 1) * 80 + ty * 40) * 64;
            const u32 mb = (3 * wr + i) * (G ? VBLK : 2560);
            dmac<G ? L_V : L_K, 0>(v, rws, so, mb);
            dmac<G ? L_V : L_K, 1024>(v, rws, so, mb);
            if (lane < 32) dmac<G ? L_V : L_K, 2048>(v, rws, so, mb);
        }
        if constexpr (G) {
            if (lane < 12) sts128(L_V + (3 * wr + (lane >> 2)) * VBLK + 2560 + (lane & 3) * 16, u32x4{0x3F803F80u, 0x3F803F80u, 0x3F803F80u, 0x3F803F80u});
        }
    };

    auto phase1 = [&](auto GC) {
        constexpr int G = decltype(GC)::v;
        constexpr int NT = G ? 3 : 2;
        constexpr int NP = G ? 5 : 4;
        constexpr int NQ = G ? 2 : 3;
        constexpr int PD = 4;
        constexpr int NTOT = 100 + 20 * NT;
        const u32 wb0 = L_WR + (16 * (g4 >> 1) + 4 * (g4 & 1) + (l15 >> 2)) * 320 + (lane & 3) * 8;
        const u32 wbE = wb0 + 32 * (g4 & 1), wbO = wb0 - 32 * (g4 & 1);
        const u32 xb0 = L_XR + xrow * 128 + ((g4 ^ (l15 & 6)) * 16);
        const u32 xb1 = xb0 ^ 64;
        const u32 gwb = L_GW + rot * 128 + g4 * 16;
        const u32 l16 = lane * 16;
        bf16x8 xf[10][2][2], gf[2], wf[PD + 1];
        f32x4 acc[2][5], accg[2], qa[2][NT];
        u32 pv = 0;
        bool ok = false;

        auto qissue = [&](auto DC, auto IC_) {
            constexpr int d = decltype(DC)::v, i = decltype(IC_)::v;
            const u32 sa = WS_WQ + cg * 102400 + kbof(2 * d) * 10240, sb = WS_WQ + cg * 102400 + kbof(2 * d + 1) * 10240;
            constexpr int ca = QCELL[2 * d] * CELL, cb = QCELL[2 * d + 1] * CELL;
            if constexpr (i == 0) dmac<ca + G * 4096>(l16, rws, sa + G * 4096 + wr * 1024, wr * 1024);
            else if constexpr (i == 1) dmac<cb + G * 4096>(l16, rws, sb + G * 4096 + wr * 1024, wr * 1024);
            else dmac<8192>(l16, rws, (wr < 2 ? sa : sb) + 8192 + (wr & 1) * 1024, (wr < 2 ? ca : cb) + (wr & 1) * 1024);
        };
        auto loadw = [&](auto NC) {
            constexpr int n = decltype(NC)::v;
            if constexpr (n < 100) {
                constexpr int s = n / 10, f = n % 10, ks2 = f / 5, u = 5 * G + f % 5, slot = s % 4;
                if constexpr (f == 0) {
                    constexpr int y = s == 0 ? NP : s == 1 || s == 2 ? NP + 1 : s == 4 ? NP + (G ? 0 : 1) : s == 7 ? NP + 1 : s == 9 ? NQ : NP;
                    if constexpr (s == 0) tag = wait_rfl<y>(ev) + TAGC; else if constexpr (s == 8) ok = wait_eq<y>(pv, tag); else wait_vm<y>();
                    sched0(); bar(); sched0();
                    STAMP(1 + s);
                    constexpr u32 xo = (s % 3) * 16384;
                    xf[s][0][0] = asbf8(lds128(xb0 + xo));
                    xf[s][0][1] = asbf8(lds128(xb0 + xo + 1024));
                    if constexpr (!G) gf[0] = asbf8(lds128(gwb + s * 128));
                }
                constexpr u32 off = slot * 20480 + ks2 * 10240 + u * 32;
                const u32 base = (u & 1) ? wbO : wbE;
                wf[n % (PD + 1)] = ldtr2(base + off, base + off + 2560);
                if constexpr (f == 1) {
                    constexpr u32 xo = (s % 3) * 16384;
                    xf[s][1][0] = asbf8(lds128(xb1 + xo));
                    xf[s][1][1] = asbf8(lds128(xb1 + xo + 1024));
                }
                if constexpr (!G && f == 3) gf[1] = asbf8(lds128(gwb + s * 128 + 64));
            } else {
                constexpr int m = n - 100, st = m / (2 * NT), f = m % (2 * NT), ks2 = f / NT, a = f % NT + 2 * G;
                if constexpr (f == 0 && st % 2 == 0) {
                    constexpr int d = st / 2;
                    if constexpr (d == 0) wait_vm<2 * NQ>();
                    else if constexpr (d == 1) { if (ty) wait_vm<2 * NQ>(); else wait_vm<2 * NQ + 5>(); }
                    else if constexpr (d == 2) {
                        if (ty) wait_vm<2 * NQ>();
                        else { wait_vm<NQ>(); stflag(tag, WS_FT + ((hg * 2 + G) * 64 + rb * 4 + wr) * 4, rws); }
                        wait_lgkm<PD - 1>();
                    } else ok = wait_eq<0>(pv, tag);
                    sched0(); bar(); sched0();
                    STAMP(11 + d);
                    if constexpr (d >= 3) {
                        if (!kvdone && ok) { kvissue(GC); kvdone = true; }
                    }
                    if constexpr (d >= 2) pv = ldflag(kvflag_off(G), rws);
                }
                wf[n % (PD + 1)] = asbf8(lds128(l16 + QCELL[st] * CELL + ks2 * 5120 + a * 1024));
            }
        };
        auto stash = [&](u32 base, int c, u32x4 v) { sts128(base + wr * 5120 + c * 1024 + l15 * 64 + ((g4 ^ (l15 >> 2)) * 16), v); };
        sfor<0, PD>([&](auto NC) { loadw(NC); });
        sfor<0, NTOT>([&](auto NC) {
            constexpr int n = decltype(NC)::v;
            if constexpr (n + PD < NTOT) loadw(IC<n + PD>{});
            const bf16x8 w = wf[n % (PD + 1)];
            if constexpr (n < 100) {
                constexpr int s = n / 10, f = n % 10, ks2 = f / 5, c = f % 5;
                constexpr bool first = n < 5;
                mfma16a<first>(acc[0][c], xf[s][ks2][0], w);
                mfma16a<first>(acc[1][c], xf[s][ks2][1], w);
                if constexpr (!G && c == 2) {
                    mfma16a<first>(accg[0], gf[ks2], xf[s][ks2][0]);
                    mfma16a<first>(accg[1], gf[ks2], xf[s][ks2][1]);
                }
            } else {
                constexpr int m = n - 100, st = m / (2 * NT), f = m % (2 * NT), ks2 = f / NT, a = f % NT;
                constexpr bool first = m < NT;
                mfma16a<first>(qa[0][a], w, xf[st][ks2][0]);
                mfma16a<first>(qa[1][a], w, xf[st][ks2][1]);
            }
            sched0();
            if constexpr (n < 100) {
                constexpr int sb = (n + PD) / 10, e = sb == 0 ? n : n + PD - 10 * sb;
                constexpr int step = sb == 0 ? 1 : 2;
                if constexpr (sb == 3 && e == 0 && !G) stflag(tag, WS_FQ + hg * 128 + (rb * 2 + ty) * 4, rws);
                if constexpr (sb == 6 && e == 0) pv = ldflag(WS_FQ + hg * 128 + l31 * 4, rws);
                if constexpr (sb < 8 && e % step == 0 && e / step < NP) {
                    constexpr int i = e / step, s2 = sb + 2;
                    if constexpr (G) dma<L_WR + (s2 % 4) * 20480 + i * 1024>(vo[i], rsw, kbof(s2) * 245760, wr * 5120);
                    else dma<L_XR + (s2 % 3) * 16384 + i * 1024>(vo[0], rsx, kbof(s2) * 128 + i * 10240, wr * 4096);
                }
                if constexpr (n == 5) {
                    bf16x8 r = ldtr2(rpk_src, rpk_src + 640);
                    __builtin_amdgcn_raw_buffer_store_b128(__builtin_bit_cast(u32x4, r), wsr, rpk_dst, 0, 16);
                }
                if constexpr (sb == 8) {
                    if constexpr (e == 0) { if (!ok) spin(WS_FQ + hg * 128 + l31 * 4, rws, tag); }
                    if constexpr (e % 2 == 0 && e / 2 < NQ) qissue(IC<0>{}, IC<e / 2>{});
                }
                if constexpr (sb == 9 && e < 2 * NQ) qissue(IC<1 + e / NQ>{}, IC<e % NQ>{});
                if constexpr (sb == 10 && e < NQ) qissue(IC<3>{}, IC<e>{});
            } else {
                constexpr int e4 = n + PD - (100 + 4 * NT);
                if constexpr (e4 >= 0 && e4 < NQ) qissue(IC<4>{}, IC<e4>{});
            }
            if constexpr (n == 99) {
                mfma_pad();
                if (ty == 0) {
                    const u32 to = WS_T + (hg * 64 + rb * 4 + wr) * 10240 + (80 * G + l15) * 64 + g4 * 16;
#pragma unroll
                    for (int c = 0; c < 5; ++c) {
                        u32x4 v = {pk2(acc[0][c][0], acc[0][c][1]), pk2(acc[0][c][2], acc[0][c][3]),
                                   pk2(acc[1][c][0], acc[1][c][1]), pk2(acc[1][c][2], acc[1][c][3])};
                        __builtin_amdgcn_raw_buffer_store_b128(v, wsr, to + c * 1024, 0, 16);
                        sched0();
                    }
                } else if constexpr (G) {
#pragma unroll
                    for (int c = 0; c < 5; ++c) {
                        u32x4 v = {pk2(acc[0][c][0], acc[0][c][1]), pk2(acc[0][c][2], acc[0][c][3]),
                                   pk2(acc[1][c][0], acc[1][c][1]), pk2(acc[1][c][2], acc[1][c][3])};
                        stash(L_VS, c, v);
                        sched0();
                    }
                }
                if constexpr (!G) {
                    if (g4 == 0) {
#pragma unroll
                        for (int t = 0; t < 2; ++t) {
                            float gv = __builtin_amdgcn_rcpf(1.f + __builtin_amdgcn_exp2f(-accg[t][0] * LOG2E));
                            stsf(L_G + (xrow + 8 * t) * 4, gv);
                        }
                    }
                }
                asm volatile("s_nop 1");
            }
            if constexpr (!G && n >= 99 && n < 109 && (n - 99) % 2 == 0) {
                constexpr int c = (n - 99) / 2;
                if (ty) {
                    u32x4 v = {pk2(phi(acc[0][c][0]), phi(acc[0][c][1])), pk2(phi(acc[0][c][2]), phi(acc[0][c][3])),
                               pk2(phi(acc[1][c][0]), phi(acc[1][c][1])), pk2(phi(acc[1][c][2]), phi(acc[1][c][3]))};
                    stash(L_PK, c, v);
                }
                asm volatile("s_nop 1");
            }
            if constexpr (!G && n + PD == 100 + 8 * NT) {
                if (ty) {
                    const u32 ra = 20 * wr + (l31 < 20 ? l31 : 0);
                    const u32 sw = (ra >> 2) & 3;
                    f32x16 d;
#pragma unroll
                    for (int i = 0; i < 16; ++i) d[i] = 0.f;
#pragma unroll
                    for (int w2 = 0; w2 < 4; ++w2)
#pragma unroll
                        for (int ks = 0; ks < 2; ++ks) {
                            const u32 o = w2 * 5120 + ra * 64 + (((2 * ks + hf) ^ sw) * 16);
                            bf16x8 a = asbf8(lds128(L_PK + o));
                            bf16x8 bb = asbf8(lds128(l31 < 20 ? L_VS + o : L_ONE));
                            d = mfma32(a, bb, d);
                        }
                    const u32 po = WS_KVP + ((hg * 16 + rb) * 4 + wr) * KVP_H + l31 * 16;
                    if (l31 < 21) {
                        f32x4 q0 = {d[0], d[1], d[2], d[3]}, q1 = {d[4], d[5], d[6], d[7]}, q2 = {d[8], d[9], d[10], d[11]};
                        __builtin_amdgcn_raw_buffer_store_b128(__builtin_bit_cast(u32x4, q0), wsr, po + hf * 336, 0, 16);
                        __builtin_amdgcn_raw_buffer_store_b128(__builtin_bit_cast(u32x4, q1), wsr, po + (2 + hf) * 336, 0, 16);
                        if (hf == 0) __builtin_amdgcn_raw_buffer_store_b128(__builtin_bit_cast(u32x4, q2), wsr, po + 4 * 336, 0, 16);
                    }
                }
                asm volatile("s_nop 1");
            }
            if constexpr (!G && n + PD == 100 + 12 * NT) {
                if (ty) stflag(tag, WS_FKV + ((hg * 4 + wr) * 16 + rb) * 4, rws);
            }
            sched0();
        });
        mfma_pad();
        STAMP(16);
#pragma unroll
        for (int t = 0; t < 2; ++t) {
            const u32 row = xrow + 8 * t;
#pragma unroll
            for (int a = 0; a < (G ? 1 : 2); ++a) {
                const u32 Q = 4 * (a + 2 * G) + g4;
                const u32 hh = Q >= 5 ? 1 : 0, dq = Q - 5 * hh;
                u32x2 v = {pk2(qa[t][a][0] * QSCALE, qa[t][a][1] * QSCALE), pk2(qa[t][a][2] * QSCALE, qa[t][a][3] * QSCALE)};
                const u32 ad = dq < 4 ? L_Q + hh * 4096 + row * 32 + dq * 8 : L_QR + hh * 1024 + row * 8;
                if (!G || g4 < 2) sts64(ad, v);
            }
#pragma unroll
            for (int a = 0; a < 3; ++a) {
                if constexpr (G) {
                    pq[t][2 * a] = pk2(phi(qa[t][a][0]), phi(qa[t][a][1]));
                    pq[t][2 * a + 1] = pk2(phi(qa[t][a][2]), phi(qa[t][a][3]));
                } else pq[t][2 * a] = pq[t][2 * a + 1] = 0;
            }
        }
        ok = wait_eq<0>(pv, tag);
        if (!kvdone) {
            if (!ok) spin(kvflag_off(G), rws, tag);
            kvissue(GC);
        }
        STAMP(17);
    };
    if (opaque_s(wid) < 4) phase1(IC<0>{}); else phase1(IC<1>{});

    asm volatile("v_mbcnt_lo_u32_b32 %0, -1, 0\n\tv_mbcnt_hi_u32_b32 %0, -1, %0" : "=v"(lane));
    l15 = lane & 15; g4 = lane >> 4; l31 = lane & 31; hf = lane >> 5;
    spin(WS_FKV + (hg * 4 + 2 * ty) * 64 + l31 * 4, rws, tag);
    STAMP(18);
    {
        const u32 qd = wid * 32 + l31;
        if (qd < 210) {
            const u32 hb = qd >= 105 ? 1 : 0, qi = qd - 105 * hb;
            const u32 po = WS_KVP + ((hg * 16 + hf * 8) * 4 + 2 * ty + hb) * KVP_H + qi * 16;
            f32x4 s = {0, 0, 0, 0};
            u32x4 v[8];
#pragma unroll
            for (int i = 0; i < 8; ++i) v[i] = __builtin_amdgcn_raw_buffer_load_b128(wsr, po + i * (4 * KVP_H), 0, 16);
#pragma unroll
            for (int i = 0; i < 8; ++i) s += __builtin_bit_cast(f32x4, v[i]);
#pragma unroll
            for (int r = 0; r < 4; ++r) {
                u32 a = f2u(s[r]);
                auto sw = __builtin_amdgcn_permlane32_swap(a, a, false, false);
                s[r] = u2f(sw[0]) + u2f(sw[1]);
            }
            const u32 dq = qi / 21, e = qi - dq * 21;
            const u32 Q = 10 + 5 * hb + dq, gq = Q & 3, u = (Q >> 2) - (2 + hb);
            u32x2 pv = {pk2(s[0], s[1]), pk2(s[2], s[3])};
            const u32 fr = e < 16 ? hb : 2 + hb;
            const u32 ix = e < 16 ? e : (e < 20 ? 4 * hb + e - 16 : 8 + hb + 4 * hf);
            if (hf == 0 || e == 20) sts64(L_KVI + fr * 1024 + (gq * 16 + ix) * 16 + u * 8, pv);
        }
    }
    wait_vm<0>();
    wait_lgkm<0>();
    bar();
    STAMP(19);
    const u32 H0 = 4 * hg + 2 * ty;
    const u32 unit = H0 >> 3, h8 = H0 & 7;
    if (wid >= 4) {
        bf16x8 m0 = asbf8(lds128(L_KVI + lane * 16)), m1 = asbf8(lds128(L_KVI + 1024 + lane * 16));
        bf16x8 x0 = asbf8(lds128(L_KVI + 2048 + lane * 16)), x1 = asbf8(lds128(L_KVI + 3072 + lane * 16));
#pragma unroll
        for (int t = 0; t < 2; ++t) {
            const u32 row = 32 * wr + 16 * (l15 >> 3) + (l15 & 7) + 8 * t;
            const float gv = 1.f - ldsf(L_G + row * 4);
            bf16x8 p23 = asbf8(u32x4{pq[t][0], pq[t][1], pq[t][2], pq[t][3]});
            bf16x8 p34 = asbf8(u32x4{pq[t][2], pq[t][3], pq[t][4], pq[t][5]});
            const f32x4 z = {0, 0, 0, 0};
            f32x4 n0 = mfma16(m0, p23, z), n1 = mfma16(m1, p34, z);
            f32x4 xx = mfma16(x0, p23, z);
            xx = mfma16(x1, p34, xx);
            u32 a0 = f2u(xx[0]), a1 = f2u(xx[1]);
            auto s0 = __builtin_amdgcn_permlane32_swap(a0, a0, false, false);
            auto s1 = __builtin_amdgcn_permlane32_swap(a1, a1, false, false);
            const float r0 = gv * __builtin_amdgcn_rcpf(u2f(s0[1]) + 1e-6f);
            const float r1 = gv * __builtin_amdgcn_rcpf(u2f(s1[1]) + 1e-6f);
            const u32 ob = WS_O + (rb * 8 + (row >> 4)) * 40960 + (row & 15) * 16;
            u32x4 v = {pk2(n0[0] * r0, n0[1] * r0), pk2(n0[2] * r0, n0[3] * r0), pk2(n1[0] * r1, n1[1] * r1), pk2(n1[2] * r1, n1[3] * r1)};
            __builtin_amdgcn_raw_buffer_store_b128(v, wsr, ob + (80 + 20 * unit + 4 * (h8 >> 1) + g4) * 256, 0, 16);
            const float rr = g4 == 0 ? r0 : r1;
            u32x2 v2 = {pk2(xx[0] * rr, xx[1] * rr), pk2(xx[2] * rr, xx[3] * rr)};
            if (g4 < 2) __builtin_amdgcn_raw_buffer_store_b64(v2, wsr, ob + (80 + 20 * unit + 16 + g4 + 2 * (h8 >> 2)) * 256 + ((h8 >> 1) & 1) * 8, 0, 16);
        }
    }
    STAMP(20);
    {
        const u32 hh = wid >> 2, qt = wr;
        const u32 qrow = 32 * qt + l31;
        bf16x8 qf = asbf8(lds128(L_Q + hh * 4096 + qrow * 32 + hf * 16));
        u32x2 qr_ = lds64(L_QR + hh * 1024 + qrow * 8);
        if (hf) qr_ = u32x2{0, 0};
        bf16x4 qr = __builtin_bit_cast(bf16x4, qr_);
        const u32 kh = g4 & 1, r4 = l15 >> 2, c4 = lane & 3;
        const u32 kcol = kh * 32 + (((c4 & 1) << 1) | (c4 >> 1)) * 8;
        const u32 ka = L_K + qt * 2560 + hh * 1280 + (8 * hf + r4) * 64 + kcol;
        const u32 kr = L_K + qt * 2560 + hh * 1280 + (16 + r4) * 64 + kcol;
        f32x16 S[9];
        f32x16 zero;
#pragma unroll
        for (int i = 0; i < 16; ++i) zero[i] = 0.f;
#pragma unroll
        for (int i = 0; i < 9; ++i) {
            bf16x8 kf = ldtr2(ka + i * 2560, ka + i * 2560 + 256);
            bf16x4 k2 = ldtr(kr + i * 2560);
            S[i] = mfma32(kf, qf, zero);
            S[i] = mfma32k8(k2, qr, S[i]);
        }
        const int nskip = 8 - 4 * (int)rb - (int)qt;
        if (nskip > 0) {
#pragma unroll
            for (int i = 0; i < 8; ++i)
                if (i < nskip) {
#pragma unroll
                    for (int r = 0; r < 16; ++r) S[i][r] = -1e30f;
                }
        }
        const int qq = (int)l31 - 4 * (int)hf;
#pragma unroll
        for (int r = 0; r < 16; ++r) S[0][r] = ((r & 3) + 8 * (r >> 2) <= qq) ? S[8][r] : S[0][r];
        float m = S[0][0];
#pragma unroll
        for (int i = 0; i < 8; ++i)
#pragma unroll
            for (int r = 0; r < 16; r += 2) m = fmaxf(fmaxf(m, S[i][r]), S[i][r + 1]);
        {
            u32 a = f2u(m);
            auto sw = __builtin_amdgcn_permlane32_swap(a, a, false, false);
            m = fmaxf(u2f(sw[0]), u2f(sw[1]));
        }
        const u32 vrow = 20 * hh + l31;
        const u32 vsw = (vrow >> 2) & 3;
        const u32 va0 = L_V + qt * VBLK + (l31 < 20 ? vrow * 64 + ((hf ^ vsw) * 16) : 2560);
        const u32 va1 = L_V + qt * VBLK + (l31 < 20 ? vrow * 64 + (((2 + hf) ^ vsw) * 16) : 2560);
        f32x16 o = zero;
#pragma unroll
        for (int i = 0; i < 8; ++i) {
            u32 p[8];
#pragma unroll
            for (int j = 0; j < 8; ++j)
                p[j] = pk2(__builtin_amdgcn_exp2f(S[i][2 * j] - m), __builtin_amdgcn_exp2f(S[i][2 * j + 1] - m));
            if (i == 0) {
                u32 pd[8];
#pragma unroll
                for (int j = 0; j < 8; ++j) {
                    const int k0 = ((2 * j) & 3) + 8 * ((2 * j) >> 2);
                    const u32 mk = (k0 <= qq ? 0xFFFFu : 0u) | (k0 + 1 <= qq ? 0xFFFF0000u : 0u);
                    pd[j] = p[j] & mk;
                    p[j] = p[j] & ~mk;
                }
                o = mfma32(asbf8(lds128(va0 + 8 * VBLK)), asbf8(u32x4{pd[0], pd[1], pd[2], pd[3]}), o);
                o = mfma32(asbf8(lds128(va1 + 8 * VBLK)), asbf8(u32x4{pd[4], pd[5], pd[6], pd[7]}), o);
            }
            o = mfma32(asbf8(lds128(va0 + i * VBLK)), asbf8(u32x4{p[0], p[1], p[2], p[3]}), o);
            o = mfma32(asbf8(lds128(va1 + i * VBLK)), asbf8(u32x4{p[4], p[5], p[6], p[7]}), o);
        }
        const float sc = ldsf(L_G + qrow * 4) * __builtin_amdgcn_rcpf(o[12]);
        const u32 hx = h8 + hh;
        const u32 ob = WS_O + (rb * 8 + (qrow >> 4)) * 40960 + (qrow & 15) * 16;
        u32x4 v = {pk2(o[0] * sc, o[1] * sc), pk2(o[2] * sc, o[3] * sc), pk2(o[4] * sc, o[5] * sc), pk2(o[6] * sc, o[7] * sc)};
        __builtin_amdgcn_raw_buffer_store_b128(v, wsr, ob + (20 * unit + 4 * (hx >> 1) + 2 * (hx & 1) + hf) * 256, 0, 16);
        u32x2 v2 = {pk2(o[8] * sc, o[9] * sc), pk2(o[10] * sc, o[11] * sc)};
        if (hf == 0) __builtin_amdgcn_raw_buffer_store_b64(v2, wsr, ob + (20 * unit + 16 + (hx & 1) + 2 * (hx >> 2)) * 256 + ((hx >> 1) & 1) * 8, 0, 16);
    }
    STAMP(21);
    wait_vm<0>();
    bar();
    if (wid == 0) stflag(tag, WS_FOA + rb * 64 + cg * 4, rws);
    STAMP(22);
    {
        const u32 r64 = xcd * 4 + (idx >> 3), c80 = idx & 7;
        const u32 kh = wid >> 2, rt = wr;
        const u32x4 rsa = mkrs(woa, 640 * 1280), rsb = mkrs(wob, 640 * 1280);
        auto unit_dma = [&](u32x4 rs, int un, int slot) {
#pragma unroll
            for (int i = 0; i < 4; ++i) {
                const u32 pc = wid + 8 * i;
                if (pc < 25) {
                    const u32 ch = pc * 64 + lane;
                    const u32 row = ch / 10, c = ch - row * 10;
                    dma<0>(row * 1280 + c * 16, rs, un * 160 * 1280 + c80 * 160, slot * 25600 + pc * 1024);
                }
            }
        };
        unit_dma(rsb, 0, 0); unit_dma(rsb, 2, 1); unit_dma(rsb, 1, 2); unit_dma(rsb, 3, 3); unit_dma(rsa, 0, 4); unit_dma(rsa, 2, 5);
        if (b == 255 && wid == 6) {
            spin(WS_FQ + lane * 4, rws, tag); spin(WS_FQ + 256 + lane * 4, rws, tag);
            spin(WS_FQ + 512 + lane * 4, rws, tag); spin(WS_FQ + 768 + lane * 4, rws, tag);
            stflag(tag, WS_E, rws);
        }
        if (wid == 7) spin(WS_FOA + (r64 >> 1) * 64 + l15 * 4, rws, tag);
        wait_vm<0>();
        bar();
        STAMP(23);
        const u32 oa = WS_O + (r64 * 4 + rt) * 40960 + lane * 16 + kh * 10240;
        u32x4 of[20];
#pragma unroll
        for (int i = 0; i < 10; ++i) of[i] = __builtin_amdgcn_raw_buffer_load_b128(wsr, oa + 20480 + i * 1024, 0, 16);
#pragma unroll
        for (int i = 0; i < 10; ++i) of[10 + i] = __builtin_amdgcn_raw_buffer_load_b128(wsr, oa + i * 1024, 0, 16);
        const u32 r4 = l15 >> 2, c4 = lane & 3;
        const u32 aA = kh * 25600 + (20 * (g4 >> 1) + 4 * (g4 & 1) + r4) * 160 + c4 * 8;
        const u32 aB = kh * 25600 + (4 * g4 + r4) * 160 + c4 * 8;
        const u32 aR = kh * 25600 + (20 * (g4 & 1) + 80 * (g4 >> 1) + 16 + r4) * 160 + c4 * 8;
        f32x4 acc[5];
#pragma unroll
        for (int c = 0; c < 5; ++c) acc[c] = f32x4{0, 0, 0, 0};
        auto do_unit = [&](auto BR, u32 so, int fo) {
            constexpr int br = decltype(BR)::v;
#pragma unroll
            for (int ks = 0; ks < 5; ++ks) {
                const bf16x8 ofr = asbf8(of[fo + ks]);
#pragma unroll
                for (int c = 0; c < 5; ++c) {
                    bf16x8 w;
                    if (ks < 4) {
                        if (br == 0) w = ldtr2(aA + so + ks * 6400 + c * 32, aA + so + ks * 6400 + c * 32 + 8 * 160);
                        else w = ldtr2(aB + so + ks * 6400 + c * 32, aB + so + ks * 6400 + c * 32 + 20 * 160);
                    } else w = ldtr2(aR + so + c * 32, aR + so + c * 32 + 40 * 160);
                    acc[c] = mfma16(w, ofr, acc[c]);
                }
            }
        };
        do_unit(IC<1>{}, 0, 0);
        wait_lgkm<0>(); sched0();
        bar();
        unit_dma(rsa, 1, 0); unit_dma(rsa, 3, 1);
        do_unit(IC<1>{}, 51200, 5);
        do_unit(IC<0>{}, 102400, 10);
        wait_vm<0>();
        bar();
        do_unit(IC<0>{}, 0, 15);
        STAMP(24);
        const u32 ra = 51200 + rt * 5120 + lane * 16;
        wait_lgkm<0>(); sched0();
        if (kh == 1) {
#pragma unroll
            for (int c = 0; c < 5; ++c) sts128(ra + c * 1024, __builtin_bit_cast(u32x4, acc[c]));
        }
        wait_lgkm<0>();
        bar();
        if (kh == 0) {
            u16* op = out + (r64 * 64 + rt * 16 + l15) * 640 + c80 * 80 + g4 * 4;
#pragma unroll
            for (int c = 0; c < 5; ++c) {
                f32x4 o2 = __builtin_bit_cast(f32x4, lds128(ra + c * 1024));
                f32x4 s = acc[c] + o2;
                u32x2 v = {pk2(s[0], s[1]), pk2(s[2], s[3])};
                *(u32x2*)(op + c * 16) = v;
            }
        }
        STAMP(25);
    }
    asm volatile(".Lcode_hi:");
}

extern "C" void kernel_launch(void* const* d_in, const int* in_sizes, int n_in, void* d_out, int out_size, void* d_ws,
                              size_t ws_size, hipStream_t stream) {
    (void)in_sizes; (void)n_in; (void)out_size; (void)ws_size;
    (void)hipFuncSetAttribute((const void*)fused_kernel, hipFuncAttributeMaxDynamicSharedMemorySize, LDS_BYTES);
    fused_kernel<<<256, 512, LDS_BYTES, stream>>>((const u16*)d_in[0], (const u16*)d_in[1], (const u16*)d_in[2], (const u16*)d_in[4],
                                                  (const u16*)d_in[5], (const u16*)d_in[6], (u16*)d_out, (char*)d_ws);
}
```

```cpp
#include <hip/hip_runtime.h>
#include <stdint.h>
#pragma clang diagnostic ignored "-Wint-to-pointer-cast"

typedef unsigned short u16;
typedef unsigned int u32;
typedef unsigned long long u64;
typedef __attribute__((ext_vector_type(8))) __bf16 bf16x8;
typedef __attribute__((ext_vector_type(4))) __bf16 bf16x4;
typedef __attribute__((ext_vector_type(2))) __bf16 bf16x2;
typedef __attribute__((ext_vector_type(2))) float f32x2;
typedef __attribute__((ext_vector_type(4))) float f32x4;
typedef __attribute__((ext_vector_type(16))) float f32x16;
typedef __attribute__((ext_vector_type(4))) u32 u32x4;
typedef __attribute__((ext_vector_type(2))) u32 u32x2;
typedef __attribute__((ext_vector_type(4))) short s16x4;
#define AS3 __attribute__((address_space(3)))
#define DEVI __device__ __forceinline__

constexpr u32 WS_E   = 0;
constexpr u32 WS_FQ  = 4096;
constexpr u32 WS_FT  = 8192;
constexpr u32 WS_FKV = 12288;
constexpr u32 WS_G   = 16384;
constexpr u32 WS_T   = 131072;
constexpr u32 WS_KVP = WS_T + 2 * 16 * 64 * 2560;
constexpr u32 WS_O   = WS_KVP + 16 * 16 * 2048;
constexpr u32 WS_WQ  = WS_O + 128 * 40960;
constexpr u32 WS_WO  = WS_WQ + 16 * 102400;
constexpr u32 WS_END = WS_WO + 16 * 102400;
constexpr u32 OOB = 0x80000000u;

constexpr u32 WREG  = 5152;
constexpr u32 XO    = 20608;
__device__ constexpr u32 SL[3] = {102400, 36992, 0};
constexpr u32 L_SCR = 139392;
constexpr u32 L_DST = L_SCR + 16384;
constexpr u32 L_GW  = L_DST + 4096;
constexpr u32 L_DUMP = L_GW + 1024;
constexpr u32 L_REC = 87040;
constexpr u32 L_QB  = 89088;
constexpr u32 L_K   = 101376;
constexpr u32 L_V   = 132096;
constexpr u32 L_HQ  = 162944, L_MC = L_HQ + 16;
constexpr u32 LDS_BYTES = 163840;
static_assert(L_DUMP + 256 <= L_HQ && L_V + 6 * 5136 <= L_HQ, "lds");

constexpr float LOG2E = 1.4426950408889634f;
constexpr float QSCALE = 0.22360679774997896f * 1.4426950408889634f;
constexpr u32 TAGC = 0x7F4A7C15u;
constexpr u32 ONES = 0x3F803F80u;

DEVI u32 pk2(float a, float b) { f32x2 v = {a, b}; bf16x2 r = __builtin_convertvector(v, bf16x2); return __builtin_bit_cast(u32, r); }
DEVI u32 f2u(float f) { return __builtin_bit_cast(u32, f); }
DEVI float u2f(u32 u) { return __builtin_bit_cast(float, u); }
DEVI float blo(u32 u) { return u2f(u << 16); }
DEVI float bhi(u32 u) { return u2f(u & 0xFFFF0000u); }
DEVI u32 umin(u32 a, u32 b) { return a < b ? a : b; }
DEVI u32 umax(u32 a, u32 b) { return a > b ? a : b; }
DEVI u32 pk2m(float a, float b) { return umin(pk2(a, b), 0xFFFFFFFEu); }
DEVI bf16x8 asbf8(u32x4 v) { return __builtin_bit_cast(bf16x8, v); }
DEVI f32x4 mfma16(bf16x8 a, bf16x8 b, f32x4 c) { return __builtin_amdgcn_mfma_f32_16x16x32_bf16(a, b, c, 0, 0, 0); }
template <bool FIRST = false> DEVI void mfma16a(f32x4& c, bf16x8 a, bf16x8 b) {
    if constexpr (FIRST) asm volatile("v_mfma_f32_16x16x32_bf16 %0, %1, %2, 0" : "=&v"(c) : "v"(a), "v"(b));
    else asm volatile("v_mfma_f32_16x16x32_bf16 %0, %1, %2, %0" : "+v"(c) : "v"(a), "v"(b));
}
DEVI void mfma_pad() { asm volatile("s_nop 7\n\ts_nop 3" ::: "memory"); }
DEVI f32x16 mfma32(bf16x8 a, bf16x8 b, f32x16 c) { return __builtin_amdgcn_mfma_f32_32x32x16_bf16(a, b, c, 0, 0, 0); }
DEVI f32x16 mfma32k8(bf16x4 a, bf16x4 b, f32x16 c) {
    return __builtin_amdgcn_mfma_f32_32x32x8bf16_1k(__builtin_bit_cast(s16x4, a), __builtin_bit_cast(s16x4, b), c, 0, 0, 0);
}
DEVI bf16x4 ldtr(u32 a) { return __builtin_amdgcn_ds_read_tr16_b64_v4bf16((AS3 bf16x4*)a); }
DEVI bf16x8 cat8(bf16x4 x, bf16x4 y) { return __builtin_shufflevector(x, y, 0, 1, 2, 3, 4, 5, 6, 7); }
DEVI bf16x8 ldtr2(u32 a, u32 b) { bf16x4 x = ldtr(a), y = ldtr(b); return cat8(x, y); }
DEVI u32x4 lds128(u32 a) { return *(AS3 u32x4*)a; }
DEVI u32x2 lds64(u32 a) { return *(AS3 u32x2*)a; }
DEVI u32 lds32(u32 a) { return *(AS3 u32*)a; }
DEVI void sts128(u32 a, u32x4 v) { *(AS3 u32x4*)a = v; }
DEVI void sts64(u32 a, u32x2 v) { *(AS3 u32x2*)a = v; }
DEVI void sts32(u32 a, u32 v) { *(AS3 u32*)a = v; }
DEVI u32 rfl(u32 v) { return __builtin_amdgcn_readfirstlane(v); }
DEVI u32 opaque_s(u32 v) { asm volatile("" : "+s"(v)); return v; }
DEVI u32 opaque_v(u32 v) { asm volatile("" : "+v"(v)); return v; }
DEVI void cfence() { asm volatile("" ::: "memory"); }

DEVI u32x4 mkrs(const void* p, u32 bytes) { return u32x4{(u32)(uintptr_t)p, (u32)((uintptr_t)p >> 32) & 0xFFFFu, bytes, 0x00020000u}; }
template <int MO, int OFF = 0> DEVI void dma(u32 voff, u32x4 rs, u32 soff, u32 mb) {
    asm volatile("s_add_u32 m0, %3, %5\n\ts_nop 0\n\tbuffer_load_dwordx4 %0, %1, %2 offen offset:%4 lds"
                 :: "v"(voff), "s"(rs), "s"(soff), "s"(mb), "i"(OFF), "i"(MO) : "memory", "scc");
}
template <int MO, int OFF = 0> DEVI void dmac(u32 voff, u32x4 rs, u32 soff, u32 mb) {
    asm volatile("s_add_u32 m0, %3, %5\n\ts_nop 0\n\tbuffer_load_dwordx4 %0, %1, %2 offen offset:%4 sc1 lds"
                 :: "v"(voff), "s"(rs), "s"(soff), "s"(mb), "i"(OFF), "i"(MO) : "memory", "scc");
}
DEVI u32 ldflag(u32 voff, u32x4 rs) {
    u32 v;
    asm volatile("buffer_load_dword %0, %1, %2, 0 offen sc1" : "=v"(v) : "v"(voff), "s"(rs) : "memory");
    return v;
}
DEVI void stflag(u32 val, u32 soff, u32x4 rs) {
    asm volatile("s_mov_b64 exec, 1\n\tbuffer_store_dword %0, off, %1, %2 sc1\n\ts_mov_b64 exec, -1" :: "v"(val), "s"(rs), "s"(soff) : "memory");
}
template <int OFF = 0> DEVI void st128(u32x4 v, u32 voff, u32x4 rs, u32 soff) {
    asm volatile("buffer_store_dwordx4 %0, %1, %2, %3 offen offset:%4 sc1\n\ts_nop 1" :: "v"(v), "v"(voff), "s"(rs), "s"(soff), "i"(OFF) : "memory");
}
template <int OFF = 0> DEVI void st128n(u32x4 v, u32 voff, u32x4 rs, u32 soff) {
    asm volatile("buffer_store_dwordx4 %0, %1, %2, %3 offen offset:%4\n\ts_nop 1" :: "v"(v), "v"(voff), "s"(rs), "s"(soff), "i"(OFF) : "memory");
}
template <int N> DEVI void wait_vm() { asm volatile("s_waitcnt vmcnt(%0)" ::"i"(N) : "memory"); }
template <int N> DEVI u32 wait_rfl(u32 v) { u32 r; asm volatile("s_waitcnt vmcnt(%2)\n\tv_readfirstlane_b32 %0, %1" : "=s"(r) : "v"(v), "i"(N) : "memory"); return r; }
template <int N> DEVI bool wait_eq(u32 v, u32 tag) {
    u64 m; asm volatile("s_waitcnt vmcnt(%3)\n\tv_cmp_ne_u32_e64 %0, %2, %1" : "=s"(m) : "v"(v), "s"(tag), "i"(N) : "memory");
    return m == 0ull;
}
template <int N> DEVI void wait_lgkm() { asm volatile("s_waitcnt lgkmcnt(%0)" ::"i"(N) : "memory"); }
DEVI void bar() { asm volatile("s_barrier" ::: "memory"); }
DEVI void sched0() { __builtin_amdgcn_sched_barrier(0); }
DEVI void spin(u32 voff, u32x4 rs, u32 tag) {
#pragma nounroll
    for (u32 i = 0; i < (1u << 17); ++i) {
        u32 v = ldflag(voff, rs);
        if (wait_eq<0>(v, tag)) break;
        __builtin_amdgcn_s_sleep(1);
    }
}
DEVI void ctl_set(u32 a, u32 v) { asm volatile("s_mov_b64 exec, 1\n\tds_write_b32 %0, %1\n\ts_mov_b64 exec, -1" :: "v"(a), "v"(v) : "memory"); }
DEVI u32x4 ctl_get(u32 a) { return *(volatile AS3 u32x4*)a; }
DEVI bool ctl_ge(u32x4 v, u32 t) { return rfl(umin(umin(v[0], v[1]), umin(v[2], v[3]))) >= t; }
DEVI void ctl_wait(u32 a, u32 t) {
#pragma nounroll
    for (u32 i = 0; i < (1u << 20); ++i) { if (ctl_ge(ctl_get(a), t)) break; __builtin_amdgcn_s_sleep(1); }
    cfence();
}
DEVI void ctl_wait1(u32 a, u32 t) {
#pragma nounroll
    for (u32 i = 0; i < (1u << 20); ++i) { if (rfl(*(volatile AS3 u32*)a) >= t) break; __builtin_amdgcn_s_sleep(1); }
    cfence();
}
DEVI bool marked(u32 m) { return __builtin_amdgcn_ballot_w64(m == 0xFFFFFFFFu) != 0ull; }
DEVI u32 mx4(u32 m, u32x4 v) { return umax(umax(umax(m, v[0]), v[1]), umax(v[2], v[3])); }

DEVI u32 phi2(float a, float b) {
    const f32x2 t = {a, b}, m = t * LOG2E, u = t + 1.f;
    f32x2 r = {__builtin_amdgcn_fmed3f(u[0], __builtin_amdgcn_exp2f(m[0]), 1.f), __builtin_amdgcn_fmed3f(u[1], __builtin_amdgcn_exp2f(m[1]), 1.f)};
    r += 1e-6f;
    return pk2(r[0], r[1]);
}
struct OF { u32x4 m[2][4]; u32x2 r[2][2]; };
template <int OFF> DEVI void rl128(u32x4& v, u32 voff, u32x4 rs, u32 soff) {
    asm volatile("buffer_load_dwordx4 %0, %1, %2, %3 offen offset:%4 sc1" : "+v"(v) : "v"(voff), "s"(rs), "s"(soff), "i"(OFF) : "memory");
}
template <int OFF> DEVI void rl64(u32x2& v, u32 voff, u32x4 rs, u32 soff) {
    asm volatile("buffer_load_dwordx2 %0, %1, %2, %3 offen offset:%4 sc1" : "+v"(v) : "v"(voff), "s"(rs), "s"(soff), "i"(OFF) : "memory");
}
#define OF_OPS(o) "+v"(o.m[0][0]), "+v"(o.m[0][1]), "+v"(o.m[0][2]), "+v"(o.m[0][3]), "+v"(o.m[1][0]), "+v"(o.m[1][1]), "+v"(o.m[1][2]), "+v"(o.m[1][3]), \
                  "+v"(o.r[0][0]), "+v"(o.r[0][1]), "+v"(o.r[1][0]), "+v"(o.r[1][1])
DEVI void of_wait(OF& o) { asm volatile("s_waitcnt vmcnt(0)" : OF_OPS(o) :: "memory"); }
DEVI void of_tie(OF& o, bf16x8& w) { asm volatile("" : OF_OPS(o), "+v"(w) :: "memory"); }
DEVI float dot2(u32 a, u32 b, float c) { return __builtin_amdgcn_fdot2_f32_bf16(__builtin_bit_cast(bf16x2, a), __builtin_bit_cast(bf16x2, b), c, false); }

#define STAMP(i)

template <int N> struct IC { static constexpr int v = N; };
template <int I, int N, class F> DEVI void sfor(F&& f) { if constexpr (I < N) { f(IC<I>{}); sfor<I + 1, N>(f); } }

__global__ __launch_bounds__(512, 2) void fused_kernel(const u16* __restrict__ x, const u16* __restrict__ wa, const u16* __restrict__ woa,
                                                       const u16* __restrict__ wb, const u16* __restrict__ wob, const u16* __restrict__ gw,
                                                       u16* __restrict__ out, char* __restrict__ ws) {
    const u32 lane = threadIdx.x & 63;
    const u32 wid = rfl(threadIdx.x >> 6);
    const u32 b = blockIdx.x, xcd = b & 7, idx = b >> 3;
    STAMP(0);
    if (wid == 3) {
        u64 pc; u32 len;
        asm volatile("s_getpc_b64 %0\n.Lcode_lo:\n\ts_mov_b32 %1, .Lcode_hi-.Lcode_lo" : "=s"(pc), "=s"(len));
        u32 o = (idx & 15) * 4096 + lane * 64;
        o = o < len ? o : 0;
        asm volatile("s_mov_b32 m0, %2\n\ts_nop 0\n\tglobal_load_lds_dword %0, %1" :: "v"(o), "s"(pc), "i"(L_DUMP) : "memory");
    }
    const u32 rb = (xcd & 1) * 8 + (idx >> 2), c4 = idx & 3, cg = (xcd >> 1) * 4 + c4;
    const u32 hg = cg & 7, ty = cg >> 3;
    const u32 hp = 2 * hg + ty;
    const u32 wr = wid & 3;
    const u32x4 rws = mkrs(ws, WS_END);
    const __amdgpu_buffer_rsrc_t wsr = __builtin_amdgcn_make_buffer_rsrc(ws, 0, WS_END, 0x00020000);
    const u32 l15 = lane & 15, g4 = lane >> 4, l31 = lane & 31, hf = lane >> 5, l16 = lane * 16;

    if (__builtin_expect(wid >= 4, 1)) {
        const u32 rot = (rb + 3 * c4) % 10;
        auto kbof = [&](u32 s) { u32 k = rot + s; return k >= 10 ? k - 10 : k; };
        const u32x4 rsx = mkrs(x, 2048 * 1280), rsw = mkrs(ty ? wb : wa, 640 * 3840);
        const u32 vx = (rb * 128 + 32 * wr + (lane >> 3)) * 1280 + (((lane & 7) ^ ((lane >> 3) & 6)) * 16);
        u32 vo[5];
#pragma unroll
        for (int i = 0; i < 5; ++i) {
            const u32 ch = 64 * i + lane, pr = (ch * 205) >> 12, j = ch - pr * 20;
            const u32 k = 32 * (wr >> 1) + 8 * (wr & 1) + (pr & 7) + 2 * (pr & 8);
            vo[i] = k * 3840 + (ty ? 1280 + 160 * hg + (j >= 10 ? 1120 : 0) : 1280 + 320 * hg) + j * 16;
        }
        auto issue = [&](auto SC) {
            constexpr int s = decltype(SC)::v;
            const u32 kb = kbof(s);
            sfor<0, 4>([&](auto I) { dma<SL[s % 3] + XO + I.v * 1024>(vx, rsx, kb * 128 + I.v * 10240, wr * 4096); });
            sfor<0, 5>([&](auto I) { dma<SL[s % 3] + I.v * 1024>(vo[I.v], rsw, kb * 245760, wr * WREG); });
        };
        const u32 sg = xcd >> 1, gj = 3 * c4 + sg;
        issue(IC<0>{});
        const u32 ev = ldflag(0, rws);
        dma<L_GW>((lane & 7) * 16, mkrs(gw, 1280), kbof(sg) * 128, 0);
        issue(IC<1>{});
        STAMP(1);
        const u32 xb0 = XO + (32 * wr + l15) * 128 + ((g4 ^ (l15 & 6)) * 16), xb1 = xb0 ^ 64;
        const u32 xc0 = xb0 + SL[0], xc1 = xb1 + SL[0];
        const u32 qsB = wr + 6 * rot, qsA = wr + 4 * rot, qbase = WS_WQ + hp * 102400, qm = (3 - wr) * 1024;
        auto qissue = [&](auto I) {
            constexpr int i = decltype(I)::v;
            u32 p;
            if constexpr (i < 15) { p = qsB + 4 * i; p = p >= 60 ? p - 60 : p; }
            else { p = qsA + 4 * (i - 15); p = (p >= 40 ? p - 40 : p) + 60; }
            dmac<(96 - 4 * i) * 1024>(l16, rws, qbase + p * 1024, qm);
        };
        bf16x8 xf[10][2][2];
        u32 tag = 0, pv = 0;
        sfor<0, 10>([&](auto SC) {
            constexpr int s = decltype(SC)::v;
            wait_lgkm<0>();
            if constexpr (s == 0) wait_vm<11>();
            else if constexpr (s == 1) tag = wait_rfl<9>(ev) + TAGC;
            else if constexpr (s == 3) wait_vm<10>();
            else if constexpr (s == 8) { if (!wait_eq<9>(pv, tag)) spin(WS_FQ + wr * 256 + lane * 4, rws, tag); }
            else if constexpr (s == 9) wait_vm<8>();
            else wait_vm<9>();
            bar();
            STAMP(2 + s);
            constexpr u32 xo = s % 3 ? SL[s % 3] : 0;
            const u32 a0 = s % 3 ? xb0 : xc0, a1 = s % 3 ? xb1 : xc1;
            xf[s][0][0] = asbf8(lds128(a0 + xo)); xf[s][0][1] = asbf8(lds128(a0 + xo + 2048));
            xf[s][1][0] = asbf8(lds128(a1 + xo)); xf[s][1][1] = asbf8(lds128(a1 + xo + 2048));
            if constexpr (s == 7) pv = ldflag(WS_FQ + wr * 256 + lane * 4, rws);
            if constexpr (s < 8) issue(IC<s + 2>{});
            if constexpr (s == 8) sfor<0, 8>(qissue);
            if constexpr (s == 9) sfor<8, 18>(qissue);
            if constexpr (s == 2) {
                const bool gv = sg < 3 && gj < 10;
                float r = 0.f;
                if (gv) {
                    const u32x4 g0 = lds128(L_GW + g4 * 16), g1 = lds128(L_GW + 64 + g4 * 16);
                    float ga = 0.f, gb = 0.f;
                    auto gdot = [&](auto Z) {
                        constexpr int z = decltype(Z)::v;
                        const u32x4 a = __builtin_bit_cast(u32x4, xf[z][0][0]), c = __builtin_bit_cast(u32x4, xf[z][0][1]);
                        const u32x4 d = __builtin_bit_cast(u32x4, xf[z][1][0]), e = __builtin_bit_cast(u32x4, xf[z][1][1]);
#pragma unroll
                        for (int i = 0; i < 4; ++i) { ga = dot2(a[i], g0[i], ga); gb = dot2(c[i], g0[i], gb); ga = dot2(d[i], g1[i], ga); gb = dot2(e[i], g1[i], gb); }
                    };
                    if (sg == 0) gdot(IC<0>{}); else if (sg == 1) gdot(IC<1>{}); else gdot(IC<2>{});
                    auto s1 = __builtin_amdgcn_permlane16_swap(f2u(ga), f2u(gb), false, false);
                    const u32 h = f2u(u2f(s1[0]) + u2f(s1[1]));
                    auto s2 = __builtin_amdgcn_permlane32_swap(h, h, false, false);
                    r = u2f(s2[0]) + u2f(s2[1]);
                }
                __builtin_amdgcn_raw_buffer_store_b32(f2u(r), wsr, (gv && lane < 32) ? WS_G + gj * 8192 + (rb * 128 + 32 * wr + lane) * 4 : OOB, 0, 16);
                cfence();
            }
        });
        const u32 hq = L_HQ + wr * 4;
        wait_vm<8>();
        ctl_set(hq, 1);
        ctl_wait(L_HQ, 1);
        STAMP(12);
        constexpr int PD = 4;
        bf16x8 wf[PD + 1];
        f32x4 qa[2][5];
        u32x4 snap;
        const u32 lhi = l16 + 61440;
        auto loadw = [&](auto NC) {
            constexpr int n = decltype(NC)::v;
            if constexpr (n == 36 || n == 56 || n == 76) snap = ctl_get(L_HQ);
            if constexpr (n == 40 || n == 60 || n == 80) { if (!ctl_ge(snap, n / 20)) ctl_wait(L_HQ, n / 20); cfence(); }
            wf[n % (PD + 1)] = asbf8(n < 40 ? lds128(lhi + (39 - n) * 1024) : lds128(l16 + (99 - n) * 1024));
        };
        sfor<0, PD>(loadw);
        sfor<0, 100>([&](auto NC) {
            constexpr int n = decltype(NC)::v;
            constexpr int st = n < 60 ? n / 6 : (n - 60) / 4, ks = n < 60 ? (n % 6) / 3 : ((n - 60) % 4) / 2, a = n < 60 ? n % 3 : 3 + n % 2;
            constexpr bool first = n < 3 || n == 60 || n == 61;
            const bf16x8 w = wf[n % (PD + 1)];
            sched0();
            mfma16a<first>(qa[0][a], w, xf[st][ks][0]);
            sched0();
            if constexpr (n + PD < 100) loadw(IC<n + PD>{});
            sched0();
            mfma16a<first>(qa[1][a], w, xf[st][ks][1]);
            sched0();
            if constexpr (n < 21 && n % 3 == 0) qissue(IC<18 + n / 3>{});
            if constexpr (n == 20) { wait_vm<10>(); ctl_set(hq, 2); }
            if constexpr (n == 40) { wait_vm<5>(); ctl_set(hq, 3); }
            if constexpr (n >= 61 && n < 64) {
                constexpr int z = n - 61;
                const f32x4 u = z < 2 ? qa[z & 1][0] : qa[0][2], v = z < 2 ? qa[z & 1][1] : qa[1][2];
                sts128(L_QB + wr * 3072 + z * 1024 + l16, u32x4{pk2(u[0], u[1]), pk2(u[2], u[3]), pk2(v[0], v[1]), pk2(v[2], v[3])});
            }
            if constexpr (n == 64) { wait_vm<0>(); ctl_set(hq, 4); STAMP(13); }
            if constexpr (n == 80) ctl_set(hq, 5);
        });
        ctl_set(hq, 6);
        mfma_pad();
        STAMP(14);
        u32x4 qf0, qf1; u32x2 qr0, qr1;
        {
            auto cvt = [&](int a, u32x4& o) {
                auto s0 = __builtin_amdgcn_permlane16_swap(pk2(qa[0][a][0] * QSCALE, qa[0][a][1] * QSCALE), pk2(qa[1][a][0] * QSCALE, qa[1][a][1] * QSCALE), false, false);
                auto s1 = __builtin_amdgcn_permlane16_swap(pk2(qa[0][a][2] * QSCALE, qa[0][a][3] * QSCALE), pk2(qa[1][a][2] * QSCALE, qa[1][a][3] * QSCALE), false, false);
                o = u32x4{s0[0], s1[0], s0[1], s1[1]};
            };
            u32x4 r;
            cvt(3, qf0); cvt(4, qf1); cvt(2, r);
            qr0 = hf ? u32x2{0, 0} : u32x2{r[0], r[1]};
            qr1 = hf ? u32x2{0, 0} : u32x2{r[2], r[3]};
        }
        const u32 kofs = L_K + wr * 2560 + (2 * (g4 & 1) + (lane & 1)) * 320 + ((lane >> 1) & 1) * 8;
        u32 ka = kofs + (8 * hf + (l15 >> 2)) * 16, kr = kofs + (16 + (l15 >> 2)) * 16;
        u32 va[2][2];
#pragma unroll
        for (int p = 0; p < 2; ++p)
#pragma unroll
            for (int m = 0; m < 2; ++m) {
                const u32 bi = wr + p;
                va[p][m] = L_V + (bi >> 1) * 5136 + (l31 < 20 ? (bi & 1) * 2560 + (2 * m + hf) * 320 + l31 * 16 : 5120);
            }
        const u32 vstep = l31 < 20 ? 1280 : 0;
        const int qq = (int)l31 - 4 * (int)hf;
        u32 ob = WS_O + (rb * 8 + 2 * wr + (l31 >> 4)) * 40960 + 20480;
        f32x16 zero;
#pragma unroll
        for (int i = 0; i < 16; ++i) zero[i] = 0.f;
        ctl_wait(L_MC, 3);
        STAMP(15);
#pragma nounroll
        for (int hh = 0; hh < 2; ++hh) {
            const bf16x8 qf = asbf8(hh ? qf1 : qf0);
            const bf16x4 qr = __builtin_bit_cast(bf16x4, hh ? qr1 : qr0);
            auto qk = [&](u32 o) { return mfma32k8(ldtr(kr + o), qr, mfma32(ldtr2(ka + o, ka + o + 64), qf, zero)); };
            f32x16 o = zero;
            f32x16 S = qk(2560), Sn, S8;
            sfor<1, 9>([&](auto I) {
                constexpr int i = decltype(I)::v & 7;
                constexpr u32 vo_ = (i >> 1) * 5136;
                const bf16x8 v0 = asbf8(lds128(va[i & 1][0] + vo_)), v1 = asbf8(lds128(va[i & 1][1] + vo_));
                bf16x8 v2, v3;
                if constexpr (i == 0) { v2 = asbf8(lds128(va[0][0] + 4 * 5136)); v3 = asbf8(lds128(va[0][1] + 4 * 5136)); }
                if constexpr (i != 0) Sn = qk(((i + 1) & 7) * 2560);
                if constexpr (i == 6) S8 = qk(8 * 2560);
                if constexpr (i == 0) {
#pragma unroll
                    for (int r = 0; r < 16; ++r) S[r] = ((r & 3) + 8 * (r >> 2) <= qq) ? S8[r] : S[r];
                }
                u32 p[8];
#pragma unroll
                for (int j = 0; j < 8; ++j) p[j] = pk2(__builtin_amdgcn_exp2f(S[2 * j]), __builtin_amdgcn_exp2f(S[2 * j + 1]));
                if constexpr (i == 0) {
                    u32 pd[8];
#pragma unroll
                    for (int j = 0; j < 8; ++j) {
                        const int k0 = ((2 * j) & 3) + 8 * ((2 * j) >> 2);
                        const u32 mk = (k0 <= qq ? 0xFFFFu : 0u) | (k0 + 1 <= qq ? 0xFFFF0000u : 0u);
                        pd[j] = p[j] & mk;
                        p[j] = p[j] & ~mk;
                    }
                    o = mfma32(v2, asbf8(u32x4{pd[0], pd[1], pd[2], pd[3]}), o);
                    o = mfma32(v3, asbf8(u32x4{pd[4], pd[5], pd[6], pd[7]}), o);
                }
                o = mfma32(v0, asbf8(u32x4{p[0], p[1], p[2], p[3]}), o);
                o = mfma32(v1, asbf8(u32x4{p[4], p[5], p[6], p[7]}), o);
                if constexpr (i != 0) S = Sn;
            });
            float am = 0.f;
#pragma unroll
            for (int r = 0; r < 12; r += 2) am = fmaxf(fmaxf(am, fabsf(o[r])), fabsf(o[r + 1]));
            const bool fine = o[12] > 0x1p-30f && o[12] < 0x1p30f && am < 0x1p120f;
            if (__builtin_amdgcn_ballot_w64(!fine) != 0ull) {
                const int i0 = rb >= 2 ? 0 : 8 - 4 * (int)rb - (int)wr;
                auto sc = [&](int i) {
                    f32x16 s = qk(i * 2560);
#pragma unroll
                    for (int r = 0; r < 16; ++r) {
                        const bool le = (r & 3) + 8 * (r >> 2) <= qq;
                        const bool ok = i == 0 ? !le : (i == 8 ? le : true);
                        s[r] = ok ? s[r] : -__builtin_inff();
                    }
                    return s;
                };
                float m = -__builtin_inff();
#pragma nounroll
                for (int i = i0; i < 9; ++i) {
                    const f32x16 s = sc(i);
#pragma unroll
                    for (int r = 0; r < 16; ++r) m = fmaxf(m, s[r]);
                }
                auto sw = __builtin_amdgcn_permlane32_swap(f2u(m), f2u(m), false, false);
                m = fmaxf(u2f(sw[0]), u2f(sw[1]));
                o = zero;
#pragma nounroll
                for (int i = i0; i < 9; ++i) {
                    const f32x16 s = sc(i);
                    u32 p[8];
#pragma unroll
                    for (int j = 0; j < 8; ++j) p[j] = pk2(__builtin_amdgcn_exp2f(s[2 * j] - m), __builtin_amdgcn_exp2f(s[2 * j + 1] - m));
                    const u32 vo_ = (i >> 1) * 5136;
                    o = mfma32(asbf8(lds128(((i & 1) ? va[1][0] : va[0][0]) + vo_)), asbf8(u32x4{p[0], p[1], p[2], p[3]}), o);
                    o = mfma32(asbf8(lds128(((i & 1) ? va[1][1] : va[0][1]) + vo_)), asbf8(u32x4{p[4], p[5], p[6], p[7]}), o);
                }
            }
            const float rs = __builtin_amdgcn_rcpf(o[12]);
            u32x4 v = {pk2m(o[0] * rs, o[1] * rs), pk2m(o[2] * rs, o[3] * rs), pk2m(o[4] * rs, o[5] * rs), pk2m(o[6] * rs, o[7] * rs)};
            __builtin_amdgcn_raw_buffer_store_b128(v, wsr, ob + (2 * hp + hf) * 256 + l15 * 16, 0, 16);
            u32x2 v2 = {pk2m(o[8] * rs, o[9] * rs), pk2m(o[10] * rs, o[11] * rs)};
            __builtin_amdgcn_raw_buffer_store_b64(v2, wsr, hf ? OOB : ob + 8192 + hp * 128 + l15 * 8, 0, 16);
            ka += 1280; kr += 1280; ob += 10240;
            va[0][0] += vstep; va[0][1] += vstep; va[1][0] += vstep; va[1][1] += vstep;
        }
        STAMP(17);
    } else {
        const u32 wm = wr >> 1, wn = wr & 1, mc = L_MC + wr * 4;
#pragma nounroll
        for (u32 j = 0; j < 4; ++j) {
            const u32 p = (ty ? 2048 : 0) + j * 512 + (rb * 8 + hg) * 4 + wr;
            u32 dst = OOB;
            if (p < 3200) {
                const u32 mat = p / 800, q = p - mat * 800, oct = q / 10, cl = q - oct * 10;
                const u32 kx = oct >> 2, go = oct & 3, G = kx / 5, e = kx - G * 5;
                u32 r0 = 8 * oct, step = 4;
                if (mat == 2) {
                    r0 = 20 * (G >> 1) + 320 * (G & 1) + (e < 4 ? 80 * e + 40 * (go >> 1) + 4 * (go & 1) : 40 * go + 16);
                    step = e < 4 ? 8 : 160;
                } else if (mat == 3) {
                    r0 = 160 * G + (e < 4 ? 40 * e + 4 * go : 20 * go + 16);
                    step = e < 4 ? 20 : 80;
                }
                const u32 ld = mat < 2 ? 3840 : 1280;
                const u16* src = mat == 0 ? wa : mat == 1 ? wb : mat == 2 ? woa : wob;
                dma<L_SCR>((((lane >> 3) & 3) + step * hf) * ld + (lane & 7) * 16, mkrs(src, 640 * ld), r0 * ld + cl * 128, (wr * 4 + j) * 1024);
                const u32 n = 64 * cl + lane, h = (n * 205) >> 12, d = n - 20 * h;
                if (mat < 2) {
                    const u32 hb = h & 1;
                    const u32 fr = d < 16 ? (mat ? 3 * kx : 60 + 2 * kx) + hb : 3 * kx + 2;
                    const u32 pos = d < 16 ? d : 8 * mat + 4 * hb + d - 16;
                    dst = WS_WQ + (h >> 1) * 102400 + fr * 1024 + (go * 16 + pos) * 16;
                } else dst = WS_WO + ((mat - 2) * 8 + (h >> 2)) * 102400 + (kx * 5 + (d >> 2)) * 1024 + (go * 16 + 4 * (h & 3) + (d & 3)) * 16;
            }
            sts32(L_DST + (wr * 4 + j) * 256 + lane * 4, dst);
        }
        const u32 ev = ldflag(0, rws);
#pragma unroll
        for (int i = 0; i < 5; ++i)
            st128(u32x4{~0u, ~0u, ~0u, ~0u}, l16, rws, WS_O + b * 20480 + wr * 5120 + i * 1024);
        cfence();
        if (lane < 2) sts32(L_HQ + lane * 16 + wr * 4, 0);
        u32 wb_[2][3], xb_[2][2];
        {
            const u32 q3 = lane & 3, wrow = (g4 & 1) * WREG + (8 * (g4 >> 1) + (l15 >> 2)) * 320;
            wb_[1][0] = wrow + (ty ? 80 : 160) * wn + q3 * 8;
            wb_[1][1] = wb_[1][0] + (ty ? 160 : 80);
            wb_[1][2] = wrow + 32 + (ty ? 80 * wn + ((q3 & 2) ? 0 : 160) + 40 * (q3 & 1) : 160 * wn + 40 * q3);
            xb_[1][0] = XO + (64 * wm + 16 * (l15 >> 3) + (l15 & 7)) * 128 + ((g4 ^ (l15 & 6)) * 16);
            xb_[1][1] = xb_[1][0] ^ 64;
#pragma unroll
            for (int i = 0; i < 3; ++i) wb_[0][i] = opaque_v(wb_[1][i] + SL[0]);
#pragma unroll
            for (int i = 0; i < 2; ++i) xb_[0][i] = opaque_v(xb_[1][i] + SL[0]);
        }
        const u32 jsrc = opaque_v(L_SCR + wr * 4096 + (l15 >> 2) * 128 + (4 * g4 + (lane & 3)) * 8), jdst = opaque_v(L_DST + wr * 1024 + lane * 4);
        auto wad = [&](int s, int m) { return wb_[s % 3 != 0][(m % 5) >> 1] + (s % 3 ? SL[s % 3] : 0) + (m / 5) * (2 * WREG) + ((m % 5) & 1) * 40; };
        auto xad = [&](int s, int ks, int t) { return xb_[s % 3 != 0][ks] + (s % 3 ? SL[s % 3] : 0) + (t >> 1) * 4096 + (t & 1) * 1024; };
        f32x4 acc[4][5];
        u32 tag = 0;
        {
            bf16x8 wf[5], xa[2][4];
            bf16x4 ja, jb, jc, jd;
            u32 d0, d1;
            STAMP(1);
            wait_lgkm<0>();
            bar();
            STAMP(2);
            sfor<0, 4>([&](auto I) { xa[0][I.v] = asbf8(lds128(xad(0, 0, I.v))); });
            wf[0] = ldtr2(wad(0, 0), wad(0, 0) + 1280);
            wf[1] = ldtr2(wad(0, 1), wad(0, 1) + 1280);
            sfor<0, 100>([&](auto NC) {
                constexpr int s = decltype(NC)::v / 10, n = decltype(NC)::v % 10, ks = n / 5, c = n % 5;
                constexpr int s2 = n >= 8 ? s + 1 : s, m = (n + 2) % 10;
                constexpr bool more = s2 < 10, first = s == 0 && ks == 0;
                bf16x4 lo, hi;
                bf16x8 t0, t1;
                auto mm = [&](auto I) { sched0(); mfma16a<first>(acc[I.v][c], xa[ks][I.v], wf[c]); sched0(); };
                mm(IC<0>{});
                if constexpr (n == 8 && more) {
                    wait_lgkm<0>();
                    if constexpr (s == 3) wait_vm<0>();
                    bar();
                    sched0();
                }
                if constexpr (more) lo = ldtr(wad(s2, m));
                mm(IC<1>{});
                if constexpr (more) hi = ldtr(wad(s2, m) + 1280);
                mm(IC<2>{});
                if constexpr (n >= 8 && more) t0 = asbf8(lds128(xad(s2, 0, 2 * (n - 8))));
                if constexpr (n < 4) xa[1][n] = asbf8(lds128(xad(s, 1, n)));
                if constexpr (s == 0 && n == 4) tag = wait_rfl<5>(ev) + TAGC;
                if constexpr (s < 2 && n == 4) ja = ldtr(jsrc + s * 2048);
                if constexpr (s < 2 && n == 5) d0 = lds32(jdst + s * 512);
                if constexpr (s < 2 && n == 6) jc = ldtr(jsrc + s * 2048 + 1024);
                if constexpr (s < 2 && n == 7) __builtin_amdgcn_raw_buffer_store_b128(__builtin_bit_cast(u32x4, cat8(ja, jb)), wsr, d0, 0, 16);
                mm(IC<3>{});
                if constexpr (n >= 8 && more) { t1 = asbf8(lds128(xad(s2, 0, 2 * (n - 8) + 1))); xa[0][2 * (n - 8)] = t0; xa[0][2 * (n - 8) + 1] = t1; }
                if constexpr (s < 2 && n == 4) jb = ldtr(jsrc + s * 2048 + 512);
                if constexpr (s < 2 && n == 5) d1 = lds32(jdst + s * 512 + 256);
                if constexpr (s < 2 && n == 6) jd = ldtr(jsrc + s * 2048 + 1536);
                if constexpr (s < 2 && n == 8) __builtin_amdgcn_raw_buffer_store_b128(__builtin_bit_cast(u32x4, cat8(jc, jd)), wsr, d1, 0, 16);
                if constexpr (s == 3 && n == 8) { if (wr == 0) stflag(tag, WS_FQ + b * 4, rws); }
                if constexpr (s == 7 && n == 8) { if (b == 0 && wr == 0) stflag(tag, WS_E, rws); }
                if constexpr (more) wf[m % 5] = cat8(lo, hi);
                sched0();
            });
        }
        mfma_pad();
        STAMP(12);
        auto pk8 = [&](int u, int c) {
            return u32x4{pk2(acc[2 * u][c][0], acc[2 * u][c][1]), pk2(acc[2 * u][c][2], acc[2 * u][c][3]),
                         pk2(acc[2 * u + 1][c][0], acc[2 * u + 1][c][1]), pk2(acc[2 * u + 1][c][2], acc[2 * u + 1][c][3])};
        };
        auto ph8 = [&](int u, int c) {
            return u32x4{phi2(acc[2 * u][c][0], acc[2 * u][c][1]), phi2(acc[2 * u][c][2], acc[2 * u][c][3]),
                         phi2(acc[2 * u + 1][c][0], acc[2 * u + 1][c][1]), phi2(acc[2 * u + 1][c][2], acc[2 * u + 1][c][3])};
        };
        if (!ty) {
            ctl_set(mc, 2);
            const u32 so = WS_T + ((4 * hg + 2 * wn) * 64 + 4 * rb + 2 * wm) * 2560;
            const u32 vm = g4 * 320 + l15 * 16, vr = g4 * 320 + 256 + (l15 & 3) * 16 + (l15 >> 3) * 163840 + ((l15 >> 2) & 1) * 1280;
#pragma unroll
            for (int c = 0; c < 5; ++c)
#pragma unroll
                for (int u = 0; u < 2; ++u)
                    st128(pk8(u, c), (c < 4 ? vm + (c & 1) * 1280 : vr) + u * 2560, rws, so + (c == 2 || c == 3 ? 163840 : 0));
            cfence();
        }
        const u32 r64 = xcd * 4 + (idx >> 3), c80 = idx & 7;
        const u32 woff = WS_WO + c80 * 102400;
        bf16x8 W[25];
#pragma unroll
        for (int i = 0; i < 25; ++i) W[i] = asbf8(__builtin_amdgcn_raw_buffer_load_b128(wsr, l16 + (i & 3) * 1024, woff + 819200 + wn * 25600 + (i >> 2) * 4096, 16));
        u32 gp[5];
#pragma unroll
        for (int i = 0; i < 5; ++i) gp[i] = __builtin_amdgcn_raw_buffer_load_b32(wsr, hf * 40960 + (l31 ^ (16 * wn)) * 4, WS_G + i * 8192 + (r64 * 64 + wm * 32) * 4, 16);
        cfence();
        if (!ty) {
            wait_vm<30>();
            stflag(tag, WS_FT + ((hg * 2 + wn) * 32 + 2 * rb + wm) * 4, rws);
        } else {
            f32x4 Q0 = {0, 0, 0, 0}, Q1 = Q0, X0 = Q0, X1 = Q0, Y0 = Q0, Y1 = Q0, U = Q0;
#pragma unroll
            for (int u = 0; u < 2; ++u) {
                const bf16x8 v0 = asbf8(pk8(u, 2)), v1 = asbf8(pk8(u, 3));
                const u32x4 rr = pk8(u, 4);
                const bf16x8 rbv = asbf8(l15 < 8 ? rr : u32x4{ONES, ONES, ONES, ONES});
                const bf16x8 k0 = asbf8(ph8(u, 0));
                Q0 = mfma16(k0, v0, Q0); X0 = mfma16(k0, rbv, X0);
                const bf16x8 k1 = asbf8(ph8(u, 1));
                Q1 = mfma16(k1, v1, Q1); X1 = mfma16(k1, rbv, X1);
                const bf16x8 ra = asbf8(ph8(u, 4));
                Y0 = mfma16(ra, v0, Y0); Y1 = mfma16(ra, v1, Y1); U = mfma16(ra, rbv, U);
            }
            f32x4 X = (l15 & 4) ? X1 : X0, Z;
#pragma unroll
            for (int r = 0; r < 4; ++r) Z[r] = u2f(__builtin_amdgcn_permlane32_swap(f2u((g4 & 1) ? Y1[r] : Y0[r]), f2u(U[r]), false, false)[1]);
            sts128(L_SCR + wr * 2048 + l16, __builtin_bit_cast(u32x4, wm ? Q0 : X));
            sts128(L_SCR + wr * 2048 + 1024 + l16, __builtin_bit_cast(u32x4, wm ? Q1 : Z));
            ctl_set(mc, 1);
            ctl_wait1(L_MC + (wr ^ 2) * 4, 1);
            const f32x4 a = (wm ? X : Q0) + __builtin_bit_cast(f32x4, lds128(L_SCR + (wr ^ 2) * 2048 + l16));
            const f32x4 c = (wm ? Z : Q1) + __builtin_bit_cast(f32x4, lds128(L_SCR + (wr ^ 2) * 2048 + 1024 + l16));
            ctl_set(mc, 2);
            st128(u32x4{pk2(a[0], a[1]), pk2(a[2], a[3]), pk2(c[0], c[1]), pk2(c[2], c[3])}, l16, rws, WS_KVP + ((2 * hg + wn) * 16 + rb) * 2048 + wm * 1024);
            wait_vm<0>();
            stflag(tag, WS_FKV + (((2 * hg + wn) * 16 + rb) * 2 + wm) * 4, rws);
        }
        STAMP(13);
        {
            const int g0 = 2 * (int)rb - 4 + 3 * (int)wn;
            int gl = g0 + (int)(lane < 2 ? lane : 2);
            gl = gl < 0 ? 0 : gl;
            spin(WS_FT + ((wm * 8 + hg) * 32 + gl) * 4, rws, tag);
            ctl_wait(L_HQ, 2);
            ctl_wait(L_MC, 2);
            STAMP(14);
#pragma unroll
            for (int i = 0; i < 3; ++i) {
                const u32 mb = wm ? L_V + (3 * wn + i) * 5136 : L_K + (3 * wn + i) * 5120;
                if (g0 + i >= 0) {
                    const u32 so = WS_T + ((wm * 16 + hp) * 32 + g0 + i) * 5120;
                    dmac<0, 0>(l16, rws, so, mb); dmac<0, 1024>(l16, rws, so, mb); dmac<0, 2048>(l16, rws, so, mb); dmac<0, 3072>(l16, rws, so, mb);
                    dmac<4096, 0>(l16, rws, so + 4096, mb);
                } else {
#pragma unroll
                    for (int k = 0; k < 5; ++k) sts128(mb + k * 1024 + l16, u32x4{0, 0, 0, 0});
                }
            }
            if (wm && lane < 12) sts32(L_V + (3 * wn + (lane >> 2)) * 5136 + 5120 + (lane & 3) * 4, g0 + (int)(lane >> 2) >= 0 ? ONES : 0);
        }
        {
            const u32 fo = WS_FKV + ((hp * 16 + l15) * 2 + wm) * 4;
            const u32 pv = ldflag(fo, rws);
            ctl_wait1(L_HQ + wr * 4, 4);
            u32x4 P[3];
#pragma unroll
            for (int z = 0; z < 3; ++z) {
                const u32x4 q = lds128(L_QB + wr * 3072 + z * 1024 + (z < 2 ? l16 : (l16 | 512)));
#pragma unroll
                for (int i = 0; i < 4; ++i) P[z][i] = phi2(blo(q[i]), bhi(q[i]));
            }
            const bool ok = wait_eq<0>(pv, tag);
            ctl_set(mc, 3);
            STAMP(15);
            if (!ok) spin(fo, rws, tag);
            u32x4 v[8];
#pragma unroll
            for (int i = 0; i < 8; ++i) v[i] = __builtin_amdgcn_raw_buffer_load_b128(wsr, hf * 2048 + l31 * 16, WS_KVP + hp * 32768 + i * 4096 + wr * 512, 16);
            f32x2 s[4];
#pragma unroll
            for (int k = 0; k < 4; ++k) {
                s[k] = f32x2{blo(v[0][k]), bhi(v[0][k])};
#pragma unroll
                for (int i = 1; i < 8; ++i) s[k] += f32x2{blo(v[i][k]), bhi(v[i][k])};
            }
            float t[4];
#pragma unroll
            for (int k = 0; k < 4; ++k) {
                auto sw = __builtin_amdgcn_permlane32_swap(f2u(s[k >> 1][k & 1]), f2u(s[2 + (k >> 1)][k & 1]), false, false);
                t[k] = u2f(sw[0]) + u2f(sw[1]);
            }
            sts64(L_REC + wr * 512 + l31 * 16 + hf * 8, u32x2{pk2(t[0], t[1]), pk2(t[2], t[3])});
            ctl_set(mc, 4);
            ctl_wait(L_MC, 4);
            STAMP(16);
            const u32x4 m = lds128(L_REC + l16), n = lds128(L_REC + 1024 + l16);
            ctl_set(mc, 5);
            const bool m1 = l15 & 4;
            const bf16x8 a0 = asbf8(u32x4{m[0], m[1], g4 == 0 ? n[2] : 0, g4 == 0 ? n[3] : 0});
            const bf16x8 a1 = asbf8(u32x4{m[2], m[3], g4 == 1 ? n[2] : 0, g4 == 1 ? n[3] : 0});
            const bf16x8 x0 = asbf8(u32x4{m1 ? 0 : n[0], m1 ? 0 : n[1], (g4 == 2 && !m1) ? n[2] : 0, (g4 == 2 && !m1) ? n[3] : 0});
            const bf16x8 x1 = asbf8(u32x4{m1 ? n[0] : 0, m1 ? n[1] : 0, (g4 == 3 && m1) ? n[2] : 0, (g4 == 3 && m1) ? n[3] : 0});
#pragma unroll
            for (int t2 = 0; t2 < 2; ++t2) {
                const bf16x8 b0 = asbf8(u32x4{P[t2][0], P[t2][1], P[2][2 * t2], P[2][2 * t2 + 1]});
                const bf16x8 b1 = asbf8(u32x4{P[t2][2], P[t2][3], P[2][2 * t2], P[2][2 * t2 + 1]});
                const f32x4 z = {0, 0, 0, 0};
                const f32x4 xx = mfma16(x1, b1, mfma16(x0, b0, z));
                const f32x4 n0 = mfma16(a0, b0, z), n1 = mfma16(a1, b1, z);
                const u32 rc = f2u(__builtin_amdgcn_rcpf(xx[0] + 1e-6f));
                const u32 rs = __builtin_amdgcn_permlane32_swap(rc, rc, false, false)[1];
                auto sw = __builtin_amdgcn_permlane16_swap(rs, rs, false, false);
                const float r0 = u2f(sw[0]), r1 = u2f(sw[1]), rr = u2f(rs);
                const u32 so = WS_O + (rb * 8 + 2 * wr + t2) * 40960;
                st128(u32x4{pk2m(n0[0] * r0, n0[1] * r0), pk2m(n0[2] * r0, n0[3] * r0), pk2m(n1[0] * r1, n1[1] * r1), pk2m(n1[2] * r1, n1[3] * r1)}, l16, rws, so + hp * 1024);
                __builtin_amdgcn_raw_buffer_store_b64(u32x2{pk2m(xx[0] * rr, xx[1] * rr), pk2m(xx[2] * rr, xx[3] * rr)}, wsr, hf ? OOB : lane * 8, so + 16384 + hp * 256, 16);
            }
            cfence();
        }
        STAMP(17);
        {
            const u32 ot0 = WS_O + (r64 * 4 + wr) * 40960, ot1 = WS_O + (r64 * 4 + (wr ^ 1)) * 40960;
            const u32 vr = lane * 8;
            auto wdma = [&](u32 src, u32 buf) {
                const u32 so = woff + src + wm * 25600 + wn * 12288, mb = buf + wr * 12288;
                sfor<0, 3>([&](auto I) {
                    dmac<I.v * 4096, 0>(l16, rws, so + I.v * 4096, mb); dmac<I.v * 4096, 1024>(l16, rws, so + I.v * 4096, mb);
                    dmac<I.v * 4096, 2048>(l16, rws, so + I.v * 4096, mb); dmac<I.v * 4096, 3072>(l16, rws, so + I.v * 4096, mb);
                });
            };
            auto wlast = [&](u32 src) { return asbf8(__builtin_amdgcn_raw_buffer_load_b128(wsr, l16, woff + src + wn * 25600 + 24576, 16)); };
            auto wget = [&](u32 buf) {
                const u32 a = buf + wn * 24576 + l16;
#pragma unroll
                for (int i = 0; i < 24; ++i) W[i] = asbf8(lds128(a + i * 1024));
            };
            auto oload = [&](OF& o, u32 om, u32 orr) {
#pragma unroll
                for (int e = 0; e < 4; ++e) {
                    o.m[0][e] = __builtin_amdgcn_raw_buffer_load_b128(wsr, l16 + e * 1024, ot0 + om, 16);
                    o.m[1][e] = __builtin_amdgcn_raw_buffer_load_b128(wsr, l16 + e * 1024, ot1 + om, 16);
                }
#pragma unroll
                for (int h = 0; h < 2; ++h) {
                    o.r[0][h] = __builtin_amdgcn_raw_buffer_load_b64(wsr, vr + h * 512, ot0 + orr, 16);
                    o.r[1][h] = __builtin_amdgcn_raw_buffer_load_b64(wsr, vr + h * 512, ot1 + orr, 16);
                }
                cfence();
            };
            auto pass = [&](OF& o, f32x4 (&ac)[2][5], u32 om, u32 orr) {
                u32 nx = 0, bad = 31;
#pragma nounroll
                for (u32 it = 0; it < (1u << 17); ++it) {
                    const u32 chk = bad;
                    bad = 32;
                    sfor<0, 4>([&](auto E) {
                        if (chk & (1u << E.v)) {
                            if (marked(mx4(mx4(0, o.m[0][E.v]), o.m[1][E.v]))) {
                                rl128<E.v * 1024>(o.m[0][E.v], l16, rws, ot0 + om); rl128<E.v * 1024>(o.m[1][E.v], l16, rws, ot1 + om);
                                bad |= 1u << E.v;
                            }
                        }
                    });
                    if (marked(umax(umax(umax(o.r[0][0][0], o.r[0][0][1]), umax(o.r[0][1][0], o.r[0][1][1])), umax(umax(o.r[1][0][0], o.r[1][0][1]), umax(o.r[1][1][0], o.r[1][1][1]))))) {
                        rl64<0>(o.r[0][0], vr, rws, ot0 + orr); rl64<0>(o.r[1][0], vr, rws, ot1 + orr);
                        rl64<512>(o.r[0][1], vr, rws, ot0 + orr); rl64<512>(o.r[1][1], vr, rws, ot1 + orr);
                        bad |= 16;
                    }
                    const u32 lim = __builtin_ctz(bad);
                    sfor<0, 5>([&](auto E) {
                        constexpr int e = decltype(E)::v;
                        if (e >= nx && e < lim) {
#pragma unroll
                            for (int t = 0; t < 2; ++t) {
                                const bf16x8 f = asbf8(e < 4 ? o.m[t][e & 3] : u32x4{o.r[t][0][0], o.r[t][0][1], o.r[t][1][0], o.r[t][1][1]});
#pragma unroll
                                for (int c = 0; c < 5; ++c) ac[t][c] = mfma16(W[e * 5 + c], f, ac[t][c]);
                            }
                        }
                    });
                    nx = lim;
                    if (lim == 5) break;
                    of_wait(o);
                }
            };
            f32x4 accA[2][5], accB[2][5];
#pragma unroll
            for (int i = 0; i < 10; ++i) accA[i / 5][i % 5] = accB[i / 5][i % 5] = f32x4{0, 0, 0, 0};
            OF oa, ob;
            ctl_wait(L_MC, 5);
            ctl_wait(L_HQ, 6);
            wdma(819200 + 51200, 0);
            oload(oa, wn * 4096, 16384 + wn * 1024);
            bf16x8 wx = wlast(819200 + 51200);
            oload(ob, 8192 + wn * 4096, 16384 + 2048 + wn * 1024);
            float g0, g1;
            {
                const u32 h = f2u(((u2f(gp[0]) + u2f(gp[1])) + (u2f(gp[2]) + u2f(gp[3]))) + u2f(gp[4]));
                auto sw = __builtin_amdgcn_permlane32_swap(h, h, false, false);
                const u32 g = f2u(__builtin_amdgcn_rcpf(1.f + __builtin_amdgcn_exp2f(-(u2f(sw[0]) + u2f(sw[1])) * LOG2E)));
                auto s2 = __builtin_amdgcn_permlane16_swap(g, g, false, false);
                g0 = u2f(s2[0]); g1 = u2f(s2[1]);
            }
            of_tie(oa, W[24]);
            ctl_set(mc, 6);
            wdma(0, 49152);
            pass(oa, accB, wn * 4096, 16384 + wn * 1024);
            STAMP(18);
            W[24] = wx;
            wx = wlast(0);
            oload(oa, 20480 + wn * 4096, 28672 + wn * 1024);
            ctl_wait(L_MC, 6);
            wget(0);
            wait_vm<13>();
            ctl_set(mc, 7);
            of_tie(ob, W[24]);
            pass(ob, accB, 8192 + wn * 4096, 16384 + 2048 + wn * 1024);
            STAMP(19);
            W[24] = wx;
            of_tie(oa, W[24]);
            ctl_wait(L_MC, 7);
            wdma(51200, 0);
            wget(49152);
#pragma unroll
            for (int c = 0; c < 5; ++c) { accB[0][c] *= 1.f - g0; accB[1][c] *= 1.f - g1; }
            pass(oa, accA, 20480 + wn * 4096, 28672 + wn * 1024);
            STAMP(20);
            W[24] = wlast(51200);
            oload(ob, 30720 + wn * 4096, 38912 + wn * 1024);
            wait_vm<13>();
            ctl_set(mc, 8);
            ctl_wait(L_MC, 8);
            wget(0);
            of_tie(ob, W[24]);
            pass(ob, accA, 30720 + wn * 4096, 38912 + wn * 1024);
            STAMP(21);
#pragma unroll
            for (int c = 0; c < 5; ++c) sts128(49152 + wr * 5120 + c * 1024 + l16, __builtin_bit_cast(u32x4, accA[1][c] * g1 + accB[1][c]));
            ctl_set(mc, 9);
            ctl_wait1(L_MC + (wr ^ 1) * 4, 9);
            u32 r[10];
#pragma unroll
            for (int c = 0; c < 5; ++c) {
                const f32x4 y = (accA[0][c] * g0 + accB[0][c]) + __builtin_bit_cast(f32x4, lds128(49152 + (wr ^ 1) * 5120 + c * 1024 + l16));
                r[2 * c] = pk2(y[0], y[1]); r[2 * c + 1] = pk2(y[2], y[3]);
            }
            const __amdgpu_buffer_rsrc_t ors = __builtin_amdgcn_make_buffer_rsrc(out, 0, 2048 * 1280, 0x00020000);
            const u32 vo = l15 * 1280 + g4 * 40 + (r64 * 64 + wr * 16) * 1280 + c80 * 160;
            __builtin_amdgcn_raw_buffer_store_b128(u32x4{r[0], r[1], r[2], r[3]}, ors, vo, 0, 0);
            __builtin_amdgcn_raw_buffer_store_b128(u32x4{r[4], r[5], r[6], r[7]}, ors, vo + 16, 0, 0);
            __builtin_amdgcn_raw_buffer_store_b64(u32x2{r[8], r[9]}, ors, vo + 32, 0, 0);
        }
        STAMP(22);
    }
    asm volatile(".Lcode_hi:");
}

extern "C" void kernel_launch(void* const* d_in, const int* in_sizes, int n_in, void* d_out, int out_size, void* d_ws,
                              size_t ws_size, hipStream_t stream) {
    (void)in_sizes; (void)n_in; (void)out_size; (void)ws_size;
    (void)hipFuncSetAttribute((const void*)fused_kernel, hipFuncAttributeMaxDynamicSharedMemorySize, LDS_BYTES);
    fused_kernel<<<256, 512, LDS_BYTES, stream>>>((const u16*)d_in[0], (const u16*)d_in[1], (const u16*)d_in[2], (const u16*)d_in[4],
                                                  (const u16*)d_in[5], (const u16*)d_in[6], (u16*)d_out, (char*)d_ws);
}
```

```cpp
#include <hip/hip_runtime.h>
#include <stdint.h>
#pragma clang diagnostic ignored "-Wint-to-pointer-cast"

#define SPINS 1
#define PRIO 3
typedef unsigned short u16;
typedef unsigned int u32;
typedef unsigned long long u64;
typedef __attribute__((ext_vector_type(8))) __bf16 bf16x8;
typedef __attribute__((ext_vector_type(4))) __bf16 bf16x4;
typedef __attribute__((ext_vector_type(2))) __bf16 bf16x2;
typedef __attribute__((ext_vector_type(2))) float f32x2;
typedef __attribute__((ext_vector_type(4))) float f32x4;
typedef __attribute__((ext_vector_type(16))) float f32x16;
typedef __attribute__((ext_vector_type(4))) u32 u32x4;
typedef __attribute__((ext_vector_type(2))) u32 u32x2;
typedef __attribute__((ext_vector_type(4))) short s16x4;
#define AS3 __attribute__((address_space(3)))
#define DEVI __device__ __forceinline__

constexpr u32 WS_E   = 0;
constexpr u32 WS_FS  = 4096;
constexpr u32 WS_F2  = 12288;
constexpr u32 WS_FQ  = 8192;
constexpr u32 WS_FT  = 16384;
constexpr u32 WS_T   = 81920;
constexpr u32 WS_KVP = WS_T + 2 * 16 * 64 * 2560;
constexpr u32 WS_O   = WS_KVP + 16 * 16 * 2048;
constexpr u32 WS_WO  = WS_O + 128 * 40960;
constexpr u32 WS_WQ  = WS_WO + 16 * 102400;
constexpr u32 WS_END = WS_WQ + 16 * 102400;
constexpr u32 OOB = 0x80000000u;

constexpr u32 WREG  = 5152;
constexpr u32 XO    = 20608;
constexpr u32 SLOT  = 36992;
__device__ constexpr u32 SL[4] = {0, 3 * SLOT, 2 * SLOT, SLOT};
constexpr u32 L_SCR = 4 * SLOT;
constexpr u32 L_GW  = L_SCR + 8192;
constexpr u32 L_DUMP = 163328;
constexpr u32 L_REC = 0;
constexpr u32 L_XQ  = 2048;
constexpr u32 L_QT  = 3 * SLOT;
constexpr u32 L_K   = 101376;
constexpr u32 L_V   = 132096;
constexpr u32 L_RC  = 8192;
constexpr u32 L_HQ  = 162944, L_MC = L_HQ + 16, L_TAG = L_HQ + 32;
constexpr u32 LDS_BYTES = 163840;
static_assert(L_GW + 2048 <= L_HQ && L_DUMP >= L_TAG + 4 && L_DUMP + 256 <= LDS_BYTES && L_V + 6 * 5136 <= L_HQ && L_XQ + 6144 <= L_QT - 102400, "lds");

constexpr float LOG2E = 1.4426950408889634f;
constexpr float QSCALE = 0.22360679774997896f * 1.4426950408889634f;
constexpr u32 TAGC = 0x7F4A7C15u;
constexpr u32 ONES = 0x3F803F80u;

DEVI u32 pk2(float a, float b) { f32x2 v = {a, b}; bf16x2 r = __builtin_convertvector(v, bf16x2); return __builtin_bit_cast(u32, r); }
DEVI u32 f2u(float f) { return __builtin_bit_cast(u32, f); }
DEVI float u2f(u32 u) { return __builtin_bit_cast(float, u); }
DEVI float blo(u32 u) { return u2f(u << 16); }
DEVI float bhi(u32 u) { return u2f(u & 0xFFFF0000u); }
DEVI u32 umin(u32 a, u32 b) { return a < b ? a : b; }
DEVI u32 umax(u32 a, u32 b) { return a > b ? a : b; }
DEVI u32 pk2m(float a, float b) { return umin(pk2(a, b), 0xFFFFFFFEu); }
DEVI bf16x8 asbf8(u32x4 v) { return __builtin_bit_cast(bf16x8, v); }
DEVI f32x4 mfma16(bf16x8 a, bf16x8 b, f32x4 c) { return __builtin_amdgcn_mfma_f32_16x16x32_bf16(a, b, c, 0, 0, 0); }
template <bool FIRST = false> DEVI void mfma16a(f32x4& c, bf16x8 a, bf16x8 b) {
    if constexpr (FIRST) asm volatile("v_mfma_f32_16x16x32_bf16 %0, %1, %2, 0" : "=&v"(c) : "v"(a), "v"(b));
    else asm volatile("v_mfma_f32_16x16x32_bf16 %0, %1, %2, %0" : "+v"(c) : "v"(a), "v"(b));
}
template <bool FIRST> DEVI void mfma16b(f32x4& c, bf16x8 a, bf16x8 b) {
    if constexpr (FIRST) asm volatile("v_mfma_f32_16x16x32_bf16 %0, %1, %2, 0" : "+v"(c) : "v"(a), "v"(b));
    else asm volatile("v_mfma_f32_16x16x32_bf16 %0, %1, %2, %0" : "+v"(c) : "v"(a), "v"(b));
}
DEVI void mfma_pad() { __builtin_amdgcn_sched_barrier(0); asm volatile("s_nop 7\n\ts_nop 3" ::: "memory"); __builtin_amdgcn_sched_barrier(0); }
DEVI f32x16 mfma32(bf16x8 a, bf16x8 b, f32x16 c) { return __builtin_amdgcn_mfma_f32_32x32x16_bf16(a, b, c, 0, 0, 0); }
DEVI f32x16 mfma32k8(bf16x4 a, bf16x4 b, f32x16 c) {
    return __builtin_amdgcn_mfma_f32_32x32x8bf16_1k(__builtin_bit_cast(s16x4, a), __builtin_bit_cast(s16x4, b), c, 0, 0, 0);
}
DEVI bf16x4 ldtr(u32 a) { return __builtin_amdgcn_ds_read_tr16_b64_v4bf16((AS3 bf16x4*)a); }
DEVI bf16x8 cat8(bf16x4 x, bf16x4 y) { return __builtin_shufflevector(x, y, 0, 1, 2, 3, 4, 5, 6, 7); }
DEVI bf16x8 ldtr2(u32 a, u32 b) { bf16x4 x = ldtr(a), y = ldtr(b); return cat8(x, y); }
DEVI u32x4 lds128(u32 a) { return *(AS3 u32x4*)a; }
DEVI u32x2 lds64(u32 a) { return *(AS3 u32x2*)a; }
DEVI u32 lds32(u32 a) { return *(AS3 u32*)a; }
DEVI void sts128(u32 a, u32x4 v) { *(AS3 u32x4*)a = v; }
DEVI void sts64(u32 a, u32x2 v) { *(AS3 u32x2*)a = v; }
DEVI void sts32(u32 a, u32 v) { *(AS3 u32*)a = v; }
DEVI u32 rfl(u32 v) { return __builtin_amdgcn_readfirstlane(v); }
DEVI u32 opaque_s(u32 v) { asm volatile("" : "+s"(v)); return v; }
DEVI u32 opaque_v(u32 v) { asm volatile("" : "+v"(v)); return v; }
DEVI void cfence() { asm volatile("" ::: "memory"); }

DEVI u32x4 mkrs(const void* p, u32 bytes) { return u32x4{(u32)(uintptr_t)p, (u32)((uintptr_t)p >> 32) & 0xFFFFu, bytes, 0x00020000u}; }
template <int MO, int OFF = 0> DEVI void dma(u32 voff, u32x4 rs, u32 soff, u32 mb) {
    asm volatile("s_add_u32 m0, %3, %5\n\ts_nop 0\n\tbuffer_load_dwordx4 %0, %1, %2 offen offset:%4 lds"
                 :: "v"(voff), "s"(rs), "s"(soff), "s"(mb), "i"(OFF), "i"(MO) : "memory", "scc");
}
template <int MO, int OFF = 0> DEVI void dmac(u32 voff, u32x4 rs, u32 soff, u32 mb) {
    asm volatile("s_add_u32 m0, %3, %5\n\ts_nop 0\n\tbuffer_load_dwordx4 %0, %1, %2 offen offset:%4 sc1 lds"
                 :: "v"(voff), "s"(rs), "s"(soff), "s"(mb), "i"(OFF), "i"(MO) : "memory", "scc");
}
DEVI void pref(u32 voff, u32x4 rs, u32 soff) {
    asm volatile("s_mov_b32 m0, %3\n\ts_nop 0\n\tbuffer_load_dword %0, %1, %2 offen sc1 lds" :: "v"(voff), "s"(rs), "s"(soff), "i"(L_DUMP) : "memory");
}
DEVI u32 ldflag(u32 voff, u32x4 rs) {
    u32 v;
    asm volatile("buffer_load_dword %0, %1, %2, 0 offen sc1" : "=v"(v) : "v"(voff), "s"(rs) : "memory");
    return v;
}
DEVI void stflag(u32 val, u32 soff, u32x4 rs) {
    asm volatile("s_mov_b64 exec, 1\n\tbuffer_store_dword %0, off, %1, %2 sc1\n\ts_mov_b64 exec, -1" :: "v"(val), "s"(rs), "s"(soff) : "memory");
}
template <int OFF = 0> DEVI void st128(u32x4 v, u32 voff, u32x4 rs, u32 soff) {
    asm volatile("buffer_store_dwordx4 %0, %1, %2, %3 offen offset:%4 sc1\n\ts_nop 1" :: "v"(v), "v"(voff), "s"(rs), "s"(soff), "i"(OFF) : "memory");
}
template <int OFF = 0> DEVI void st128n(u32x4 v, u32 voff, u32x4 rs, u32 soff) {
    asm volatile("buffer_store_dwordx4 %0, %1, %2, %3 offen offset:%4\n\ts_nop 1" :: "v"(v), "v"(voff), "s"(rs), "s"(soff), "i"(OFF) : "memory");
}
template <int N> DEVI void wait_vm() { asm volatile("s_waitcnt vmcnt(%0)" ::"i"(N) : "memory"); }
template <int N> DEVI u32 wait_rfl(u32 v) { u32 r; asm volatile("s_waitcnt vmcnt(%2)\n\tv_readfirstlane_b32 %0, %1" : "=s"(r) : "v"(v), "i"(N) : "memory"); return r; }
template <int N> DEVI bool wait_eq(u32 v, u32 tag) {
    u64 m; asm volatile("s_waitcnt vmcnt(%3)\n\tv_cmp_ne_u32_e64 %0, %2, %1" : "=s"(m) : "v"(v), "s"(tag), "i"(N) : "memory");
    return m == 0ull;
}
template <int N> DEVI void wait_lgkm() { asm volatile("s_waitcnt lgkmcnt(%0)" ::"i"(N) : "memory"); }
DEVI void bar() { asm volatile("s_barrier" ::: "memory"); }
DEVI void sched0() { __builtin_amdgcn_sched_barrier(0); }
DEVI void spin(u32 voff, u32x4 rs, u32 tag) {
#pragma nounroll
    for (u32 i = 0; i < (1u << 17); ++i) {
        u32 v = ldflag(voff, rs);
        if (wait_eq<0>(v, tag)) break;
        __builtin_amdgcn_s_sleep(SPINS);
    }
}
DEVI void ctl_set(u32 a, u32 v) { asm volatile("s_mov_b64 exec, 1\n\tds_write_b32 %0, %1\n\ts_mov_b64 exec, -1" :: "v"(a), "v"(v) : "memory"); }
DEVI u32x4 ctl_get(u32 a) { return *(volatile AS3 u32x4*)a; }
DEVI bool ctl_ge(u32x4 v, u32 t) { return rfl(umin(umin(v[0], v[1]), umin(v[2], v[3]))) >= t; }
DEVI void ctl_wait(u32 a, u32 t) {
#pragma nounroll
    for (u32 i = 0; i < (1u << 20); ++i) { if (ctl_ge(ctl_get(a), t)) break; __builtin_amdgcn_s_sleep(1); }
    cfence();
}
DEVI void ctl_wait8(u32 t) {
#pragma nounroll
    for (u32 i = 0; i < (1u << 20); ++i) {
        const u32x4 x = ctl_get(L_HQ), y = ctl_get(L_MC);
        if (ctl_ge(u32x4{umin(x[0], y[0]), umin(x[1], y[1]), umin(x[2], y[2]), umin(x[3], y[3])}, t)) break;
        __builtin_amdgcn_s_sleep(1);
    }
    cfence();
}
DEVI void ctl_wait1(u32 a, u32 t) {
#pragma nounroll
    for (u32 i = 0; i < (1u << 20); ++i) { if (rfl(*(volatile AS3 u32*)a) >= t) break; __builtin_amdgcn_s_sleep(1); }
    cfence();
}
DEVI bool marked(u32 m) { return __builtin_amdgcn_ballot_w64(m == 0xFFFFFFFFu) != 0ull; }
DEVI u32 mx4(u32 m, u32x4 v) { return umax(umax(umax(m, v[0]), v[1]), umax(v[2], v[3])); }

DEVI float phi(float t) { return __builtin_amdgcn_fmed3f(t + 1.f, __builtin_amdgcn_exp2f(t * LOG2E), 1.f) + 1e-6f; }
DEVI u32 phi2(float a, float b) { return pk2(phi(a), phi(b)); }
struct OF { u32x4 m[2][4]; u32x2 r[2][2]; };
template <int OFF> DEVI void rl128(u32x4& v, u32 voff, u32x4 rs, u32 soff) {
    asm volatile("buffer_load_dwordx4 %0, %1, %2, %3 offen offset:%4 sc1" : "+v"(v) : "v"(voff), "s"(rs), "s"(soff), "i"(OFF) : "memory");
}
DEVI u32x4 ld128(u32 voff, u32x4 rs, u32 soff) {
    u32x4 v;
    asm volatile("buffer_load_dwordx4 %0, %1, %2, %3 offen" : "=v"(v) : "v"(voff), "s"(rs), "s"(soff) : "memory");
    return v;
}
template <int OFF> DEVI void rl64(u32x2& v, u32 voff, u32x4 rs, u32 soff) {
    asm volatile("buffer_load_dwordx2 %0, %1, %2, %3 offen offset:%4 sc1" : "+v"(v) : "v"(voff), "s"(rs), "s"(soff), "i"(OFF) : "memory");
}
#define OF_OPS(o) "+v"(o.m[0][0]), "+v"(o.m[0][1]), "+v"(o.m[0][2]), "+v"(o.m[0][3]), "+v"(o.m[1][0]), "+v"(o.m[1][1]), "+v"(o.m[1][2]), "+v"(o.m[1][3]), \
                  "+v"(o.r[0][0]), "+v"(o.r[0][1]), "+v"(o.r[1][0]), "+v"(o.r[1][1])
DEVI void of_wait(OF& o) { asm volatile("s_waitcnt vmcnt(0)" : OF_OPS(o) :: "memory"); }
DEVI void of_tie(OF& o) { asm volatile("" : OF_OPS(o) :: "memory"); }

#define STAMP(i)
#define STAMPC(i)

template <int N> struct IC { static constexpr int v = N; };
template <int I, int N, class F> DEVI void sfor(F&& f) { if constexpr (I < N) { f(IC<I>{}); sfor<I + 1, N>(f); } }

#define ROTA 1
#define ROTB 3
#define UPTO {2, 4, 5, 6, 7, 8, 9, 10, 10, 10, 10}
#define HK1 3
#define HK2 5
#define HA 4
#define HB 6
#define XPM 0
#define HPRIO 3
#define QPRIO 0
#define PFQ 0
#define PFO 0
#define OLATE 1
#define FQS 1
__device__ constexpr int UP[11] = UPTO;
__global__ __launch_bounds__(512, 2) void fused_kernel(const u16* __restrict__ x, const u16* __restrict__ wa, const u16* __restrict__ woa,
                                                       const u16* __restrict__ wb, const u16* __restrict__ wob, const u16* __restrict__ gw,
                                                       u16* __restrict__ out, char* __restrict__ ws) {
    const u32 lane = threadIdx.x & 63;
    const u32 wid = rfl(threadIdx.x >> 6);
    const u32 b = blockIdx.x, xcd = b & 7, idx = b >> 3;
    STAMP(0);
    STAMPC(62);
    if (wid == 3) {
        u64 pc; u32 len;
        asm volatile("s_getpc_b64 %0\n.Lcode_lo:\n\ts_mov_b32 %1, .Lcode_hi-.Lcode_lo" : "=s"(pc), "=s"(len));
        u32 o = (idx & 15) * 4096 + lane * 64;
        o = o < len ? o : 0;
        asm volatile("s_mov_b32 m0, %2\n\ts_nop 0\n\tglobal_load_lds_dword %0, %1" :: "v"(o), "s"(pc), "i"(L_DUMP) : "memory");
    }
    const u32 rb = (xcd & 1) * 8 + (idx >> 2), c4 = idx & 3, sg = xcd >> 1;
    const u32 ty = c4 >> 1;
    const u32 hg = ty ? 2 * sg + (c4 & 1) : sg + 4 * (c4 & 1);
    const u32 hp = 4 * sg + c4;
    const u32 wr = wid & 3;
    const u32x4 rws = mkrs(ws, WS_END);
    const __amdgpu_buffer_rsrc_t wsr = __builtin_amdgcn_make_buffer_rsrc(ws, 0, WS_END, 0x00020000);
    const u32 l15 = lane & 15, g4 = lane >> 4, l31 = lane & 31, hf = lane >> 5, l16 = lane * 16;
    const u32 me = (wid >= 4 ? L_HQ : L_MC) + wr * 4;
    const u32 oprod = WS_O + (rb * 8 + 2 * wr) * 40960 + sg * 5120;
    u32x4 qf; u32x2 qr; float gt;
    u32x4 P[3]; float g0, g1;
    u32 tag;

    if (__builtin_expect(wid >= 4, 1)) {
        const u32 rot = (ROTA * rb + ROTB * c4) % 10;
        auto kbof = [&](u32 s) { u32 k = rot + s; return k >= 10 ? k - 10 : k; };
        const u32x4 rsx = mkrs(x, 2048 * 1280), rsw = mkrs(ty ? wb : wa, 640 * 3840);
        const u32 vx = (rb * 128 + 32 * wr + (lane >> 3)) * 1280 + (((lane & 7) ^ ((lane >> 3) & 6)) * 16);
        u32 vo[5];
#pragma unroll
        for (int i = 0; i < 5; ++i) {
            const u32 ch = 64 * i + lane, pr = (ch * 205) >> 12, j = ch - pr * 20;
            const u32 k = 32 * (wr >> 1) + 8 * (wr & 1) + (pr & 7) + 2 * (pr & 8);
            vo[i] = k * 3840 + (ty ? 1280 + 160 * hg + (j >= 10 ? 1120 : 0) : 1280 + 320 * hg) + j * 16;
        }
        auto issuep = [&](auto SC, auto A, auto B) {
            constexpr int s = decltype(SC)::v;
            const u32 kb = kbof(s);
            sfor<A.v, B.v>([&](auto I) {
                if constexpr (I.v < 4) dma<SL[s % 4] + XO + I.v * 1024>(vx, rsx, kb * 128 + I.v * 10240, wr * 4096);
                else dma<SL[s % 4] + (I.v - 4) * 1024>(vo[I.v - 4], rsw, kb * 245760, wr * WREG);
            });
        };
        auto issue = [&](auto SC) { issuep(SC, IC<0>{}, IC<9>{}); };
        STAMP(30);
        issue(IC<0>{});
        STAMP(31);
        {
            const u32 o = l16 + (wr & 1) * 1024 + rot * 128;
            dma<L_GW>(o >= 1280 ? o - 1280 : o, mkrs(gw, 1280), 0, (wr & 1) * 1024);
        }
        sfor<1, UP[0]>(issue);
        STAMP(1);
        const u32 xb0 = XO + (32 * wr + l15) * 128 + ((g4 ^ (l15 & 6)) * 16), xb1 = xb0 ^ 64;
        const u32 xc0 = xb0 + 2 * SLOT, xc1 = xb1 + 2 * SLOT, ga = L_GW + g4 * 16;
        const u32 qm0 = (wr & 1) * 5120 + (1 - (wr >> 1)) * 10240, qs0 = WS_WQ + hp * 102400 + (wr & 1) * 5120;
        auto qissue = [&](auto JC) {
            constexpr int jj = decltype(JC)::v;
            const u32 so = qs0 + kbof(2 * jj + (wr >> 1)) * 10240;
            sfor<0, 4>([&](auto I) { dmac<L_QT - 20480 * (jj + 1), I.v * 1024>(l16, rws, so, qm0); });
            dmac<L_QT - 20480 * (jj + 1) + 4096>(l16, rws, so + 4096, qm0);
        };
        const u32 fqo = WS_FQ + sg * 1024 + g4 * 256 + l15 * 4;
        bf16x8 xf[10][2][2];
        f32x4 gacc[2];
        u32 pv = 0;
        bool qok = true;
        auto wojob = [&](auto ST) {
#pragma unroll
            for (u32 j = 0; j < 2; ++j) {
                const u32 p = j * 1024 + b * 4 + wr;
                const bool br = p >= 800;
                const u32 q = br ? p - 800 : p, oct = q / 10, cl = q - oct * 10;
                const u32 kx = oct >> 2, go = oct & 3, G = kx / 5, e = kx - G * 5;
                if constexpr (!ST.v) {
                    const u32 r0 = 160 * G + (e == 4 ? 20 * go + 16 : 40 * e + (br ? 20 * (go >> 1) + 4 * (go & 1) : 4 * go));
                    const u32 step = e == 4 ? 80 : br ? 8 : 20;
                    dma<L_SCR>(p < 1600 ? (((lane >> 3) & 3) + step * hf) * 1280 + (lane & 7) * 16 : OOB, mkrs(br ? woa : wob, 640 * 1280),
                               p < 1600 ? r0 * 1280 + cl * 128 : 0, wr * 2048 + j * 1024);
                } else {
                    const u32 n = 64 * cl + lane, h = (n * 205) >> 12, d = n - 20 * h;
                    const u32 jd = p < 1600 ? WS_WO + ((h >> 2) * 2 + br) * 102400 + (kx * 5 + (d >> 2)) * 1024 + (go * 16 + 4 * (h & 3) + (d & 3)) * 16 : OOB;
                    const u32 js = L_SCR + wr * 2048 + j * 1024 + (l15 >> 2) * 128 + (4 * g4 + (lane & 3)) * 8;
                    st128(__builtin_bit_cast(u32x4, ldtr2(js, js + 512)), jd, rws, 0);
                }
            }
        };
        constexpr int XA[10] = {0, 2, 0, 0, 2, 0, 1, 0, 0, 0}, XB[10] = {0, 0, 0, PFQ == 3, PFQ == 4, PFQ == 5, 0, 0, 0, 0};
        sfor<0, 10>([&](auto SC) {
            constexpr int s = decltype(SC)::v;
            wait_lgkm<0>();
            constexpr int ex = s < 2 ? 0 : s < 4 ? XA[1] : XB[s - 3] + XA[s - 2] + XB[s - 2] + XA[s - 1] + XB[s - 1];
            if constexpr (s < 8) wait_vm<9 * (UP[s] - s - 1) + ex>();
            if constexpr (s == 9) { if (qok) wait_vm<15>(); else wait_vm<0>(); }
            bar();
            if constexpr (s == 0) __builtin_amdgcn_s_setprio(HPRIO);
            STAMP(2 + s);
            if constexpr (s == 0 || s == 2 || s == 4 || s == 7) STAMPC(s == 0 ? 51 : s == 2 ? 52 : s == 4 ? 53 : 54);
            constexpr bool up = SL[s % 4] >= 2 * SLOT, any = UP[s + 1] > UP[s];
            constexpr u32 xo = SL[s % 4] - (up ? 2 * SLOT : 0);
            const u32 a0 = up ? xc0 : xb0, a1 = up ? xc1 : xb1;
            if constexpr (s == 1) wojob(IC<0>{});
            if constexpr (s == 4) wojob(IC<1>{});
            if constexpr (s == 5) tag = rfl(lds32(L_TAG));
            if constexpr (s == 6) pv = ldflag(fqo, rws);
            if constexpr (s == 7) { qok = wait_eq<9>(pv, tag); if (qok) qissue(IC<0>{}); }
            if constexpr (s == 8) { if (qok) qissue(IC<1>{}); }
            if constexpr (s == 9) { if (qok) qissue(IC<3>{}); }
            if constexpr (any) issuep(IC<UP[s]>{}, IC<0>{}, IC<HA>{});
            xf[s][0][0] = asbf8(lds128(a0 + xo)); xf[s][0][1] = asbf8(lds128(a0 + xo + 2048));
            xf[s][1][0] = asbf8(lds128(a1 + xo)); xf[s][1][1] = asbf8(lds128(a1 + xo + 2048));
            const bf16x8 w0 = asbf8(lds128(ga + s * 128)), w1 = asbf8(lds128(ga + s * 128 + 64));
            if constexpr (any) issuep(IC<UP[s]>{}, IC<HA>{}, IC<HB>{});
            mfma16a<s == 0>(gacc[0], w0, xf[s][0][0]); mfma16a<s == 0>(gacc[1], w0, xf[s][0][1]);
            mfma16a(gacc[0], w1, xf[s][1][0]); mfma16a(gacc[1], w1, xf[s][1][1]);
            if constexpr (any) { issuep(IC<UP[s]>{}, IC<HB>{}, IC<9>{}); sfor<UP[s] + 1, UP[s + 1]>(issue); }
            if constexpr (s == PFQ) pref(lane < 25 ? lane * 128 : OOB, rws, WS_WQ + sg * 409600 + (idx * 4 + wr) * 3200);
            if constexpr (s == 8) { if (qok) qissue(IC<2>{}); }
            if constexpr (s == 9) { if (qok) qissue(IC<4>{}); }
        });
        __builtin_amdgcn_s_setprio(QPRIO);
        if (!qok) { spin(fqo, rws, tag); sfor<0, 5>(qissue); }
        asm volatile("s_nop 7\n\ts_nop 3" : "+v"(gacc[0]), "+v"(gacc[1]));
        wait_vm<20 + 2 * PFO>();
        ctl_set(me, 1);
        const u32 lhi = opaque_v(l16 + L_QT - 51200);
        ctl_wait(L_HQ, 1);
        if (wr == 0) stflag(tag, WS_FS + (b >> 4) * 256 + (b & 15) * 4, rws);
        STAMP(12);
        STAMPC(55);
        constexpr int PD = 4;
        bf16x8 wf[PD + 1];
        f32x4 qa[2][5];
        u32x4 snap;
        auto wadr = [&](int n) {
            const int j = n / 10;
            return (j < 5 ? lhi + 40960 - 10240 * j : l16 + L_QT - 10240 * (j + 1)) + (n % 10) * 1024;
        };
        sfor<0, PD>([&](auto NC) { wf[NC.v] = asbf8(lds128(wadr(NC.v))); });
        sfor<0, 100>([&](auto NC) {
            constexpr int n = decltype(NC)::v, m = n + PD;
            constexpr int st = n / 10, ks = (n % 10) / 5, a = n % 5;
            const bf16x8 w = wf[n % (PD + 1)];
            if constexpr (m < 100 && m % 20 == 0) { if (!ctl_ge(snap, m / 20 + 1)) ctl_wait(L_HQ, m / 20 + 1); cfence(); }
            sched0();
            mfma16a<(n < 5)>(qa[0][a], w, xf[st][ks][0]);
            sched0();
            if constexpr (m < 100) wf[m % (PD + 1)] = asbf8(lds128(wadr(m)));
            sched0();
            mfma16a<(n < 5)>(qa[1][a], w, xf[st][ks][1]);
            sched0();
            if constexpr (n < 80 && n % 20 == 8) {
                wait_vm<15 + 2 * PFO - 5 * (n / 20)>();
                ctl_set(me, n / 20 + 2);
            }
            if constexpr (n < 80 && n % 20 == 12) snap = ctl_get(L_HQ);
            sched0();
        });
        mfma_pad();
        STAMP(13);
        STAMPC(56);
        g0 = __builtin_amdgcn_rcpf(1.f + __builtin_amdgcn_exp2f(-gacc[0][0] * LOG2E));
        g1 = __builtin_amdgcn_rcpf(1.f + __builtin_amdgcn_exp2f(-gacc[1][0] * LOG2E));
        gt = (lane & 16) ? g1 : g0;
        {
            auto cvt = [&](int a, u32x4& o) {
                auto s0 = __builtin_amdgcn_permlane16_swap(pk2(qa[0][a][0] * QSCALE, qa[0][a][1] * QSCALE), pk2(qa[1][a][0] * QSCALE, qa[1][a][1] * QSCALE), false, false);
                auto s1 = __builtin_amdgcn_permlane16_swap(pk2(qa[0][a][2] * QSCALE, qa[0][a][3] * QSCALE), pk2(qa[1][a][2] * QSCALE, qa[1][a][3] * QSCALE), false, false);
                o = u32x4{s0[0], s1[0], s0[1], s1[1]};
            };
            u32x4 r, q1;
            cvt(4, q1); cvt(2, r);
            sts128(L_XQ + wr * 1536 + l16, q1);
            sts64(L_XQ + wr * 1536 + 1024 + lane * 8, u32x2{hf ? f2u(gt) : r[2], r[3]});
            ctl_set(me, 6);
            cvt(3, qf);
            qr = hf ? u32x2{0, 0} : u32x2{r[0], r[1]};
        }
#pragma unroll
        for (int t = 0; t < 2; ++t) {
            P[t] = u32x4{phi2(qa[t][0][0], qa[t][0][1]), phi2(qa[t][0][2], qa[t][0][3]), phi2(qa[t][1][0], qa[t][1][1]), phi2(qa[t][1][2], qa[t][1][3])};
            const u32 u = phi2(qa[t][2][0], qa[t][2][1]), v = phi2(qa[t][2][2], qa[t][2][3]);
            P[2][2 * t] = __builtin_amdgcn_permlane32_swap(u, u, false, false)[1];
            P[2][2 * t + 1] = __builtin_amdgcn_permlane32_swap(v, v, false, false)[1];
        }
        asm volatile("" : "+v"(P[0]), "+v"(P[1]), "+v"(P[2]), "+v"(qf), "+v"(qr));
        STAMP(14);
        __builtin_amdgcn_s_setprio(0);
        ctl_wait(L_MC, 3);
        STAMP(16);
        STAMPC(57);
    } else {
        const u32 wm = wr >> 1, wn = wr & 1;
        const u32 ev = ldflag(0, rws);
        const u32 pw0 = (xcd & 1) * 128 + idx * 4 + wr;
        u32 qd[2];
#pragma unroll
        for (u32 j = 0; j < 2; ++j) {
            const u32 p = pw0 + 256 * j, isb = p >= 200, q = isb ? p - 200 : p, rg = (q * 205) >> 10;
            const u32 so = p < 400 ? rg * 61440 + sg * 320 + (q - rg * 5) * 64 : 0;
            dma<L_SCR>(p < 400 ? (lane >> 2) * 3840 + (lane & 3) * 16 : OOB, mkrs(isb ? wb : wa, 640 * 3840), so, wr * 2048 + j * 1024);
            const u32 n = (q - rg * 5) * 32 + l31, h = (n * 205) >> 12, d = n - 20 * h, o8 = 2 * rg + hf;
            const u32 t = d < 16 ? (h & 1) * 1024 + (isb ? 0 : 3072) + d * 16 : 2048 - 256 + (8 * isb + 4 * (h & 1) + d) * 16;
            qd[j] = p < 400 ? WS_WQ + (4 * sg + (h >> 1)) * 102400 + (o8 >> 2) * 5120 + (o8 & 3) * 256 + t : OOB;
        }
        const u32 krec = WS_KVP + ((2 * hg + wn) * 16 + rb) * 2048 + wm * 1024;
        if (lane < 2) sts32(L_HQ + lane * 16 + wr * 4, 0);
        u32 wb_[2][3], xb_[2][2];
        {
            const u32 q3 = lane & 3, wrow = (g4 & 1) * WREG + (8 * (g4 >> 1) + (l15 >> 2)) * 320;
            wb_[0][0] = wrow + (ty ? 80 : 160) * wn + q3 * 8;
            wb_[0][1] = wb_[0][0] + (ty ? 160 : 80);
            wb_[0][2] = wrow + 32 + (ty ? 80 * wn + ((q3 & 2) ? 0 : 160) + 40 * (q3 & 1) : 160 * wn + 40 * q3);
            xb_[0][0] = XO + (64 * wm + 16 * (l15 >> 3) + (l15 & 7)) * 128 + ((g4 ^ (l15 & 6)) * 16);
            xb_[0][1] = xb_[0][0] ^ 64;
#pragma unroll
            for (int i = 0; i < 3; ++i) wb_[1][i] = opaque_v(wb_[0][i] + 2 * SLOT);
#pragma unroll
            for (int i = 0; i < 2; ++i) xb_[1][i] = opaque_v(xb_[0][i] + 2 * SLOT);
        }
        const u32 qsrc = L_SCR + wr * 2048 + (8 * hf + (l15 >> 2)) * 64 + (g4 & 1) * 32 + (lane & 3) * 8;
        auto wad = [&](int s, int m) { return wb_[SL[s % 4] >= 2 * SLOT][(m % 5) >> 1] + SL[s % 4] % (2 * SLOT) + (m / 5) * (2 * WREG) + ((m % 5) & 1) * 40; };
        auto xad = [&](int s, int ks, int t) { return xb_[SL[s % 4] >= 2 * SLOT][ks] + SL[s % 4] % (2 * SLOT) + (t >> 1) * 4096 + (t & 1) * 1024; };
        f32x4 acc[4][5];
        STAMP(30);
        tag = wait_rfl<0>(ev) + TAGC;
        STAMP(31);
        st128(__builtin_bit_cast(u32x4, ldtr2(qsrc, qsrc + 256)), qd[0], rws, 0);
        st128(__builtin_bit_cast(u32x4, ldtr2(qsrc + 1024, qsrc + 1280)), qd[1], rws, 0);
        if (wr == 0 && lane == 0) sts32(L_TAG, tag);
        {
            bf16x8 wf[5], xa[2][4];
            STAMP(1);
            wait_lgkm<0>();
            bar();
            STAMP(2);
            sfor<0, 4>([&](auto I) { xa[0][I.v] = asbf8(lds128(xad(0, 0, I.v))); });
            wf[0] = ldtr2(wad(0, 0), wad(0, 0) + 1280);
            wf[1] = ldtr2(wad(0, 1), wad(0, 1) + 1280);
            sfor<0, 100>([&](auto NC) {
                constexpr int s = decltype(NC)::v / 10, n = decltype(NC)::v % 10, ks = n / 5, c = n % 5;
                constexpr int s2 = n >= 8 ? s + 1 : s, m = (n + 2) % 10;
                constexpr bool more = s2 < 10, first = s == 0 && ks == 0, rdw = more && (!(XPM & 1) || s == 0), rdx = !(XPM & 2) || s == 0;
                bf16x4 lo, hi;
                bf16x8 t0, t1;
                auto mm = [&](auto I) { sched0(); if constexpr (!(XPM & 4) || s == 0) mfma16a<first>(acc[I.v][c], xa[ks][I.v], wf[c]); else asm volatile("" :: "v"(xa[ks][I.v]), "v"(wf[c])); sched0(); };
                mm(IC<0>{});
                if constexpr (n == 8 && more) {
                    wait_lgkm<0>();
                    if constexpr (s == FQS) wait_vm<0>();
                    bar();
                    sched0();
                }
                if constexpr (rdw) lo = ldtr(wad(s2, m));
                mm(IC<1>{});
                if constexpr (rdw) hi = ldtr(wad(s2, m) + 1280);
                mm(IC<2>{});
                if constexpr (n >= 8 && more && rdx) t0 = asbf8(lds128(xad(s2, 0, 2 * (n - 8))));
                if constexpr (n < 4 && rdx) xa[1][n] = asbf8(lds128(xad(s, 1, n)));
                mm(IC<3>{});
                if constexpr (n >= 8 && more && rdx) { t1 = asbf8(lds128(xad(s2, 0, 2 * (n - 8) + 1))); xa[0][2 * (n - 8)] = t0; xa[0][2 * (n - 8) + 1] = t1; }
                if constexpr (s == FQS && n == 8) { if (wr == 0) stflag(tag, WS_FQ + sg * 1024 + (idx >> 3) * 256 + ((idx & 7) * 2 + (xcd & 1)) * 4, rws); }
                if constexpr (rdw) wf[m % 5] = cat8(lo, hi);
                sched0();
            });
        }
        mfma_pad();
        __builtin_amdgcn_s_setprio(PRIO);
        STAMP(12);
        auto pk8 = [&](int u, int c) {
            return u32x4{pk2(acc[2 * u][c][0], acc[2 * u][c][1]), pk2(acc[2 * u][c][2], acc[2 * u][c][3]),
                         pk2(acc[2 * u + 1][c][0], acc[2 * u + 1][c][1]), pk2(acc[2 * u + 1][c][2], acc[2 * u + 1][c][3])};
        };
        auto ph8 = [&](int u, int c) {
            return u32x4{phi2(acc[2 * u][c][0], acc[2 * u][c][1]), phi2(acc[2 * u][c][2], acc[2 * u][c][3]),
                         phi2(acc[2 * u + 1][c][0], acc[2 * u + 1][c][1]), phi2(acc[2 * u + 1][c][2], acc[2 * u + 1][c][3])};
        };
        auto markers = [&]() {
            const u32x4 mk = {~0u, ~0u, ~0u, ~0u};
            sfor<0, 4>([&](auto I) { st128(mk, l16, rws, oprod + c4 * 1024 + I.v * 20480); });
            st128(mk, (g4 >> 1) * 40960 + (g4 & 1) * 20480 + l15 * 16, rws, oprod + 4096 + c4 * 256);
        };
        if (!ty) {
            const u32 so = WS_T + ((4 * hg + 2 * wn) * 64 + 4 * rb + 2 * wm) * 2560;
            const u32 vm = g4 * 256 + ((l15 ^ (4 * g4)) * 16);
            const u32 vr = 2048 + g4 * 128 + ((((l15 >> 2) ^ (g4 >> 1)) & 1) * 64) + (l15 & 3) * 16 + (l15 >> 3) * 163840;
            sfor<0, 10>([&](auto I) {
                constexpr int c = decltype(I)::v >> 1, u = decltype(I)::v & 1;
                st128<(c < 4 ? (c & 1) * 1024 : 0) + u * 2560>(pk8(u, c), c < 4 ? vm : vr, rws, so + (c == 2 || c == 3 ? 163840 : 0));
            });
            markers();
            wait_vm<5>();
            stflag(tag, WS_FT + ((hg * 2 + wn) * 32 + 2 * rb + wm) * 128, rws);
        } else {
            markers();
            f32x4 Q0 = {0, 0, 0, 0}, Q1 = Q0, X0 = Q0, X1 = Q0, Y0 = Q0, Y1 = Q0, U = Q0;
#pragma unroll
            for (int u = 0; u < 2; ++u) {
                const bf16x8 v0 = asbf8(pk8(u, 2)), v1 = asbf8(pk8(u, 3));
                const u32x4 rr = pk8(u, 4);
                const bf16x8 rbv = asbf8(l15 < 8 ? rr : u32x4{ONES, ONES, ONES, ONES});
                const bf16x8 k0 = asbf8(ph8(u, 0));
                Q0 = mfma16(k0, v0, Q0); X0 = mfma16(k0, rbv, X0);
                const bf16x8 k1 = asbf8(ph8(u, 1));
                Q1 = mfma16(k1, v1, Q1); X1 = mfma16(k1, rbv, X1);
                const bf16x8 ra = asbf8(ph8(u, 4));
                Y0 = mfma16(ra, v0, Y0); Y1 = mfma16(ra, v1, Y1); U = mfma16(ra, rbv, U);
            }
            f32x4 X = (l15 & 4) ? X1 : X0, Z;
#pragma unroll
            for (int r = 0; r < 4; ++r) Z[r] = u2f(__builtin_amdgcn_permlane32_swap(f2u((g4 & 1) ? Y1[r] : Y0[r]), f2u(U[r]), false, false)[1]);
            sts128(L_SCR + wr * 2048 + l16, __builtin_bit_cast(u32x4, wm ? Q0 : X));
            sts128(L_SCR + wr * 2048 + 1024 + l16, __builtin_bit_cast(u32x4, wm ? Q1 : Z));
            ctl_set(me, 1);
            ctl_wait1(L_MC + (wr ^ 2) * 4, 1);
            const f32x4 a = (wm ? X : Q0) + __builtin_bit_cast(f32x4, lds128(L_SCR + (wr ^ 2) * 2048 + l16));
            const f32x4 c = (wm ? Z : Q1) + __builtin_bit_cast(f32x4, lds128(L_SCR + (wr ^ 2) * 2048 + 1024 + l16));
            st128(u32x4{pk2(a[0], a[1]), pk2(a[2], a[3]), pk2(c[0], c[1]), pk2(c[2], c[3])}, l16, rws, krec);
        }
        STAMP(13);
        {
            const int g0 = 2 * (int)rb - 4 + 3 * (int)wn;
            int gl = g0 + (int)(lane < 2 ? lane : 2);
            gl = gl < 0 ? 0 : gl;
            spin(WS_FT + ((wm * 8 + (hp >> 1)) * 32 + gl) * 128, rws, tag);
            ctl_set(me, 2);
            ctl_wait8(2);
            if (wr == 0) stflag(tag, WS_F2 + rb * 256 + hp * 4, rws);
            STAMP(14);
#pragma unroll
            for (int i = 0; i < 3; ++i) {
                const u32 mb = wm ? L_V + (3 * wn + i) * 5136 : L_K + (3 * wn + i) * 5120;
                if (g0 + i >= 0) {
                    const u32 so = WS_T + ((wm * 16 + hp) * 32 + g0 + i) * 5120;
                    dmac<0, 0>(l16, rws, so, mb); dmac<0, 1024>(l16, rws, so, mb); dmac<0, 2048>(l16, rws, so, mb); dmac<0, 3072>(l16, rws, so, mb);
                    dmac<4096, 0>(l16, rws, so + 4096, mb);
                } else {
#pragma unroll
                    for (int k = 0; k < 5; ++k) sts128(mb + k * 1024 + l16, u32x4{0, 0, 0, 0});
                }
            }
            if (wm && lane < 12) sts32(L_V + (3 * wn + (lane >> 2)) * 5136 + 5120 + (lane & 3) * 4, g0 + (int)(lane >> 2) >= 0 ? ONES : 0);
            spin(WS_FS + wr * 1024 + g4 * 256 + l15 * 4, rws, tag);
            ctl_set(me, 3);
            STAMP(15);
        }
        ctl_wait(L_HQ, 6);
        qf = lds128(L_XQ + wr * 1536 + l16);
        const u32x2 r = lds64(L_XQ + wr * 1536 + 1024 + lane * 8);
        qr = hf ? u32x2{0, 0} : r;
        gt = u2f(lds32(L_XQ + wr * 1536 + 1280 + l31 * 8));
        ctl_wait(L_MC, 3);
        if (b == 0 && wr == 0) stflag(tag, WS_E, rws);
        __builtin_amdgcn_s_setprio(0);
        STAMP(16);
    }
    const u32 r64 = xcd * 4 + (idx >> 3), c80 = idx & 7;
    bf16x8 W[25];
    auto wload = [&](auto I) { W[I.v] = asbf8(__builtin_amdgcn_raw_buffer_load_b128(wsr, l16 + (I.v & 3) * 1024, WS_WO + c80 * 204800 + wid * 25600 + (I.v >> 2) * 4096, 16)); };
    const u32 f2o = WS_F2 + ((lane & 16) ? (r64 >> 1) * 256 + l15 * 4 : l15 * 256 + ((hp & 12) + 2 + ((hp >> 1) & 1)) * 4);
    auto recs = [&]() {
        const u32 vo = hf * 2048 + l31 * 16, so = WS_KVP + hp * 32768 + wr * 512;
        sfor<0, 8>([&](auto I) { dmac<L_RC + I.v * 1024>(vo, rws, so + I.v * 4096, wr * 8192); });
    };
    u32 pv;
    bool rok = false;
    {
        const u32 hh = wid < 4;
        const u32 ch = 2 * (g4 & 1) + (lane & 1), kb = L_K + wr * 2560 + ((lane >> 1) & 1) * 8;
        const u32 ka = kb + hh * 1024 + ch * 256 + (((8 * hf + (l15 >> 2)) ^ (4 * ch)) * 16), kc = ka ^ 64;
        const u32 kr = kb + 2048 + ch * 128 + ((hh ^ (ch >> 1)) * 64) + (l15 >> 2) * 16;
        u32 va[2][2];
#pragma unroll
        for (int p = 0; p < 2; ++p)
#pragma unroll
            for (int m = 0; m < 2; ++m) {
                const u32 bi = wr + p, c = 2 * m + hf;
                va[p][m] = L_V + (bi >> 1) * 5136 + (l31 < 20 ? (bi & 1) * 2560 + (l31 < 16 ? hh * 1024 + c * 256 + ((l31 ^ (4 * c)) * 16) : 2048 + c * 128 + ((hh ^ m) * 64) + (l31 - 16) * 16) : 5120);
            }
        const int qq = (int)l31 - 4 * (int)hf;
        f32x16 zero;
#pragma unroll
        for (int i = 0; i < 16; ++i) zero[i] = 0.f;
        const bf16x8 q16 = asbf8(qf);
        const bf16x4 q4 = __builtin_bit_cast(bf16x4, qr);
        auto qk = [&](u32 o) { return mfma32k8(ldtr(kr + o), q4, mfma32(ldtr2(ka + o, kc + o), q16, zero)); };
        f32x16 o = zero;
        f32x16 S = qk(2560), Sn, S8;
        sfor<1, 9>([&](auto I) {
            constexpr int i = decltype(I)::v & 7;
            constexpr u32 vo_ = (i >> 1) * 5136;
            constexpr int it = decltype(I)::v;
            cfence();
            if constexpr (it == HK1) pv = ldflag(f2o, rws);
            if constexpr (it == HK2) { rok = wait_eq<3 * (HK2 - HK1)>(pv, tag); if (rok && wid < 4) recs(); }
            sfor<(it == 1 ? 0 : 3 * it - 2), 3 * it + 1>(wload);
            cfence();
            const bf16x8 v0 = asbf8(lds128(va[i & 1][0] + vo_)), v1 = asbf8(lds128(va[i & 1][1] + vo_));
            bf16x8 v2, v3;
            if constexpr (i == 0) { v2 = asbf8(lds128(va[0][0] + 4 * 5136)); v3 = asbf8(lds128(va[0][1] + 4 * 5136)); }
            if constexpr (i != 0) Sn = qk(((i + 1) & 7) * 2560);
            if constexpr (i == 6) S8 = qk(8 * 2560);
            if constexpr (i == 0) {
#pragma unroll
                for (int r = 0; r < 16; ++r) S[r] = ((r & 3) + 8 * (r >> 2) <= qq) ? S8[r] : S[r];
            }
            u32 p[8];
#pragma unroll
            for (int j = 0; j < 8; ++j) p[j] = pk2(__builtin_amdgcn_exp2f(S[2 * j]), __builtin_amdgcn_exp2f(S[2 * j + 1]));
            if constexpr (i == 0) {
                u32 pd[8];
#pragma unroll
                for (int j = 0; j < 8; ++j) {
                    const int k0 = ((2 * j) & 3) + 8 * ((2 * j) >> 2);
                    const u32 mk = (k0 <= qq ? 0xFFFFu : 0u) | (k0 + 1 <= qq ? 0xFFFF0000u : 0u);
                    pd[j] = p[j] & mk;
                    p[j] = p[j] & ~mk;
                }
                o = mfma32(v2, asbf8(u32x4{pd[0], pd[1], pd[2], pd[3]}), o);
                o = mfma32(v3, asbf8(u32x4{pd[4], pd[5], pd[6], pd[7]}), o);
            }
            o = mfma32(v0, asbf8(u32x4{p[0], p[1], p[2], p[3]}), o);
            o = mfma32(v1, asbf8(u32x4{p[4], p[5], p[6], p[7]}), o);
            if constexpr (i != 0) S = Sn;
        });
        float am = 0.f;
#pragma unroll
        for (int r = 0; r < 12; r += 2) am = fmaxf(fmaxf(am, fabsf(o[r])), fabsf(o[r + 1]));
        const bool fine = o[12] > 0x1p-30f && o[12] < 0x1p30f && am < 0x1p120f;
        if (__builtin_amdgcn_ballot_w64(!fine) != 0ull) {
            const int i0 = rb >= 2 ? 0 : 8 - 4 * (int)rb - (int)wr;
            auto sc = [&](int i) {
                f32x16 s = qk(i * 2560);
#pragma unroll
                for (int r = 0; r < 16; ++r) {
                    const bool le = (r & 3) + 8 * (r >> 2) <= qq;
                    const bool ok = i == 0 ? !le : (i == 8 ? le : true);
                    s[r] = ok ? s[r] : -__builtin_inff();
                }
                return s;
            };
            float m = -__builtin_inff();
#pragma nounroll
            for (int i = i0; i < 9; ++i) {
                const f32x16 s = sc(i);
#pragma unroll
                for (int r = 0; r < 16; ++r) m = fmaxf(m, s[r]);
            }
            auto sw = __builtin_amdgcn_permlane32_swap(f2u(m), f2u(m), false, false);
            m = fmaxf(u2f(sw[0]), u2f(sw[1]));
            o = zero;
#pragma nounroll
            for (int i = i0; i < 9; ++i) {
                const f32x16 s = sc(i);
                u32 p[8];
#pragma unroll
                for (int j = 0; j < 8; ++j) p[j] = pk2(__builtin_amdgcn_exp2f(s[2 * j] - m), __builtin_amdgcn_exp2f(s[2 * j + 1] - m));
                const u32 vo_ = (i >> 1) * 5136;
                o = mfma32(asbf8(lds128(((i & 1) ? va[1][0] : va[0][0]) + vo_)), asbf8(u32x4{p[0], p[1], p[2], p[3]}), o);
                o = mfma32(asbf8(lds128(((i & 1) ? va[1][1] : va[0][1]) + vo_)), asbf8(u32x4{p[4], p[5], p[6], p[7]}), o);
            }
        }
        const float rs = __builtin_amdgcn_rcpf(o[12]) * gt;
        const u32 ob = oprod + 20480 + (l31 >> 4) * 40960;
        u32x4 v = {pk2m(o[0] * rs, o[1] * rs), pk2m(o[2] * rs, o[3] * rs), pk2m(o[4] * rs, o[5] * rs), pk2m(o[6] * rs, o[7] * rs)};
        __builtin_amdgcn_raw_buffer_store_b128(v, wsr, ob + c4 * 1024 + (2 * hh + hf) * 256 + l15 * 16, 0, 16);
        u32x2 v2 = {pk2m(o[8] * rs, o[9] * rs), pk2m(o[10] * rs, o[11] * rs)};
        __builtin_amdgcn_raw_buffer_store_b64(v2, wsr, hf ? OOB : ob + 4096 + (2 * c4 + hh) * 128 + l15 * 8, 0, 16);
        cfence();
    }
    STAMP(17);
    STAMPC(58);
    if (!rok) spin(f2o, rws, tag);
    if (wid < 4) {
        if (rok) wait_vm<3 * (9 - HK2) + 2>(); else { recs(); wait_vm<0>(); }
        float s[8];
#pragma unroll
        for (int i = 0; i < 8; ++i) {
            const u32x4 v = lds128(L_RC + wr * 8192 + i * 1024 + l16);
#pragma unroll
            for (int k = 0; k < 4; ++k) { s[2 * k] = i ? s[2 * k] + blo(v[k]) : blo(v[k]); s[2 * k + 1] = i ? s[2 * k + 1] + bhi(v[k]) : bhi(v[k]); }
        }
        float t[4];
#pragma unroll
        for (int k = 0; k < 4; ++k) {
            auto sw = __builtin_amdgcn_permlane32_swap(f2u(s[k]), f2u(s[4 + k]), false, false);
            t[k] = u2f(sw[0]) + u2f(sw[1]);
        }
        sts64(L_REC + wr * 512 + l31 * 16 + hf * 8, u32x2{pk2(t[0], t[1]), pk2(t[2], t[3])});
    } else {
        ctl_wait(L_MC, 10);
        STAMP(23);
        const u32x4 m = lds128(L_REC + l16), n = lds128(L_REC + 1024 + l16);
        const bool m1 = l15 & 4;
        const bf16x8 a0 = asbf8(u32x4{m[0], m[1], g4 == 0 ? n[2] : 0, g4 == 0 ? n[3] : 0});
        const bf16x8 a1 = asbf8(u32x4{m[2], m[3], g4 == 1 ? n[2] : 0, g4 == 1 ? n[3] : 0});
        const bf16x8 x0 = asbf8(u32x4{m1 ? 0 : n[0], m1 ? 0 : n[1], (g4 == 2 && !m1) ? n[2] : 0, (g4 == 2 && !m1) ? n[3] : 0});
        const bf16x8 x1 = asbf8(u32x4{m1 ? n[0] : 0, m1 ? n[1] : 0, (g4 == 3 && m1) ? n[2] : 0, (g4 == 3 && m1) ? n[3] : 0});
#pragma unroll
        for (int t2 = 0; t2 < 2; ++t2) {
            const bf16x8 b0 = asbf8(u32x4{P[t2][0], P[t2][1], P[2][2 * t2], P[2][2 * t2 + 1]});
            const bf16x8 b1 = asbf8(u32x4{P[t2][2], P[t2][3], P[2][2 * t2], P[2][2 * t2 + 1]});
            const f32x4 z = {0, 0, 0, 0};
            const f32x4 xx = mfma16(x1, b1, mfma16(x0, b0, z));
            const f32x4 n0 = mfma16(a0, b0, z), n1 = mfma16(a1, b1, z);
            const u32 rc = f2u(__builtin_amdgcn_rcpf(xx[0] + 1e-6f) * (1.f - (t2 ? g1 : g0)));
            const u32 rs = __builtin_amdgcn_permlane32_swap(rc, rc, false, false)[1];
            auto sw = __builtin_amdgcn_permlane16_swap(rs, rs, false, false);
            const float r0 = u2f(sw[0]), r1 = u2f(sw[1]), rr = u2f(rs);
            const u32 so = oprod + t2 * 40960;
            st128(u32x4{pk2m(n0[0] * r0, n0[1] * r0), pk2m(n0[2] * r0, n0[3] * r0), pk2m(n1[0] * r1, n1[1] * r1), pk2m(n1[2] * r1, n1[3] * r1)}, l16, rws, so + c4 * 1024);
            __builtin_amdgcn_raw_buffer_store_b64(u32x2{pk2m(xx[0] * rr, xx[1] * rr), pk2m(xx[2] * rr, xx[3] * rr)}, wsr, hf ? OOB : lane * 8, so + 4096 + c4 * 256, 16);
        }
        cfence();
    }
    ctl_set(me, 10);
    STAMP(24);
    {
        const u32 vr = lane * 8;
        auto ot = [&](u32 x) { return WS_O + (r64 * 4 + (wr ^ x)) * 40960 + wid * 5120; };
        auto oload = [&](OF& o, u32 s0, u32 s1) {
#pragma unroll
            for (int e = 0; e < 4; ++e) {
                o.m[0][e] = __builtin_amdgcn_raw_buffer_load_b128(wsr, l16 + e * 1024, s0, 16);
                o.m[1][e] = __builtin_amdgcn_raw_buffer_load_b128(wsr, l16 + e * 1024, s1, 16);
            }
#pragma unroll
            for (int h = 0; h < 2; ++h) {
                o.r[0][h] = __builtin_amdgcn_raw_buffer_load_b64(wsr, vr + h * 512, s0 + 4096, 16);
                o.r[1][h] = __builtin_amdgcn_raw_buffer_load_b64(wsr, vr + h * 512, s1 + 4096, 16);
            }
            cfence();
        };
        auto pass = [&](OF& o, f32x4 (&ac)[2][5], u32 s0, u32 s1) {
            u32 nx = 0, bad = 31;
#pragma unroll
            for (int i = 0; i < 10; ++i) asm volatile("" : "=v"(ac[i / 5][i % 5]));
#pragma nounroll
            for (u32 it = 0; it < (1u << 17); ++it) {
                const u32 chk = bad;
                bad = 32;
                sfor<0, 4>([&](auto E) {
                    if (chk & (1u << E.v)) {
                        if (marked(mx4(mx4(0, o.m[0][E.v]), o.m[1][E.v]))) {
                            rl128<E.v * 1024>(o.m[0][E.v], l16, rws, s0); rl128<E.v * 1024>(o.m[1][E.v], l16, rws, s1);
                            bad |= 1u << E.v;
                        }
                    }
                });
                if (marked(umax(umax(umax(o.r[0][0][0], o.r[0][0][1]), umax(o.r[0][1][0], o.r[0][1][1])), umax(umax(o.r[1][0][0], o.r[1][0][1]), umax(o.r[1][1][0], o.r[1][1][1]))))) {
                    rl64<0>(o.r[0][0], vr, rws, s0 + 4096); rl64<0>(o.r[1][0], vr, rws, s1 + 4096);
                    rl64<512>(o.r[0][1], vr, rws, s0 + 4096); rl64<512>(o.r[1][1], vr, rws, s1 + 4096);
                    bad |= 16;
                }
                const u32 lim = __builtin_ctz(bad);
                sfor<0, 5>([&](auto E) {
                    constexpr int e = decltype(E)::v;
                    if (e >= nx && e < lim) {
#pragma unroll
                        for (int t = 0; t < 2; ++t) {
                            const bf16x8 f = asbf8(e < 4 ? o.m[t][e & 3] : u32x4{o.r[t][0][0], o.r[t][0][1], o.r[t][1][0], o.r[t][1][1]});
#pragma unroll
                            for (int c = 0; c < 5; ++c) mfma16b<e == 0>(ac[t][c], W[e * 5 + c], f);
                        }
                    }
                });
                nx = lim;
                if (lim == 5) break;
                of_wait(o);
            }
            mfma_pad();
        };
        f32x4 ac[2][5];
        OF oa, ob;
        const u32 pw = (wid >= 4 ? wr * 20480 : L_K - 5120 + wr * 15360) + l16;
        if (OLATE) ctl_wait8(10);
        STAMP(25);
        oload(oa, ot(2), ot(3));
        oload(ob, ot(0), ot(1));
        of_tie(oa);
        pass(oa, ac, ot(2), ot(3));
        STAMP(18);
        ctl_wait8(10);
#pragma unroll
        for (int i = 0; i < 10; ++i) sts128(pw + 10240 + i * 1024, __builtin_bit_cast(u32x4, ac[i / 5][i % 5]));
        ctl_set(me, 11);
        of_tie(ob);
        pass(ob, ac, ot(0), ot(1));
        STAMP(19);
#pragma unroll
        for (int c = 0; c < 5; ++c) sts128(pw + 5120 + c * 1024, __builtin_bit_cast(u32x4, ac[1][c]));
        if (wid >= 4) {
#pragma unroll
            for (int c = 0; c < 5; ++c) sts128(pw + c * 1024, __builtin_bit_cast(u32x4, ac[0][c]));
            ctl_set(me, 12);
        } else {
            ctl_set(me, 12);
            auto add = [&](u32 a) {
#pragma unroll
                for (int c = 0; c < 5; ++c) ac[0][c] += __builtin_bit_cast(f32x4, lds128(a + l16 + c * 1024));
            };
            ctl_wait(L_MC, 12);
#pragma unroll
            for (u32 x = 1; x < 4; ++x) add(L_K - 5120 + (wr ^ x) * 15360 + x * 5120);
            STAMP(20);
            ctl_wait(L_HQ, 11);
#pragma unroll
            for (u32 x = 2; x < 4; ++x) add((wr ^ x) * 20480 + x * 5120);
            ctl_wait(L_HQ, 12);
            STAMP(21);
#pragma unroll
            for (u32 x = 0; x < 2; ++x) add((wr ^ x) * 20480 + x * 5120);
            u32 r[10];
#pragma unroll
            for (int c = 0; c < 5; ++c) { r[2 * c] = pk2(ac[0][c][0], ac[0][c][1]); r[2 * c + 1] = pk2(ac[0][c][2], ac[0][c][3]); }
            const __amdgpu_buffer_rsrc_t ors = __builtin_amdgcn_make_buffer_rsrc(out, 0, 2048 * 1280, 0x00020000);
            const u32 vo = l15 * 1280 + g4 * 40 + (r64 * 64 + wr * 16) * 1280 + c80 * 160;
            __builtin_amdgcn_raw_buffer_store_b128(u32x4{r[0], r[1], r[2], r[3]}, ors, vo, 0, 0);
            __builtin_amdgcn_raw_buffer_store_b128(u32x4{r[4], r[5], r[6], r[7]}, ors, vo + 16, 0, 0);
            __builtin_amdgcn_raw_buffer_store_b64(u32x2{r[8], r[9]}, ors, vo + 32, 0, 0);
        }
    }
    STAMP(22);
    STAMPC(63);
    asm volatile(".Lcode_hi:");
}

extern "C" void kernel_launch(void* const* d_in, const int* in_sizes, int n_in, void* d_out, int out_size, void* d_ws,
                              size_t ws_size, hipStream_t stream) {
    (void)in_sizes; (void)n_in; (void)out_size; (void)ws_size;
    (void)hipFuncSetAttribute((const void*)fused_kernel, hipFuncAttributeMaxDynamicSharedMemorySize, LDS_BYTES);
    fused_kernel<<<256, 512, LDS_BYTES, stream>>>((const u16*)d_in[0], (const u16*)d_in[1], (const u16*)d_in[2], (const u16*)d_in[4],
                                                  (const u16*)d_in[5], (const u16*)d_in[6], (u16*)d_out, (char*)d_ws);
}
```

```cpp
#include <hip/hip_runtime.h>
#include <stdint.h>
#pragma clang diagnostic ignored "-Wint-to-pointer-cast"

#define SPINS 1
#define PRIO 3
typedef unsigned short u16;
typedef unsigned int u32;
typedef unsigned long long u64;
typedef __attribute__((ext_vector_type(8))) __bf16 bf16x8;
typedef __attribute__((ext_vector_type(4))) __bf16 bf16x4;
typedef __attribute__((ext_vector_type(2))) __bf16 bf16x2;
typedef __attribute__((ext_vector_type(2))) float f32x2;
typedef __attribute__((ext_vector_type(4))) float f32x4;
typedef __attribute__((ext_vector_type(16))) float f32x16;
typedef __attribute__((ext_vector_type(4))) u32 u32x4;
typedef __attribute__((ext_vector_type(2))) u32 u32x2;
typedef __attribute__((ext_vector_type(4))) short s16x4;
#define AS3 __attribute__((address_space(3)))
#define DEVI __device__ __forceinline__

constexpr u32 WS_E   = 0;
constexpr u32 WS_FS  = 4096;
constexpr u32 WS_F2  = 12288;
constexpr u32 WS_FQ  = 8192;
constexpr u32 WS_FT  = 16384;
constexpr u32 WS_T   = 81920;
constexpr u32 WS_KVP = WS_T + 2 * 16 * 64 * 2560;
constexpr u32 WS_O   = WS_KVP + 16 * 16 * 2048;
constexpr u32 WS_WO  = WS_O + 128 * 40960;
constexpr u32 WS_WQ  = WS_WO + 16 * 102400;
constexpr u32 WS_END = WS_WQ + 16 * 102400;
constexpr u32 OOB = 0x80000000u;

constexpr u32 WREG  = 5152;
constexpr u32 XO    = 20608;
constexpr u32 SLOT  = 36992;
__device__ constexpr u32 SL[4] = {0, 3 * SLOT, 2 * SLOT, SLOT};
constexpr u32 L_SCR = 4 * SLOT;
constexpr u32 L_GW  = L_SCR + 8192;
constexpr u32 L_DUMP = 163328;
constexpr u32 L_REC = 0;
constexpr u32 L_XQ  = 2048;
constexpr u32 L_QT  = 3 * SLOT;
constexpr u32 L_K   = 101376;
constexpr u32 L_V   = 132096;
constexpr u32 L_RC  = 8192;
constexpr u32 L_HQ  = 162944, L_MC = L_HQ + 16, L_TAG = L_HQ + 32;
constexpr u32 LDS_BYTES = 163840;
static_assert(L_GW + 2048 <= L_HQ && L_DUMP >= L_TAG + 4 && L_DUMP + 256 <= LDS_BYTES && L_V + 6 * 5136 <= L_HQ && L_XQ + 6144 <= L_QT - 102400, "lds");

constexpr float LOG2E = 1.4426950408889634f;
constexpr float QSCALE = 0.22360679774997896f * 1.4426950408889634f;
constexpr u32 TAGC = 0x7F4A7C15u;
constexpr u32 ONES = 0x3F803F80u;

DEVI u32 pk2(float a, float b) { f32x2 v = {a, b}; bf16x2 r = __builtin_convertvector(v, bf16x2); return __builtin_bit_cast(u32, r); }
DEVI u32 f2u(float f) { return __builtin_bit_cast(u32, f); }
DEVI float u2f(u32 u) { return __builtin_bit_cast(float, u); }
DEVI float blo(u32 u) { return u2f(u << 16); }
DEVI float bhi(u32 u) { return u2f(u & 0xFFFF0000u); }
DEVI u32 umin(u32 a, u32 b) { return a < b ? a : b; }
DEVI u32 umax(u32 a, u32 b) { return a > b ? a : b; }
DEVI u32 pk2m(float a, float b) { return umin(pk2(a, b), 0xFFFFFFFEu); }
DEVI bf16x8 asbf8(u32x4 v) { return __builtin_bit_cast(bf16x8, v); }
DEVI f32x4 mfma16(bf16x8 a, bf16x8 b, f32x4 c) { return __builtin_amdgcn_mfma_f32_16x16x32_bf16(a, b, c, 0, 0, 0); }
template <bool FIRST = false> DEVI void mfma16a(f32x4& c, bf16x8 a, bf16x8 b) {
    if constexpr (FIRST) asm volatile("v_mfma_f32_16x16x32_bf16 %0, %1, %2, 0" : "=&v"(c) : "v"(a), "v"(b));
    else asm volatile("v_mfma_f32_16x16x32_bf16 %0, %1, %2, %0" : "+v"(c) : "v"(a), "v"(b));
}
template <bool FIRST> DEVI void mfma16b(f32x4& c, bf16x8 a, bf16x8 b) {
    if constexpr (FIRST) asm volatile("v_mfma_f32_16x16x32_bf16 %0, %1, %2, 0" : "+v"(c) : "v"(a), "v"(b));
    else asm volatile("v_mfma_f32_16x16x32_bf16 %0, %1, %2, %0" : "+v"(c) : "v"(a), "v"(b));
}
DEVI void mfma_pad() { __builtin_amdgcn_sched_barrier(0); asm volatile("s_nop 7\n\ts_nop 3" ::: "memory"); __builtin_amdgcn_sched_barrier(0); }
DEVI f32x16 mfma32(bf16x8 a, bf16x8 b, f32x16 c) { return __builtin_amdgcn_mfma_f32_32x32x16_bf16(a, b, c, 0, 0, 0); }
DEVI f32x16 mfma32k8(bf16x4 a, bf16x4 b, f32x16 c) {
    return __builtin_amdgcn_mfma_f32_32x32x8bf16_1k(__builtin_bit_cast(s16x4, a), __builtin_bit_cast(s16x4, b), c, 0, 0, 0);
}
DEVI bf16x4 ldtr(u32 a) { return __builtin_amdgcn_ds_read_tr16_b64_v4bf16((AS3 bf16x4*)a); }
DEVI bf16x8 cat8(bf16x4 x, bf16x4 y) { return __builtin_shufflevector(x, y, 0, 1, 2, 3, 4, 5, 6, 7); }
DEVI bf16x8 ldtr2(u32 a, u32 b) { bf16x4 x = ldtr(a), y = ldtr(b); return cat8(x, y); }
DEVI u32x4 lds128(u32 a) { return *(AS3 u32x4*)a; }
DEVI u32x2 lds64(u32 a) { return *(AS3 u32x2*)a; }
DEVI u32 lds32(u32 a) { return *(AS3 u32*)a; }
DEVI void sts128(u32 a, u32x4 v) { *(AS3 u32x4*)a = v; }
DEVI void sts64(u32 a, u32x2 v) { *(AS3 u32x2*)a = v; }
DEVI void sts32(u32 a, u32 v) { *(AS3 u32*)a = v; }
DEVI u32 rfl(u32 v) { return __builtin_amdgcn_readfirstlane(v); }
DEVI u32 opaque_s(u32 v) { asm volatile("" : "+s"(v)); return v; }
DEVI u32 opaque_v(u32 v) { asm volatile("" : "+v"(v)); return v; }
DEVI void undef(u32x4& v) { asm volatile("" : "=v"(v)); }
DEVI void cfence() { asm volatile("" ::: "memory"); }

DEVI u32x4 mkrs(const void* p, u32 bytes) { return u32x4{(u32)(uintptr_t)p, (u32)((uintptr_t)p >> 32) & 0xFFFFu, bytes, 0x00020000u}; }
template <int MO, int OFF = 0> DEVI void dma(u32 voff, u32x4 rs, u32 soff, u32 mb) {
    asm volatile("s_add_u32 m0, %3, %5\n\ts_nop 0\n\tbuffer_load_dwordx4 %0, %1, %2 offen offset:%4 lds"
                 :: "v"(voff), "s"(rs), "s"(soff), "s"(mb), "i"(OFF), "i"(MO) : "memory", "scc");
}
template <int MO, int OFF = 0> DEVI void dmac(u32 voff, u32x4 rs, u32 soff, u32 mb) {
    asm volatile("s_add_u32 m0, %3, %5\n\ts_nop 0\n\tbuffer_load_dwordx4 %0, %1, %2 offen offset:%4 sc1 lds"
                 :: "v"(voff), "s"(rs), "s"(soff), "s"(mb), "i"(OFF), "i"(MO) : "memory", "scc");
}
DEVI void pref(u32 voff, u32x4 rs, u32 soff) {
    asm volatile("s_mov_b32 m0, %3\n\ts_nop 0\n\tbuffer_load_dword %0, %1, %2 offen sc1 lds" :: "v"(voff), "s"(rs), "s"(soff), "i"(L_DUMP) : "memory");
}
DEVI u32 ldflag(u32 voff, u32x4 rs) {
    u32 v;
    asm volatile("buffer_load_dword %0, %1, %2, 0 offen sc1" : "=v"(v) : "v"(voff), "s"(rs) : "memory");
    return v;
}
DEVI u32x4 ldflag4(u32 voff, u32x4 rs) {
    u32x4 v;
    asm volatile("buffer_load_dwordx4 %0, %1, %2, 0 offen sc1" : "=v"(v) : "v"(voff), "s"(rs) : "memory");
    return v;
}
DEVI void stflag(u32 val, u32 soff, u32x4 rs) {
    asm volatile("s_mov_b64 exec, 1\n\tbuffer_store_dword %0, off, %1, %2 sc1\n\ts_mov_b64 exec, -1" :: "v"(val), "s"(rs), "s"(soff) : "memory");
}
template <int OFF = 0> DEVI void st128(u32x4 v, u32 voff, u32x4 rs, u32 soff) {
    asm volatile("buffer_store_dwordx4 %0, %1, %2, %3 offen offset:%4 sc1\n\ts_nop 1" :: "v"(v), "v"(voff), "s"(rs), "s"(soff), "i"(OFF) : "memory");
}
template <int OFF = 0> DEVI void st128n(u32x4 v, u32 voff, u32x4 rs, u32 soff) {
    asm volatile("buffer_store_dwordx4 %0, %1, %2, %3 offen offset:%4\n\ts_nop 1" :: "v"(v), "v"(voff), "s"(rs), "s"(soff), "i"(OFF) : "memory");
}
template <int N> DEVI void wait_vm() { asm volatile("s_waitcnt vmcnt(%0)" ::"i"(N) : "memory"); }
template <int N> DEVI u32 wait_rfl(u32 v) { u32 r; asm volatile("s_waitcnt vmcnt(%2)\n\tv_readfirstlane_b32 %0, %1" : "=s"(r) : "v"(v), "i"(N) : "memory"); return r; }
template <int N> DEVI bool wait_eq(u32 v, u32 tag) {
    u64 m; asm volatile("s_waitcnt vmcnt(%3)\n\tv_cmp_ne_u32_e64 %0, %2, %1" : "=s"(m) : "v"(v), "s"(tag), "i"(N) : "memory");
    return m == 0ull;
}
template <int N> DEVI bool wait_eq4(u32x4 v, u32 tag) {
    asm volatile("s_waitcnt vmcnt(%1)" : "+v"(v) : "i"(N) : "memory");
    return __builtin_amdgcn_ballot_w64(((v[0] ^ tag) | (v[1] ^ tag) | (v[2] ^ tag) | (v[3] ^ tag)) != 0u) == 0ull;
}
template <int N> DEVI void wait_lgkm() { asm volatile("s_waitcnt lgkmcnt(%0)" ::"i"(N) : "memory"); }
DEVI void bar() { asm volatile("s_barrier" ::: "memory"); }
DEVI void sched0() { __builtin_amdgcn_sched_barrier(0); }
DEVI void spin(u32 voff, u32x4 rs, u32 tag) {
#pragma nounroll
    for (u32 i = 0; i < (1u << 17); ++i) {
        u32 v = ldflag(voff, rs);
        if (wait_eq<0>(v, tag)) break;
        __builtin_amdgcn_s_sleep(SPINS);
    }
}
DEVI void ctl_set(u32 a, u32 v) { asm volatile("s_mov_b64 exec, 1\n\tds_write_b32 %0, %1\n\ts_mov_b64 exec, -1" :: "v"(a), "v"(v) : "memory"); }
DEVI u32x4 ctl_get(u32 a) { return *(volatile AS3 u32x4*)a; }
DEVI bool ctl_ge(u32x4 v, u32 t) { return rfl(umin(umin(v[0], v[1]), umin(v[2], v[3]))) >= t; }
DEVI void ctl_wait(u32 a, u32 t) {
#pragma nounroll
    for (u32 i = 0; i < (1u << 20); ++i) { if (ctl_ge(ctl_get(a), t)) break; __builtin_amdgcn_s_sleep(1); }
    cfence();
}
DEVI void ctl_wait8(u32 t) {
#pragma nounroll
    for (u32 i = 0; i < (1u << 20); ++i) {
        const u32x4 x = ctl_get(L_HQ), y = ctl_get(L_MC);
        if (ctl_ge(u32x4{umin(x[0], y[0]), umin(x[1], y[1]), umin(x[2], y[2]), umin(x[3], y[3])}, t)) break;
        __builtin_amdgcn_s_sleep(1);
    }
    cfence();
}
DEVI void ctl_wait1(u32 a, u32 t) {
#pragma nounroll
    for (u32 i = 0; i < (1u << 20); ++i) { if (rfl(*(volatile AS3 u32*)a) >= t) break; __builtin_amdgcn_s_sleep(1); }
    cfence();
}
DEVI bool marked(u32 m) { return __builtin_amdgcn_ballot_w64(m == 0xFFFFFFFFu) != 0ull; }
DEVI u32 mx4(u32 m, u32x4 v) { return umax(umax(umax(m, v[0]), v[1]), umax(v[2], v[3])); }

DEVI float phi(float t) { return __builtin_amdgcn_fmed3f(t + 1.f, __builtin_amdgcn_exp2f(t * LOG2E), 1.f) + 1e-6f; }
DEVI u32 phi2(float a, float b) { return pk2(phi(a), phi(b)); }
struct OF { u32x4 m[2][4]; u32x2 r[2][2]; };
template <int OFF> DEVI void ul128(u32x4& v, u32 voff, u32x4 rs, u32 soff) {
    asm volatile("buffer_load_dwordx4 %0, %1, %2, %3 offen offset:%4 sc1" : "=v"(v) : "v"(voff), "s"(rs), "s"(soff), "i"(OFF) : "memory");
}
template <int OFF> DEVI void rl128(u32x4& v, u32 voff, u32x4 rs, u32 soff) {
    asm volatile("buffer_load_dwordx4 %0, %1, %2, %3 offen offset:%4 sc1" : "+v"(v) : "v"(voff), "s"(rs), "s"(soff), "i"(OFF) : "memory");
}
DEVI u32x4 ld128(u32 voff, u32x4 rs, u32 soff) {
    u32x4 v;
    asm volatile("buffer_load_dwordx4 %0, %1, %2, %3 offen" : "=v"(v) : "v"(voff), "s"(rs), "s"(soff) : "memory");
    return v;
}
template <int OFF> DEVI void rl64(u32x2& v, u32 voff, u32x4 rs, u32 soff) {
    asm volatile("buffer_load_dwordx2 %0, %1, %2, %3 offen offset:%4 sc1" : "+v"(v) : "v"(voff), "s"(rs), "s"(soff), "i"(OFF) : "memory");
}
#define OF_OPS(o) "+v"(o.m[0][0]), "+v"(o.m[0][1]), "+v"(o.m[0][2]), "+v"(o.m[0][3]), "+v"(o.m[1][0]), "+v"(o.m[1][1]), "+v"(o.m[1][2]), "+v"(o.m[1][3]), \
                  "+v"(o.r[0][0]), "+v"(o.r[0][1]), "+v"(o.r[1][0]), "+v"(o.r[1][1])
DEVI void of_wait(OF& o) { asm volatile("s_waitcnt vmcnt(0)" : OF_OPS(o) :: "memory"); }
DEVI void of_tie(OF& o) { asm volatile("" : OF_OPS(o) :: "memory"); }

#define STAMP(i)
#define STAMPC(i)

template <int N> struct IC { static constexpr int v = N; };
template <int I, int N, class F> DEVI void sfor(F&& f) { if constexpr (I < N) { f(IC<I>{}); sfor<I + 1, N>(f); } }

#define ROTA 1
#define ROTB 3
#define UPTO {2, 4, 5, 6, 7, 8, 9, 10, 10, 10, 10}
#define HK1 3
#define HK2 5
#define HA 4
#define HB 6
#define XPM 0
#define HPRIO 3
#define QPRIO 0
#define PFQ -1
#define PFO 0
#define OLATE 1
#define FQS 1
#define CPF 0
#define ASM 0
#define FSB 6
#define HWN 200
#define HWS 1
#define PFW 0
#define WU 0
#define XNOW 0
constexpr u64 lem(int k) { u64 m = 0; for (int l = 0; l < 64; ++l) if (k + 4 * (l >> 5) <= (l & 31)) m |= 1ull << l; return m; }
__device__ constexpr int UP[11] = UPTO;
__global__ __launch_bounds__(512, 2) void fused_kernel(const u16* __restrict__ x, const u16* __restrict__ wa, const u16* __restrict__ woa,
                                                       const u16* __restrict__ wb, const u16* __restrict__ wob, const u16* __restrict__ gw,
                                                       u16* out, char* __restrict__ ws) {
    const u32 lane = threadIdx.x & 63;
    const u32 wid = rfl(threadIdx.x >> 6);
    const u32 b = blockIdx.x, xcd = b & 7, idx = b >> 3;
    STAMP(0);
    STAMPC(62);
    if (wid == 3) {
        u64 pc; u32 len;
        asm volatile("s_getpc_b64 %0\n.Lcode_lo:\n\ts_mov_b32 %1, .Lcode_hi-.Lcode_lo\n\ts_max_u32 %1, %1, .Lcode_hm-.Lcode_lo" : "=s"(pc), "=s"(len) :: "scc");
        u32 o = (idx & 15) * 4096 + lane * 64;
        o = o < len ? o : 0;
        asm volatile("s_mov_b32 m0, %2\n\ts_nop 0\n\tglobal_load_lds_dword %0, %1" :: "v"(o), "s"(pc), "i"(L_DUMP) : "memory");
    }
    const u32 rb = (xcd & 1) * 8 + (idx >> 2), c4 = idx & 3, sg = xcd >> 1;
    const u32 ty = c4 >> 1;
    const u32 hg = ty ? 2 * sg + (c4 & 1) : sg + 4 * (c4 & 1);
    const u32 hp = 4 * sg + c4;
    const u32 wr = wid & 3;
    const u32x4 rws = mkrs(ws, WS_END);
    const __amdgpu_buffer_rsrc_t wsr = __builtin_amdgcn_make_buffer_rsrc(ws, 0, WS_END, 0x00020000);
    const u32 l15 = lane & 15, g4 = lane >> 4, l31 = lane & 31, hf = lane >> 5, l16 = lane * 16;
    const u32 me = (wid >= 4 ? L_HQ : L_MC) + wr * 4;
    const u32 oprod = WS_O + (rb * 8 + 2 * wr) * 40960 + sg * 5120;
    u32x4 qf; u32x2 qr; float gt;
    u32x4 P[3]; float g0, g1;
    u32 tag;
    const u32 r64 = xcd * 4 + (idx >> 3), c80 = idx & 7;
    u32 f2o;
    u32x4 W[25];
    bool wok;
    const u32 fso = WS_FS + (lane >> 2) * 256 + (lane & 3) * 16;
    auto wld = [&](auto I, u32 vw) __attribute__((always_inline)) { ul128<(I.v & 3) * 1024>(W[I.v], vw, rws, WS_WO + c80 * 204800 + wid * 25600 + (I.v >> 2) * 4096); };
    auto wlate = [&]() __attribute__((always_inline)) {
        if (!wok) {
#pragma nounroll
            for (u32 i = 0; i < (1u << 17); ++i) { if (wait_eq4<0>(ldflag4(fso, rws), tag)) break; __builtin_amdgcn_s_sleep(SPINS); }
            sfor<0, 25>([&](auto I) { rl128<(I.v & 3) * 1024>(W[I.v], l16, rws, WS_WO + c80 * 204800 + wid * 25600 + (I.v >> 2) * 4096); });
        }
    };
    u32 ka, kc, kr, va[2][2], mk[8];
    auto asetup = [&](u32 hh) __attribute__((always_inline)) {
        const u32 ch = 2 * (g4 & 1) + (lane & 1), kb = L_K + wr * 2560 + ((lane >> 1) & 1) * 8;
        ka = kb + hh * 1024 + ch * 256 + (((8 * hf + (l15 >> 2)) ^ (4 * ch)) * 16); kc = ka ^ 64;
        kr = kb + 2048 + ch * 128 + ((hh ^ (ch >> 1)) * 64) + (l15 >> 2) * 16;
#pragma unroll
        for (int p = 0; p < 2; ++p)
#pragma unroll
            for (int m = 0; m < 2; ++m) {
                const u32 bi = wr + p, c = 2 * m + hf;
                va[p][m] = L_V + (bi >> 1) * 5136 + (l31 < 20 ? (bi & 1) * 2560 + (l31 < 16 ? hh * 1024 + c * 256 + ((l31 ^ (4 * c)) * 16) : 2048 + c * 128 + ((hh ^ m) * 64) + (l31 - 16) * 16) : 5120);
            }
        sfor<0, 8>([&](auto J) {
            constexpr int k0 = ((2 * J.v) & 3) + 8 * ((2 * J.v) >> 2);
            mk[J.v] = (__builtin_amdgcn_inverse_ballot_w64(lem(k0)) ? 0xFFFFu : 0u) | (__builtin_amdgcn_inverse_ballot_w64(lem(k0 + 1)) ? 0xFFFF0000u : 0u);
        });
        f2o = WS_F2 + ((lane & 16) ? (r64 >> 1) * 256 + l15 * 4 : l15 * 256 + ((hp & 12) + 2 + ((hp >> 1) & 1)) * 4);
        asm volatile("" : "+v"(ka), "+v"(kc), "+v"(kr), "+v"(va[0][0]), "+v"(va[0][1]), "+v"(va[1][0]), "+v"(va[1][1]), "+v"(f2o), "+s"(out));
        asm volatile("" : "+v"(mk[0]), "+v"(mk[1]), "+v"(mk[2]), "+v"(mk[3]), "+v"(mk[4]), "+v"(mk[5]), "+v"(mk[6]), "+v"(mk[7]));
    };

    if (__builtin_expect(wid >= 4, 1)) {
        const u32 rot = (ROTA * rb + ROTB * c4) % 10;
        auto kbof = [&](u32 s) { u32 k = rot + s; return k >= 10 ? k - 10 : k; };
        const u32x4 rsx = mkrs(x, 2048 * 1280), rsw = mkrs(ty ? wb : wa, 640 * 3840);
        const u32 vx = (rb * 128 + 32 * wr + (lane >> 3)) * 1280 + (((lane & 7) ^ ((lane >> 3) & 6)) * 16);
        u32 vo[5];
#pragma unroll
        for (int i = 0; i < 5; ++i) {
            const u32 ch = 64 * i + lane, pr = (ch * 205) >> 12, j = ch - pr * 20;
            const u32 k = 32 * (wr >> 1) + 8 * (wr & 1) + (pr & 7) + 2 * (pr & 8);
            vo[i] = k * 3840 + (ty ? 1280 + 160 * hg + (j >= 10 ? 1120 : 0) : 1280 + 320 * hg) + j * 16;
        }
        auto issuep = [&](auto SC, auto A, auto B) {
            constexpr int s = decltype(SC)::v;
            const u32 kb = kbof(s);
            sfor<A.v, B.v>([&](auto I) {
                if constexpr (I.v < 4) dma<SL[s % 4] + XO + I.v * 1024>(vx, rsx, kb * 128 + I.v * 10240, wr * 4096);
                else dma<SL[s % 4] + (I.v - 4) * 1024>(vo[I.v - 4], rsw, kb * 245760, wr * WREG);
            });
        };
        auto issue = [&](auto SC) { issuep(SC, IC<0>{}, IC<9>{}); };
        STAMP(30);
        issue(IC<0>{});
        STAMP(31);
        {
            const u32 o = l16 + (wr & 1) * 1024 + rot * 128;
            dma<L_GW>(o >= 1280 ? o - 1280 : o, mkrs(gw, 1280), 0, (wr & 1) * 1024);
        }
        sfor<1, UP[0]>(issue);
        STAMP(1);
        const u32 xb0 = XO + (32 * wr + l15) * 128 + ((g4 ^ (l15 & 6)) * 16), xb1 = xb0 ^ 64;
        const u32 xc0 = xb0 + 2 * SLOT, xc1 = xb1 + 2 * SLOT, ga = L_GW + g4 * 16;
        const u32 qm0 = (wr & 1) * 5120 + (1 - (wr >> 1)) * 10240, qs0 = WS_WQ + hp * 102400 + (wr & 1) * 5120;
        auto qissue = [&](auto JC) {
            constexpr int jj = decltype(JC)::v;
            const u32 so = qs0 + kbof(2 * jj + (wr >> 1)) * 10240;
            sfor<0, 4>([&](auto I) { dmac<L_QT - 20480 * (jj + 1), I.v * 1024>(l16, rws, so, qm0); });
            dmac<L_QT - 20480 * (jj + 1) + 4096>(l16, rws, so + 4096, qm0);
        };
        const u32 fqo = WS_FQ + sg * 1024 + g4 * 256 + l15 * 4;
        bf16x8 xf[10][2][2];
        f32x4 gacc[2];
        u32 pv = 0;
        bool qok = true;
        auto wojob = [&](auto ST) {
#pragma unroll
            for (u32 j = 0; j < 2; ++j) {
                const u32 p = j * 1024 + b * 4 + wr;
                const bool br = p >= 800;
                const u32 q = br ? p - 800 : p, oct = q / 10, cl = q - oct * 10;
                const u32 kx = oct >> 2, go = oct & 3, G = kx / 5, e = kx - G * 5;
                if constexpr (!ST.v) {
                    const u32 r0 = 160 * G + (e == 4 ? 20 * go + 16 : 40 * e + (br ? 20 * (go >> 1) + 4 * (go & 1) : 4 * go));
                    const u32 step = e == 4 ? 80 : br ? 8 : 20;
                    dma<L_SCR>(p < 1600 ? (((lane >> 3) & 3) + step * hf) * 1280 + (lane & 7) * 16 : OOB, mkrs(br ? woa : wob, 640 * 1280),
                               p < 1600 ? r0 * 1280 + cl * 128 : 0, wr * 2048 + j * 1024);
                } else {
                    const u32 n = 64 * cl + lane, h = (n * 205) >> 12, d = n - 20 * h;
                    const u32 jd = p < 1600 ? WS_WO + ((h >> 2) * 2 + br) * 102400 + (kx * 5 + (d >> 2)) * 1024 + (go * 16 + 4 * (h & 3) + (d & 3)) * 16 : OOB;
                    const u32 js = L_SCR + wr * 2048 + j * 1024 + (l15 >> 2) * 128 + (4 * g4 + (lane & 3)) * 8;
                    st128(__builtin_bit_cast(u32x4, ldtr2(js, js + 512)), jd, rws, 0);
                }
            }
        };
        constexpr int XA[10] = {0, 2, 0, 0, 2, 0, 1, 0, 0, 0}, XB[10] = {0, 0, 0, PFQ == 3, PFQ == 4, PFQ == 5, 0, 0, 0, 0};
        sfor<0, 10>([&](auto SC) {
            constexpr int s = decltype(SC)::v;
            wait_lgkm<0>();
            constexpr int ex = s < 2 ? 0 : s < 4 ? XA[1] : XB[s - 3] + XA[s - 2] + XB[s - 2] + XA[s - 1] + XB[s - 1];
            if constexpr (s < 8) wait_vm<9 * (UP[s] - s - 1) + ex - (s == FSB && s < 7 ? XA[4] : 0)>();
            if constexpr (s == 9) { if (qok) wait_vm<15>(); else wait_vm<0>(); }
            bar();
            if constexpr (s == 0) __builtin_amdgcn_s_setprio(HPRIO);
            STAMP(2 + s);
            if constexpr (s == 0 || s == 2 || s == 4 || s == 7) STAMPC(s == 0 ? 51 : s == 2 ? 52 : s == 4 ? 53 : 54);
            constexpr bool up = SL[s % 4] >= 2 * SLOT, any = UP[s + 1] > UP[s];
            constexpr u32 xo = SL[s % 4] - (up ? 2 * SLOT : 0);
            const u32 a0 = up ? xc0 : xb0, a1 = up ? xc1 : xb1;
            if constexpr (s == 1) wojob(IC<0>{});
            if constexpr (s == 4) wojob(IC<1>{});
            if constexpr (s == 5) tag = rfl(lds32(L_TAG));
            if constexpr (s == 6) pv = ldflag(fqo, rws);
            if constexpr (s == 7) { qok = wait_eq<9>(pv, tag); if (qok) qissue(IC<0>{}); }
            if constexpr (s == 8) { if (qok) qissue(IC<1>{}); }
            if constexpr (s == 9) { if (qok) qissue(IC<3>{}); }
            if constexpr (any) issuep(IC<UP[s]>{}, IC<0>{}, IC<HA>{});
            xf[s][0][0] = asbf8(lds128(a0 + xo)); xf[s][0][1] = asbf8(lds128(a0 + xo + 2048));
            xf[s][1][0] = asbf8(lds128(a1 + xo)); xf[s][1][1] = asbf8(lds128(a1 + xo + 2048));
            const bf16x8 w0 = asbf8(lds128(ga + s * 128)), w1 = asbf8(lds128(ga + s * 128 + 64));
            if constexpr (any) issuep(IC<UP[s]>{}, IC<HA>{}, IC<HB>{});
            mfma16a<s == 0>(gacc[0], w0, xf[s][0][0]); mfma16a<s == 0>(gacc[1], w0, xf[s][0][1]);
            mfma16a(gacc[0], w1, xf[s][1][0]); mfma16a(gacc[1], w1, xf[s][1][1]);
            if constexpr (any) { issuep(IC<UP[s]>{}, IC<HB>{}, IC<9>{}); sfor<UP[s] + 1, UP[s + 1]>(issue); }
            if constexpr (s == PFQ) pref(lane < 25 ? lane * 128 : OOB, rws, WS_WQ + sg * 409600 + (idx * 4 + wr) * 3200);
            if constexpr (s == 8) { if (qok) qissue(IC<2>{}); }
            if constexpr (s == 9) { if (qok) qissue(IC<4>{}); }
        });
        __builtin_amdgcn_s_setprio(QPRIO);
        if (!qok) { spin(fqo, rws, tag); sfor<0, 5>(qissue); }
        asm volatile("s_nop 7\n\ts_nop 3" : "+v"(gacc[0]), "+v"(gacc[1]));
        wait_vm<20 + 2 * PFO>();
        ctl_set(me, 1);
        const u32 lhi = opaque_v(l16 + L_QT - 51200);
        ctl_wait(L_HQ, 1);
        STAMP(12);
        STAMPC(55);
        constexpr int PD = 4;
        bf16x8 wf[PD + 1];
        f32x4 qa[2][5];
        u32x4 snap, fs;
        u32 vw;
        auto wadr = [&](int n) {
            const int j = n / 10;
            return (j < 5 ? lhi + 40960 - 10240 * j : l16 + L_QT - 10240 * (j + 1)) + (n % 10) * 1024;
        };
        sfor<0, PD>([&](auto NC) { wf[NC.v] = asbf8(lds128(wadr(NC.v))); });
        sfor<0, 100>([&](auto NC) {
            constexpr int n = decltype(NC)::v, m = n + PD;
            constexpr int st = n / 10, ks = (n % 10) / 5, a = n % 5;
            const bf16x8 w = wf[n % (PD + 1)];
            if constexpr (m < 100 && m % 20 == 0) { if (!ctl_ge(snap, m / 20 + 1)) ctl_wait(L_HQ, m / 20 + 1); cfence(); }
            sched0();
            mfma16a<(n < 5)>(qa[0][a], w, xf[st][ks][0]);
            sched0();
            if constexpr (m < 100) wf[m % (PD + 1)] = asbf8(lds128(wadr(m)));
            sched0();
            mfma16a<(n < 5)>(qa[1][a], w, xf[st][ks][1]);
            sched0();
            if constexpr (n < 80 && n % 20 == 8) {
                wait_vm<15 + 2 * PFO - 5 * (n / 20)>();
                ctl_set(me, n / 20 + 2);
            }
            if constexpr (n < 80 && n % 20 == 12) snap = ctl_get(L_HQ);
            if constexpr (n == 50 && HWN < 200) fs = ldflag4(fso, rws);
            if constexpr (n == 69 && HWN < 200) { wok = wait_eq4<0>(fs, tag); vw = wok ? l16 : OOB; }
            if constexpr (n >= HWN && (n - HWN) % HWS == 0 && (n - HWN) / HWS < 25) wld(IC<(n - HWN) / HWS>{}, vw);
            sched0();
        });
        constexpr int HW0 = HWN >= 100 ? 0 : (100 - HWN + HWS - 1) / HWS;
        if constexpr (HWN < 200) sfor<HW0, 25>([&](auto I) { wld(I, vw); });
        else sfor<0, 25>([&](auto I) { undef(W[I.v]); });
        mfma_pad();
        STAMP(13);
        STAMPC(56);
        g0 = __builtin_amdgcn_rcpf(1.f + __builtin_amdgcn_exp2f(-gacc[0][0] * LOG2E));
        g1 = __builtin_amdgcn_rcpf(1.f + __builtin_amdgcn_exp2f(-gacc[1][0] * LOG2E));
        gt = (lane & 16) ? g1 : g0;
        {
            auto cvt = [&](int a, u32x4& o) {
                auto s0 = __builtin_amdgcn_permlane16_swap(pk2(qa[0][a][0] * QSCALE, qa[0][a][1] * QSCALE), pk2(qa[1][a][0] * QSCALE, qa[1][a][1] * QSCALE), false, false);
                auto s1 = __builtin_amdgcn_permlane16_swap(pk2(qa[0][a][2] * QSCALE, qa[0][a][3] * QSCALE), pk2(qa[1][a][2] * QSCALE, qa[1][a][3] * QSCALE), false, false);
                o = u32x4{s0[0], s1[0], s0[1], s1[1]};
            };
            u32x4 r, q1;
            cvt(4, q1); cvt(2, r);
            sts128(L_XQ + wr * 1536 + l16, q1);
            sts64(L_XQ + wr * 1536 + 1024 + lane * 8, u32x2{hf ? f2u(gt) : r[2], r[3]});
            ctl_set(me, 6);
            cvt(3, qf);
            qr = hf ? u32x2{0, 0} : u32x2{r[0], r[1]};
        }
#pragma unroll
        for (int t = 0; t < 2; ++t) {
            P[t] = u32x4{phi2(qa[t][0][0], qa[t][0][1]), phi2(qa[t][0][2], qa[t][0][3]), phi2(qa[t][1][0], qa[t][1][1]), phi2(qa[t][1][2], qa[t][1][3])};
            const u32 u = phi2(qa[t][2][0], qa[t][2][1]), v = phi2(qa[t][2][2], qa[t][2][3]);
            P[2][2 * t] = __builtin_amdgcn_permlane32_swap(u, u, false, false)[1];
            P[2][2 * t + 1] = __builtin_amdgcn_permlane32_swap(v, v, false, false)[1];
        }
        asm volatile("" : "+v"(P[0]), "+v"(P[1]), "+v"(P[2]), "+v"(qf), "+v"(qr));
        asetup(0);
        if constexpr (HWN < 200) wlate();
        STAMP(14);
        __builtin_amdgcn_s_setprio(0);
        ctl_wait(L_MC, 3);
        STAMP(16);
        STAMPC(57);
    } else {
        const u32 wm = wr >> 1, wn = wr & 1;
        const u32 ev = ldflag(0, rws);
        const u32 pw0 = (xcd & 1) * 128 + idx * 4 + wr;
        u32 qd[2];
#pragma unroll
        for (u32 j = 0; j < 2; ++j) {
            const u32 p = pw0 + 256 * j, isb = p >= 200, q = isb ? p - 200 : p, rg = (q * 205) >> 10;
            const u32 so = p < 400 ? rg * 61440 + sg * 320 + (q - rg * 5) * 64 : 0;
            dma<L_SCR>(p < 400 ? (lane >> 2) * 3840 + (lane & 3) * 16 : OOB, mkrs(isb ? wb : wa, 640 * 3840), so, wr * 2048 + j * 1024);
            const u32 n = (q - rg * 5) * 32 + l31, h = (n * 205) >> 12, d = n - 20 * h, o8 = 2 * rg + hf;
            const u32 t = d < 16 ? (h & 1) * 1024 + (isb ? 0 : 3072) + d * 16 : 2048 - 256 + (8 * isb + 4 * (h & 1) + d) * 16;
            qd[j] = p < 400 ? WS_WQ + (4 * sg + (h >> 1)) * 102400 + (o8 >> 2) * 5120 + (o8 & 3) * 256 + t : OOB;
        }
        const u32 krec = WS_KVP + ((2 * hg + wn) * 16 + rb) * 2048 + wm * 1024;
        if (lane < 2) sts32(L_HQ + lane * 16 + wr * 4, 0);
        u32 wb_[2][3], xb_[2][2];
        {
            const u32 q3 = lane & 3, wrow = (g4 & 1) * WREG + (8 * (g4 >> 1) + (l15 >> 2)) * 320;
            wb_[0][0] = wrow + (ty ? 80 : 160) * wn + q3 * 8;
            wb_[0][1] = wb_[0][0] + (ty ? 160 : 80);
            wb_[0][2] = wrow + 32 + (ty ? 80 * wn + ((q3 & 2) ? 0 : 160) + 40 * (q3 & 1) : 160 * wn + 40 * q3);
            xb_[0][0] = XO + (64 * wm + 16 * (l15 >> 3) + (l15 & 7)) * 128 + ((g4 ^ (l15 & 6)) * 16);
            xb_[0][1] = xb_[0][0] ^ 64;
#pragma unroll
            for (int i = 0; i < 3; ++i) wb_[1][i] = opaque_v(wb_[0][i] + 2 * SLOT);
#pragma unroll
            for (int i = 0; i < 2; ++i) xb_[1][i] = opaque_v(xb_[0][i] + 2 * SLOT);
        }
        const u32 qsrc = L_SCR + wr * 2048 + (8 * hf + (l15 >> 2)) * 64 + (g4 & 1) * 32 + (lane & 3) * 8;
        auto wad = [&](int s, int m) { return wb_[SL[s % 4] >= 2 * SLOT][(m % 5) >> 1] + SL[s % 4] % (2 * SLOT) + (m / 5) * (2 * WREG) + ((m % 5) & 1) * 40; };
        auto xad = [&](int s, int ks, int t) { return xb_[SL[s % 4] >= 2 * SLOT][ks] + SL[s % 4] % (2 * SLOT) + (t >> 1) * 4096 + (t & 1) * 1024; };
        f32x4 acc[4][5];
        STAMP(30);
        tag = wait_rfl<0>(ev) + TAGC;
        STAMP(31);
        st128(__builtin_bit_cast(u32x4, ldtr2(qsrc, qsrc + 256)), qd[0], rws, 0);
        st128(__builtin_bit_cast(u32x4, ldtr2(qsrc + 1024, qsrc + 1280)), qd[1], rws, 0);
        if (wr == 0 && lane == 0) sts32(L_TAG, tag);
        {
            bf16x8 wf[5], xa[2][4];
            STAMP(1);
            wait_lgkm<0>();
            bar();
            STAMP(2);
            sfor<0, 4>([&](auto I) { xa[0][I.v] = asbf8(lds128(xad(0, 0, I.v))); });
            wf[0] = ldtr2(wad(0, 0), wad(0, 0) + 1280);
            wf[1] = ldtr2(wad(0, 1), wad(0, 1) + 1280);
            sfor<0, 100>([&](auto NC) {
                constexpr int s = decltype(NC)::v / 10, n = decltype(NC)::v % 10, ks = n / 5, c = n % 5;
                constexpr int s2 = n >= 8 ? s + 1 : s, m = (n + 2) % 10;
                constexpr bool more = s2 < 10, first = s == 0 && ks == 0, rdw = more && (!(XPM & 1) || s == 0), rdx = !(XPM & 2) || s == 0;
                bf16x4 lo, hi;
                bf16x8 t0, t1;
                auto mm = [&](auto I) { sched0(); if constexpr (!(XPM & 4) || s == 0) mfma16a<first>(acc[I.v][c], xa[ks][I.v], wf[c]); else asm volatile("" :: "v"(xa[ks][I.v]), "v"(wf[c])); sched0(); };
                mm(IC<0>{});
                if constexpr (n == 8 && more) {
                    wait_lgkm<0>();
                    if constexpr (s == FQS) wait_vm<0>();
                    bar();
                    sched0();
                }
                if constexpr (rdw) lo = ldtr(wad(s2, m));
                mm(IC<1>{});
                if constexpr (rdw) hi = ldtr(wad(s2, m) + 1280);
                mm(IC<2>{});
                if constexpr (n >= 8 && more && rdx) t0 = asbf8(lds128(xad(s2, 0, 2 * (n - 8))));
                if constexpr (n < 4 && rdx) xa[1][n] = asbf8(lds128(xad(s, 1, n)));
                mm(IC<3>{});
                if constexpr (n >= 8 && more && rdx) { t1 = asbf8(lds128(xad(s2, 0, 2 * (n - 8) + 1))); xa[0][2 * (n - 8)] = t0; xa[0][2 * (n - 8) + 1] = t1; }
                if constexpr (s == FQS && n == 8) { if (wr == 0) stflag(tag, WS_FQ + sg * 1024 + (idx >> 3) * 256 + ((idx & 7) * 2 + (xcd & 1)) * 4, rws); }
                if constexpr (s == FSB - 1 && n == 8) { if (wr == 1) stflag(tag, WS_FS + (b >> 4) * 256 + (b & 15) * 4, rws); }
                if constexpr (rdw) wf[m % 5] = cat8(lo, hi);
                sched0();
            });
        }
        mfma_pad();
        __builtin_amdgcn_s_setprio(PRIO);
        STAMP(12);
        auto pk8 = [&](int u, int c) {
            return u32x4{pk2(acc[2 * u][c][0], acc[2 * u][c][1]), pk2(acc[2 * u][c][2], acc[2 * u][c][3]),
                         pk2(acc[2 * u + 1][c][0], acc[2 * u + 1][c][1]), pk2(acc[2 * u + 1][c][2], acc[2 * u + 1][c][3])};
        };
        auto ph8 = [&](int u, int c) {
            return u32x4{phi2(acc[2 * u][c][0], acc[2 * u][c][1]), phi2(acc[2 * u][c][2], acc[2 * u][c][3]),
                         phi2(acc[2 * u + 1][c][0], acc[2 * u + 1][c][1]), phi2(acc[2 * u + 1][c][2], acc[2 * u + 1][c][3])};
        };
        auto markers = [&]() {
            const u32x4 mk4 = {~0u, ~0u, ~0u, ~0u};
            sfor<0, 4>([&](auto I) { st128(mk4, l16, rws, oprod + c4 * 1024 + I.v * 20480); });
            st128(mk4, (g4 >> 1) * 40960 + (g4 & 1) * 20480 + l15 * 16, rws, oprod + 4096 + c4 * 256);
        };
        if (!ty) {
            const u32x4 fs = ldflag4(fso, rws);
            const u32 so = WS_T + ((4 * hg + 2 * wn) * 64 + 4 * rb + 2 * wm) * 2560;
            const u32 vm = g4 * 256 + ((l15 ^ (4 * g4)) * 16);
            const u32 vr = 2048 + g4 * 128 + ((((l15 >> 2) ^ (g4 >> 1)) & 1) * 64) + (l15 & 3) * 16 + (l15 >> 3) * 163840;
            sfor<0, 10>([&](auto I) {
                constexpr int c = decltype(I)::v >> 1, u = decltype(I)::v & 1;
                st128<(c < 4 ? (c & 1) * 1024 : 0) + u * 2560>(pk8(u, c), c < 4 ? vm : vr, rws, so + (c == 2 || c == 3 ? 163840 : 0));
            });
            markers();
            wok = wait_eq4<5>(fs, tag);
            stflag(tag, WS_FT + ((hg * 2 + wn) * 32 + 2 * rb + wm) * 128, rws);
            const u32 vw = wok ? l16 : OOB;
            sfor<0, 25>([&](auto I) { wld(I, vw); });
        } else {
            const u32x4 fs = ldflag4(fso, rws);
            markers();
            if constexpr (PFW) {
                const u32 po = WS_WO + (((idx >> 2) * 2 + (idx & 1)) * 4 + wr) * 25600;
                pref(lane * 128, rws, po); pref(lane * 128, rws, po + 8192); pref(lane * 128, rws, po + 16384); pref(lane < 8 ? lane * 128 : OOB, rws, po + 24576);
            }
            f32x4 Q0 = {0, 0, 0, 0}, Q1 = Q0, X0 = Q0, X1 = Q0, Y0 = Q0, Y1 = Q0, U = Q0;
#pragma unroll
            for (int u = 0; u < 2; ++u) {
                const bf16x8 v0 = asbf8(pk8(u, 2)), v1 = asbf8(pk8(u, 3));
                const u32x4 rr = pk8(u, 4);
                const bf16x8 rbv = asbf8(l15 < 8 ? rr : u32x4{ONES, ONES, ONES, ONES});
                const bf16x8 k0 = asbf8(ph8(u, 0));
                Q0 = mfma16(k0, v0, Q0); X0 = mfma16(k0, rbv, X0);
                const bf16x8 k1 = asbf8(ph8(u, 1));
                Q1 = mfma16(k1, v1, Q1); X1 = mfma16(k1, rbv, X1);
                const bf16x8 ra = asbf8(ph8(u, 4));
                Y0 = mfma16(ra, v0, Y0); Y1 = mfma16(ra, v1, Y1); U = mfma16(ra, rbv, U);
                if (u == WU) {
                    sched0();
                    wok = wait_eq4<5 + 4 * PFW>(fs, tag);
                    const u32 vw = wok ? l16 : OOB;
                    sfor<0, 25>([&](auto I) { wld(I, vw); });
                    sched0();
                }
            }
            f32x4 X = (l15 & 4) ? X1 : X0, Z;
#pragma unroll
            for (int r = 0; r < 4; ++r) Z[r] = u2f(__builtin_amdgcn_permlane32_swap(f2u((g4 & 1) ? Y1[r] : Y0[r]), f2u(U[r]), false, false)[1]);
            sts128(L_SCR + wr * 2048 + l16, __builtin_bit_cast(u32x4, wm ? Q0 : X));
            sts128(L_SCR + wr * 2048 + 1024 + l16, __builtin_bit_cast(u32x4, wm ? Q1 : Z));
            ctl_set(me, 1);
            ctl_wait1(L_MC + (wr ^ 2) * 4, 1);
            const f32x4 a = (wm ? X : Q0) + __builtin_bit_cast(f32x4, lds128(L_SCR + (wr ^ 2) * 2048 + l16));
            const f32x4 c = (wm ? Z : Q1) + __builtin_bit_cast(f32x4, lds128(L_SCR + (wr ^ 2) * 2048 + 1024 + l16));
            st128(u32x4{pk2(a[0], a[1]), pk2(a[2], a[3]), pk2(c[0], c[1]), pk2(c[2], c[3])}, l16, rws, krec);
        }
        STAMP(13);
        if constexpr (CPF) {
            u64 pc; u32 len; int rel;
            asm volatile("s_getpc_b64 %0\n.Lcode_p2:\n\ts_mov_b32 %2, .Lcode_at-.Lcode_p2\n\ts_mov_b32 %1, .Lcode_hi-.Lcode_at" : "=s"(pc), "=s"(len), "=s"(rel));
            pc += (long long)rel;
            u32 o = wr * 8192 + lane * 128;
            o = o < len ? o : 0;
            asm volatile("s_mov_b32 m0, %2\n\ts_nop 0\n\tglobal_load_lds_dword %0, %1" :: "v"(o), "s"(pc), "i"(L_DUMP) : "memory");
        }
        {
            const int g0 = 2 * (int)rb - 4 + 3 * (int)wn;
            int gl = g0 + (int)(lane < 2 ? lane : 2);
            gl = gl < 0 ? 0 : gl;
            spin(WS_FT + ((wm * 8 + (hp >> 1)) * 32 + gl) * 128, rws, tag);
            ctl_set(me, 2);
            ctl_wait8(2);
            if (wr == 0) stflag(tag, WS_F2 + rb * 256 + hp * 4, rws);
            STAMP(14);
#pragma unroll
            for (int i = 0; i < 3; ++i) {
                const u32 mb = wm ? L_V + (3 * wn + i) * 5136 : L_K + (3 * wn + i) * 5120;
                if (g0 + i >= 0) {
                    const u32 so = WS_T + ((wm * 16 + hp) * 32 + g0 + i) * 5120;
                    dmac<0, 0>(l16, rws, so, mb); dmac<0, 1024>(l16, rws, so, mb); dmac<0, 2048>(l16, rws, so, mb); dmac<0, 3072>(l16, rws, so, mb);
                    dmac<4096, 0>(l16, rws, so + 4096, mb);
                } else {
#pragma unroll
                    for (int k = 0; k < 5; ++k) sts128(mb + k * 1024 + l16, u32x4{0, 0, 0, 0});
                }
            }
            if (wm && lane < 12) sts32(L_V + (3 * wn + (lane >> 2)) * 5136 + 5120 + (lane & 3) * 4, g0 + (int)(lane >> 2) >= 0 ? ONES : 0);
            asetup(1);
            asm volatile("" : "=v"(P[0]), "=v"(P[1]), "=v"(P[2]));
            if constexpr (HWN >= 200) wlate();
            wait_vm<0>();
            ctl_set(me, 3);
            if constexpr (HWN < 200) wlate();
            STAMP(15);
        }
        ctl_wait(L_HQ, 6);
        qf = lds128(L_XQ + wr * 1536 + l16);
        const u32x2 r = lds64(L_XQ + wr * 1536 + 1024 + lane * 8);
        qr = hf ? u32x2{0, 0} : r;
        gt = u2f(lds32(L_XQ + wr * 1536 + 1280 + l31 * 8));
        ctl_wait(L_MC, 3);
        if (b == 0 && wr == 0) stflag(tag, WS_E, rws);
        __builtin_amdgcn_s_setprio(0);
        STAMP(16);
        asm volatile(".Lcode_hm:");
    }
    asm volatile(".Lcode_at:");
    auto recs = [&]() {
        const u32 vo = hf * 2048 + l31 * 16, so = WS_KVP + hp * 32768 + wr * 512;
        sfor<0, 8>([&](auto I) { dmac<L_RC + I.v * 1024>(vo, rws, so + I.v * 4096, wr * 8192); });
    };
    u32 pv;
    bool rok = false;
    {
        const u32 hh = wid < 4;
        f32x16 zero;
#pragma unroll
        for (int i = 0; i < 16; ++i) zero[i] = 0.f;
        const bf16x8 q16 = asbf8(qf);
        const bf16x4 q4 = __builtin_bit_cast(bf16x4, qr);
        auto qk = [&](u32 o) { return mfma32k8(ldtr(kr + o), q4, mfma32(ldtr2(ka + o, kc + o), q16, zero)); };
        f32x16 o = zero;
        sched0();
        bf16x8 j16 = ldtr2(ka + 2560, kc + 2560), v2, v3;
        bf16x4 j4 = ldtr(kr + 2560);
        bf16x8 k16 = ldtr2(ka + 5120, kc + 5120);
        bf16x4 k4 = ldtr(kr + 5120);
        bf16x8 v0 = asbf8(lds128(va[1][0])), v1 = asbf8(lds128(va[1][1]));
        sched0();
        f32x16 S = mfma32k8(j4, q4, mfma32(j16, q16, zero)), Sn, S8;
        sfor<1, 9>([&](auto I) {
            constexpr int it = decltype(I)::v, i = it & 7, nx = (it + 1) & 7;
            auto wl = [&](auto J) {
                if constexpr (HWN >= 200) { if (wid >= 4) rl128<(J.v & 3) * 1024>(W[J.v], l16, rws, WS_WO + c80 * 204800 + wid * 25600 + (J.v >> 2) * 4096); }
            };
            float e[16];
            u32 p[8];
            auto ex = [&](auto A, auto B) {
                sfor<A.v, B.v>([&](auto R) {
                    if constexpr (i == 0) e[R.v] = __builtin_amdgcn_exp2f(__builtin_amdgcn_inverse_ballot_w64(lem((R.v & 3) + 8 * (R.v >> 2))) ? S8[R.v] : S[R.v]);
                    else e[R.v] = __builtin_amdgcn_exp2f(S[R.v]);
                });
                sched0();
            };
            auto cv = [&](int a) {
#pragma unroll
                for (int j = a; j < a + 4; ++j) p[j] = pk2(e[2 * j], e[2 * j + 1]);
                sched0();
            };
            sched0();
            if constexpr ((ASM >> it) & 1) STAMP(35 + it);
            if constexpr (it == HK1) pv = ldflag(f2o, rws);
            if constexpr (it == HK2) {
                if (wid < 4) { rok = wait_eq<0>(pv, tag); if (rok) recs(); }
                else rok = wait_eq<(HWN >= 200 ? 3 * (HK2 - HK1) : 0)>(pv, tag);
            }
            if constexpr (it == 1) wl(IC<0>{});
            sched0();
            if constexpr (i != 0) Sn = mfma32(k16, q16, zero);
            sched0();
            ex(IC<0>{}, IC<8>{});
            if constexpr (i != 0) Sn = mfma32k8(k4, q4, Sn);
            sched0();
            if constexpr (it < 7) { k16 = ldtr2(ka + ((it + 2) & 7) * 2560, kc + ((it + 2) & 7) * 2560); k4 = ldtr(kr + ((it + 2) & 7) * 2560); }
            if constexpr (it == 5) { j16 = ldtr2(ka + 8 * 2560, kc + 8 * 2560); j4 = ldtr(kr + 8 * 2560); }
            if constexpr (it == 7) { v2 = asbf8(lds128(va[0][0] + 4 * 5136)); v3 = asbf8(lds128(va[0][1] + 4 * 5136)); }
            wl(IC<3 * it - 2>{});
            sched0();
            cv(0);
            if constexpr (i == 0) { o = mfma32(v2, asbf8(u32x4{p[0] & mk[0], p[1] & mk[1], p[2] & mk[2], p[3] & mk[3]}), o); sched0(); }
            ex(IC<8>{}, IC<12>{});
            if constexpr (i == 0) o = mfma32(v0, asbf8(u32x4{p[0] & ~mk[0], p[1] & ~mk[1], p[2] & ~mk[2], p[3] & ~mk[3]}), o);
            else o = mfma32(v0, asbf8(u32x4{p[0], p[1], p[2], p[3]}), o);
            sched0();
            wl(IC<3 * it - 1>{});
            if constexpr (it == 6) S8 = mfma32(j16, q16, zero);
            sched0();
            ex(IC<12>{}, IC<16>{});
            cv(4);
            if constexpr (i == 0) {
                o = mfma32(v3, asbf8(u32x4{p[4] & mk[4], p[5] & mk[5], p[6] & mk[6], p[7] & mk[7]}), o);
                o = mfma32(v1, asbf8(u32x4{p[4] & ~mk[4], p[5] & ~mk[5], p[6] & ~mk[6], p[7] & ~mk[7]}), o);
            } else o = mfma32(v1, asbf8(u32x4{p[4], p[5], p[6], p[7]}), o);
            sched0();
            if constexpr (it == 6) S8 = mfma32k8(j4, q4, S8);
            if constexpr (i != 0) { v0 = asbf8(lds128(va[nx & 1][0] + (nx >> 1) * 5136)); v1 = asbf8(lds128(va[nx & 1][1] + (nx >> 1) * 5136)); }
            wl(IC<3 * it>{});
            sched0();
            if constexpr (i != 0) S = Sn;
        });
        if constexpr ((ASM >> 9) & 1) STAMP(44);
        const int qq = (int)l31 - 4 * (int)hf;
        float am = 0.f;
#pragma unroll
        for (int r = 0; r < 12; r += 2) am = fmaxf(fmaxf(am, fabsf(o[r])), fabsf(o[r + 1]));
        const bool fine = o[12] > 0x1p-30f && o[12] < 0x1p30f && am < 0x1p120f;
        if (__builtin_amdgcn_ballot_w64(!fine) != 0ull) {
            const int i0 = rb >= 2 ? 0 : 8 - 4 * (int)rb - (int)wr;
            auto sc = [&](int i) {
                f32x16 s = qk(i * 2560);
#pragma unroll
                for (int r = 0; r < 16; ++r) {
                    const bool le = (r & 3) + 8 * (r >> 2) <= qq;
                    const bool ok = i == 0 ? !le : (i == 8 ? le : true);
                    s[r] = ok ? s[r] : -__builtin_inff();
                }
                return s;
            };
            float m = -__builtin_inff();
#pragma nounroll
            for (int i = i0; i < 9; ++i) {
                const f32x16 s = sc(i);
#pragma unroll
                for (int r = 0; r < 16; ++r) m = fmaxf(m, s[r]);
            }
            auto sw = __builtin_amdgcn_permlane32_swap(f2u(m), f2u(m), false, false);
            m = fmaxf(u2f(sw[0]), u2f(sw[1]));
            o = zero;
#pragma nounroll
            for (int i = i0; i < 9; ++i) {
                const f32x16 s = sc(i);
                u32 p[8];
#pragma unroll
                for (int j = 0; j < 8; ++j) p[j] = pk2(__builtin_amdgcn_exp2f(s[2 * j] - m), __builtin_amdgcn_exp2f(s[2 * j + 1] - m));
                const u32 vo_ = (i >> 1) * 5136;
                o = mfma32(asbf8(lds128(((i & 1) ? va[1][0] : va[0][0]) + vo_)), asbf8(u32x4{p[0], p[1], p[2], p[3]}), o);
                o = mfma32(asbf8(lds128(((i & 1) ? va[1][1] : va[0][1]) + vo_)), asbf8(u32x4{p[4], p[5], p[6], p[7]}), o);
            }
        }
        const float rs = __builtin_amdgcn_rcpf(o[12]) * gt;
        const u32 ob = oprod + 20480 + (l31 >> 4) * 40960;
        u32x4 v = {pk2m(o[0] * rs, o[1] * rs), pk2m(o[2] * rs, o[3] * rs), pk2m(o[4] * rs, o[5] * rs), pk2m(o[6] * rs, o[7] * rs)};
        __builtin_amdgcn_raw_buffer_store_b128(v, wsr, ob + c4 * 1024 + (2 * hh + hf) * 256 + l15 * 16, 0, 16);
        u32x2 vt = {pk2m(o[8] * rs, o[9] * rs), pk2m(o[10] * rs, o[11] * rs)};
        __builtin_amdgcn_raw_buffer_store_b64(vt, wsr, hf ? OOB : ob + 4096 + (2 * c4 + hh) * 128 + l15 * 8, 0, 16);
        cfence();
    }
    STAMP(17);
    STAMPC(58);
    if (!rok) spin(f2o, rws, tag);
    if (wid < 4) {
        if (rok) wait_vm<2>(); else { recs(); wait_vm<0>(); }
        float s[8];
#pragma unroll
        for (int i = 0; i < 8; ++i) {
            const u32x4 v = lds128(L_RC + wr * 8192 + i * 1024 + l16);
#pragma unroll
            for (int k = 0; k < 4; ++k) { s[2 * k] = i ? s[2 * k] + blo(v[k]) : blo(v[k]); s[2 * k + 1] = i ? s[2 * k + 1] + bhi(v[k]) : bhi(v[k]); }
        }
        float t[4];
#pragma unroll
        for (int k = 0; k < 4; ++k) {
            auto sw = __builtin_amdgcn_permlane32_swap(f2u(s[k]), f2u(s[4 + k]), false, false);
            t[k] = u2f(sw[0]) + u2f(sw[1]);
        }
        sts64(L_REC + wr * 512 + l31 * 16 + hf * 8, u32x2{pk2(t[0], t[1]), pk2(t[2], t[3])});
    } else {
        ctl_wait(L_MC, 10);
        STAMP(23);
        const u32x4 m = lds128(L_REC + l16), n = lds128(L_REC + 1024 + l16);
        const bool m1 = l15 & 4;
        const bf16x8 a0 = asbf8(u32x4{m[0], m[1], g4 == 0 ? n[2] : 0, g4 == 0 ? n[3] : 0});
        const bf16x8 a1 = asbf8(u32x4{m[2], m[3], g4 == 1 ? n[2] : 0, g4 == 1 ? n[3] : 0});
        const bf16x8 x0 = asbf8(u32x4{m1 ? 0 : n[0], m1 ? 0 : n[1], (g4 == 2 && !m1) ? n[2] : 0, (g4 == 2 && !m1) ? n[3] : 0});
        const bf16x8 x1 = asbf8(u32x4{m1 ? n[0] : 0, m1 ? n[1] : 0, (g4 == 3 && m1) ? n[2] : 0, (g4 == 3 && m1) ? n[3] : 0});
#pragma unroll
        for (int t2 = 0; t2 < 2; ++t2) {
            const bf16x8 b0 = asbf8(u32x4{P[t2][0], P[t2][1], P[2][2 * t2], P[2][2 * t2 + 1]});
            const bf16x8 b1 = asbf8(u32x4{P[t2][2], P[t2][3], P[2][2 * t2], P[2][2 * t2 + 1]});
            const f32x4 z = {0, 0, 0, 0};
            const f32x4 xx = mfma16(x1, b1, mfma16(x0, b0, z));
            const f32x4 n0 = mfma16(a0, b0, z), n1 = mfma16(a1, b1, z);
            const u32 rc = f2u(__builtin_amdgcn_rcpf(xx[0] + 1e-6f) * (1.f - (t2 ? g1 : g0)));
            const u32 rs = __builtin_amdgcn_permlane32_swap(rc, rc, false, false)[1];
            auto sw = __builtin_amdgcn_permlane16_swap(rs, rs, false, false);
            const float r0 = u2f(sw[0]), r1 = u2f(sw[1]), rr = u2f(rs);
            const u32 so = oprod + t2 * 40960;
            st128(u32x4{pk2m(n0[0] * r0, n0[1] * r0), pk2m(n0[2] * r0, n0[3] * r0), pk2m(n1[0] * r1, n1[1] * r1), pk2m(n1[2] * r1, n1[3] * r1)}, l16, rws, so + c4 * 1024);
            __builtin_amdgcn_raw_buffer_store_b64(u32x2{pk2m(xx[0] * rr, xx[1] * rr), pk2m(xx[2] * rr, xx[3] * rr)}, wsr, hf ? OOB : lane * 8, so + 4096 + c4 * 256, 16);
        }
        cfence();
    }
    ctl_set(me, 10);
    STAMP(24);
    {
        const u32 vr = lane * 8;
        auto ot = [&](u32 x) { return WS_O + (r64 * 4 + (wr ^ x)) * 40960 + wid * 5120; };
        auto oload = [&](OF& o, u32 s0, u32 s1) {
#pragma unroll
            for (int e = 0; e < 4; ++e) {
                o.m[0][e] = __builtin_amdgcn_raw_buffer_load_b128(wsr, l16 + e * 1024, s0, 16);
                o.m[1][e] = __builtin_amdgcn_raw_buffer_load_b128(wsr, l16 + e * 1024, s1, 16);
            }
#pragma unroll
            for (int h = 0; h < 2; ++h) {
                o.r[0][h] = __builtin_amdgcn_raw_buffer_load_b64(wsr, vr + h * 512, s0 + 4096, 16);
                o.r[1][h] = __builtin_amdgcn_raw_buffer_load_b64(wsr, vr + h * 512, s1 + 4096, 16);
            }
            cfence();
        };
        auto pass = [&](OF& o, f32x4 (&ac)[2][5], u32 s0, u32 s1) {
            u32 nx = 0, bad = 31;
#pragma unroll
            for (int i = 0; i < 10; ++i) asm volatile("" : "=v"(ac[i / 5][i % 5]));
#pragma nounroll
            for (u32 it = 0; it < (1u << 17); ++it) {
                const u32 chk = bad;
                bad = 32;
                sfor<0, 4>([&](auto E) {
                    if (chk & (1u << E.v)) {
                        if (marked(mx4(mx4(0, o.m[0][E.v]), o.m[1][E.v]))) {
                            rl128<E.v * 1024>(o.m[0][E.v], l16, rws, s0); rl128<E.v * 1024>(o.m[1][E.v], l16, rws, s1);
                            bad |= 1u << E.v;
                        }
                    }
                });
                if (marked(umax(umax(umax(o.r[0][0][0], o.r[0][0][1]), umax(o.r[0][1][0], o.r[0][1][1])), umax(umax(o.r[1][0][0], o.r[1][0][1]), umax(o.r[1][1][0], o.r[1][1][1]))))) {
                    rl64<0>(o.r[0][0], vr, rws, s0 + 4096); rl64<0>(o.r[1][0], vr, rws, s1 + 4096);
                    rl64<512>(o.r[0][1], vr, rws, s0 + 4096); rl64<512>(o.r[1][1], vr, rws, s1 + 4096);
                    bad |= 16;
                }
                const u32 lim = __builtin_ctz(bad);
                sfor<0, 5>([&](auto E) {
                    constexpr int e = decltype(E)::v;
                    if (e >= nx && e < lim) {
#pragma unroll
                        for (int t = 0; t < 2; ++t) {
                            const bf16x8 f = asbf8(e < 4 ? o.m[t][e & 3] : u32x4{o.r[t][0][0], o.r[t][0][1], o.r[t][1][0], o.r[t][1][1]});
#pragma unroll
                            for (int c = 0; c < 5; ++c) mfma16b<e == 0>(ac[t][c], asbf8(W[e * 5 + c]), f);
                        }
                    }
                });
                nx = lim;
                if (lim == 5) break;
                of_wait(o);
            }
            mfma_pad();
        };
        f32x4 ac[2][5];
        OF oa, ob;
        const u32 pw = (wid >= 4 ? wr * 20480 : L_K - 5120 + wr * 15360) + l16;
        if (OLATE) ctl_wait8(10);
        STAMP(25);
        oload(oa, ot(2), ot(3));
        oload(ob, ot(0), ot(1));
        of_tie(oa);
        pass(oa, ac, ot(2), ot(3));
        STAMP(18);
        ctl_wait8(10);
#pragma unroll
        for (int i = 0; i < 10; ++i) sts128(pw + 10240 + i * 1024, __builtin_bit_cast(u32x4, ac[i / 5][i % 5]));
        ctl_set(me, 11);
        of_tie(ob);
        pass(ob, ac, ot(0), ot(1));
        STAMP(19);
#pragma unroll
        for (int c = 0; c < 5; ++c) sts128(pw + 5120 + c * 1024, __builtin_bit_cast(u32x4, ac[1][c]));
        if (wid >= 4) {
#pragma unroll
            for (int c = 0; c < 5; ++c) sts128(pw + c * 1024, __builtin_bit_cast(u32x4, ac[0][c]));
            ctl_set(me, 12);
        } else {
            ctl_set(me, 12);
            auto add = [&](u32 a) {
#pragma unroll
                for (int c = 0; c < 5; ++c) ac[0][c] += __builtin_bit_cast(f32x4, lds128(a + l16 + c * 1024));
            };
            ctl_wait(L_MC, 12);
#pragma unroll
            for (u32 x = 1; x < 4; ++x) add(L_K - 5120 + (wr ^ x) * 15360 + x * 5120);
            STAMP(20);
            ctl_wait(L_HQ, 11);
#pragma unroll
            for (u32 x = 2; x < 4; ++x) add((wr ^ x) * 20480 + x * 5120);
            ctl_wait(L_HQ, 12);
            STAMP(21);
#pragma unroll
            for (u32 x = 0; x < 2; ++x) add((wr ^ x) * 20480 + x * 5120);
            u32 r[10];
#pragma unroll
            for (int c = 0; c < 5; ++c) { r[2 * c] = pk2(ac[0][c][0], ac[0][c][1]); r[2 * c + 1] = pk2(ac[0][c][2], ac[0][c][3]); }
            const __amdgpu_buffer_rsrc_t ors = __builtin_amdgcn_make_buffer_rsrc(out, 0, 2048 * 1280, 0x00020000);
            const u32 vo = l15 * 1280 + g4 * 40 + (r64 * 64 + wr * 16) * 1280 + c80 * 160;
            __builtin_amdgcn_raw_buffer_store_b128(u32x4{r[0], r[1], r[2], r[3]}, ors, vo, 0, 0);
            __builtin_amdgcn_raw_buffer_store_b128(u32x4{r[4], r[5], r[6], r[7]}, ors, vo + 16, 0, 0);
            __builtin_amdgcn_raw_buffer_store_b64(u32x2{r[8], r[9]}, ors, vo + 32, 0, 0);
        }
    }
    STAMP(22);
    STAMPC(63);
    asm volatile(".Lcode_hi:");
}

extern "C" void kernel_launch(void* const* d_in, const int* in_sizes, int n_in, void* d_out, int out_size, void* d_ws,
                              size_t ws_size, hipStream_t stream) {
    (void)in_sizes; (void)n_in; (void)out_size; (void)ws_size;
    (void)hipFuncSetAttribute((const void*)fused_kernel, hipFuncAttributeMaxDynamicSharedMemorySize, LDS_BYTES);
    fused_kernel<<<256, 512, LDS_BYTES, stream>>>((const u16*)d_in[0], (const u16*)d_in[1], (const u16*)d_in[2], (const u16*)d_in[4],
                                                  (const u16*)d_in[5], (const u16*)d_in[6], (u16*)d_out, (char*)d_ws);
}
```

```cpp
#include <hip/hip_runtime.h>
#include <stdint.h>
#pragma clang diagnostic ignored "-Wint-to-pointer-cast"

#define SPINS 1
#define PRIO 3
typedef unsigned short u16;
typedef unsigned int u32;
typedef unsigned long long u64;
typedef __attribute__((ext_vector_type(8))) __bf16 bf16x8;
typedef __attribute__((ext_vector_type(4))) __bf16 bf16x4;
typedef __attribute__((ext_vector_type(2))) __bf16 bf16x2;
typedef __attribute__((ext_vector_type(2))) float f32x2;
typedef __attribute__((ext_vector_type(4))) float f32x4;
typedef __attribute__((ext_vector_type(16))) float f32x16;
typedef __attribute__((ext_vector_type(4))) u32 u32x4;
typedef __attribute__((ext_vector_type(2))) u32 u32x2;
typedef __attribute__((ext_vector_type(4))) short s16x4;
#define AS3 __attribute__((address_space(3)))
#define DEVI __device__ __forceinline__

constexpr u32 WS_E   = 0;
constexpr u32 WS_FS  = 4096;
constexpr u32 WS_F2  = 12288;
constexpr u32 WS_FQ  = 8192;
constexpr u32 WS_FT  = 16384;
constexpr u32 WS_T   = 81920;
constexpr u32 WS_KVP = WS_T + 2 * 16 * 64 * 2560;
constexpr u32 WS_O   = WS_KVP + 16 * 16 * 2048;
constexpr u32 WS_WO  = WS_O + 128 * 40960;
constexpr u32 WS_WQ  = WS_WO + 16 * 102400;
constexpr u32 WS_END = WS_WQ + 16 * 102400;
constexpr u32 OOB = 0x80000000u;

constexpr u32 WREG  = 5152;
constexpr u32 XO    = 20608;
constexpr u32 SLOT  = 36992;
__device__ constexpr u32 SL[4] = {0, 3 * SLOT, 2 * SLOT, SLOT};
constexpr u32 L_SCR = 4 * SLOT;
constexpr u32 L_GW  = L_SCR + 8192;
constexpr u32 L_DUMP = 163328;
constexpr u32 L_REC = 0;
constexpr u32 L_XQ  = 2048;
constexpr u32 L_QT  = 3 * SLOT;
constexpr u32 L_K   = 101376;
constexpr u32 L_V   = 132096;
constexpr u32 L_RC  = 8192;
constexpr u32 L_HQ  = 162944, L_MC = L_HQ + 16, L_TAG = L_HQ + 32;
constexpr u32 LDS_BYTES = 163840;
static_assert(L_GW + 2048 <= L_HQ && L_DUMP >= L_TAG + 4 && L_DUMP + 256 <= LDS_BYTES && L_V + 6 * 5136 <= L_HQ && L_XQ + 6144 <= L_QT - 102400, "lds");

constexpr float LOG2E = 1.4426950408889634f;
constexpr float QSCALE = 0.22360679774997896f * 1.4426950408889634f;
constexpr u32 TAGC = 0x7F4A7C15u;
constexpr u32 ONES = 0x3F803F80u;

DEVI u32 pk2(float a, float b) { f32x2 v = {a, b}; bf16x2 r = __builtin_convertvector(v, bf16x2); return __builtin_bit_cast(u32, r); }
DEVI u32 f2u(float f) { return __builtin_bit_cast(u32, f); }
DEVI float u2f(u32 u) { return __builtin_bit_cast(float, u); }
DEVI float blo(u32 u) { return u2f(u << 16); }
DEVI float bhi(u32 u) { return u2f(u & 0xFFFF0000u); }
DEVI u32 umin(u32 a, u32 b) { return a < b ? a : b; }
DEVI u32 umax(u32 a, u32 b) { return a > b ? a : b; }
DEVI u32 pk2m(float a, float b) { return umin(pk2(a, b), 0xFFFFFFFEu); }
DEVI bf16x8 asbf8(u32x4 v) { return __builtin_bit_cast(bf16x8, v); }
DEVI f32x4 mfma16(bf16x8 a, bf16x8 b, f32x4 c) { return __builtin_amdgcn_mfma_f32_16x16x32_bf16(a, b, c, 0, 0, 0); }
template <bool FIRST = false> DEVI void mfma16a(f32x4& c, bf16x8 a, bf16x8 b) {
    if constexpr (FIRST) asm volatile("v_mfma_f32_16x16x32_bf16 %0, %1, %2, 0" : "=&v"(c) : "v"(a), "v"(b));
    else asm volatile("v_mfma_f32_16x16x32_bf16 %0, %1, %2, %0" : "+v"(c) : "v"(a), "v"(b));
}
template <bool FIRST> DEVI void mfma16b(f32x4& c, bf16x8 a, bf16x8 b) {
    if constexpr (FIRST) asm volatile("v_mfma_f32_16x16x32_bf16 %0, %1, %2, 0" : "+v"(c) : "v"(a), "v"(b));
    else asm volatile("v_mfma_f32_16x16x32_bf16 %0, %1, %2, %0" : "+v"(c) : "v"(a), "v"(b));
}
DEVI void mfma_pad() { __builtin_amdgcn_sched_barrier(0); asm volatile("s_nop 7\n\ts_nop 3" ::: "memory"); __builtin_amdgcn_sched_barrier(0); }
DEVI f32x16 mfma32(bf16x8 a, bf16x8 b, f32x16 c) { return __builtin_amdgcn_mfma_f32_32x32x16_bf16(a, b, c, 0, 0, 0); }
DEVI f32x16 mfma32k8(bf16x4 a, bf16x4 b, f32x16 c) {
    return __builtin_amdgcn_mfma_f32_32x32x8bf16_1k(__builtin_bit_cast(s16x4, a), __builtin_bit_cast(s16x4, b), c, 0, 0, 0);
}
DEVI bf16x4 ldtr(u32 a) { return __builtin_amdgcn_ds_read_tr16_b64_v4bf16((AS3 bf16x4*)a); }
DEVI bf16x8 cat8(bf16x4 x, bf16x4 y) { return __builtin_shufflevector(x, y, 0, 1, 2, 3, 4, 5, 6, 7); }
DEVI bf16x8 ldtr2(u32 a, u32 b) { bf16x4 x = ldtr(a), y = ldtr(b); return cat8(x, y); }
DEVI u32x4 lds128(u32 a) { return *(AS3 u32x4*)a; }
DEVI u32x2 lds64(u32 a) { return *(AS3 u32x2*)a; }
DEVI u32 lds32(u32 a) { return *(AS3 u32*)a; }
DEVI void sts128(u32 a, u32x4 v) { *(AS3 u32x4*)a = v; }
DEVI void sts64(u32 a, u32x2 v) { *(AS3 u32x2*)a = v; }
DEVI void sts32(u32 a, u32 v) { *(AS3 u32*)a = v; }
DEVI u32 rfl(u32 v) { return __builtin_amdgcn_readfirstlane(v); }
DEVI u32 opaque_s(u32 v) { asm volatile("" : "+s"(v)); return v; }
DEVI u32 opaque_v(u32 v) { asm volatile("" : "+v"(v)); return v; }
DEVI void undef(u32x4& v) { asm volatile("" : "=v"(v)); }
DEVI void cfence() { asm volatile("" ::: "memory"); }

DEVI u32x4 mkrs(const void* p, u32 bytes) { return u32x4{(u32)(uintptr_t)p, (u32)((uintptr_t)p >> 32) & 0xFFFFu, bytes, 0x00020000u}; }
template <int MO, int OFF = 0> DEVI void dma(u32 voff, u32x4 rs, u32 soff, u32 mb) {
    asm volatile("s_add_u32 m0, %3, %5\n\ts_nop 0\n\tbuffer_load_dwordx4 %0, %1, %2 offen offset:%4 lds"
                 :: "v"(voff), "s"(rs), "s"(soff), "s"(mb), "i"(OFF), "i"(MO) : "memory", "scc");
}
template <int MO, int OFF = 0> DEVI void dmac(u32 voff, u32x4 rs, u32 soff, u32 mb) {
    asm volatile("s_add_u32 m0, %3, %5\n\ts_nop 0\n\tbuffer_load_dwordx4 %0, %1, %2 offen offset:%4 sc1 lds"
                 :: "v"(voff), "s"(rs), "s"(soff), "s"(mb), "i"(OFF), "i"(MO) : "memory", "scc");
}
DEVI void pref(u32 voff, u32x4 rs, u32 soff) {
    asm volatile("s_mov_b32 m0, %3\n\ts_nop 0\n\tbuffer_load_dword %0, %1, %2 offen sc1 lds" :: "v"(voff), "s"(rs), "s"(soff), "i"(L_DUMP) : "memory");
}
DEVI u32 ldflag(u32 voff, u32x4 rs) {
    u32 v;
    asm volatile("buffer_load_dword %0, %1, %2, 0 offen sc1" : "=v"(v) : "v"(voff), "s"(rs) : "memory");
    return v;
}
DEVI u32x4 ldflag4(u32 voff, u32x4 rs) {
    u32x4 v;
    asm volatile("buffer_load_dwordx4 %0, %1, %2, 0 offen sc1" : "=v"(v) : "v"(voff), "s"(rs) : "memory");
    return v;
}
DEVI void stflag(u32 val, u32 soff, u32x4 rs) {
    asm volatile("s_mov_b64 exec, 1\n\tbuffer_store_dword %0, off, %1, %2 sc1\n\ts_mov_b64 exec, -1" :: "v"(val), "s"(rs), "s"(soff) : "memory");
}
template <int OFF = 0> DEVI void st128(u32x4 v, u32 voff, u32x4 rs, u32 soff) {
    asm volatile("buffer_store_dwordx4 %0, %1, %2, %3 offen offset:%4 sc1\n\ts_nop 1" :: "v"(v), "v"(voff), "s"(rs), "s"(soff), "i"(OFF) : "memory");
}
template <int OFF = 0> DEVI void st128n(u32x4 v, u32 voff, u32x4 rs, u32 soff) {
    asm volatile("buffer_store_dwordx4 %0, %1, %2, %3 offen offset:%4\n\ts_nop 1" :: "v"(v), "v"(voff), "s"(rs), "s"(soff), "i"(OFF) : "memory");
}
template <int N> DEVI void wait_vm() { asm volatile("s_waitcnt vmcnt(%0)" ::"i"(N) : "memory"); }
template <int N> DEVI u32 wait_rfl(u32 v) { u32 r; asm volatile("s_waitcnt vmcnt(%2)\n\tv_readfirstlane_b32 %0, %1" : "=s"(r) : "v"(v), "i"(N) : "memory"); return r; }
template <int N> DEVI bool wait_eq(u32 v, u32 tag) {
    u64 m; asm volatile("s_waitcnt vmcnt(%3)\n\tv_cmp_ne_u32_e64 %0, %2, %1" : "=s"(m) : "v"(v), "s"(tag), "i"(N) : "memory");
    return m == 0ull;
}
template <int N> DEVI bool wait_eq4(u32x4 v, u32 tag) {
    asm volatile("s_waitcnt vmcnt(%1)" : "+v"(v) : "i"(N) : "memory");
    return __builtin_amdgcn_ballot_w64(((v[0] ^ tag) | (v[1] ^ tag) | (v[2] ^ tag) | (v[3] ^ tag)) != 0u) == 0ull;
}
template <int N> DEVI void wait_lgkm() { asm volatile("s_waitcnt lgkmcnt(%0)" ::"i"(N) : "memory"); }
DEVI void bar() { asm volatile("s_barrier" ::: "memory"); }
DEVI void sched0() { __builtin_amdgcn_sched_barrier(0); }
DEVI void spin(u32 voff, u32x4 rs, u32 tag) {
#pragma nounroll
    for (u32 i = 0; i < (1u << 17); ++i) {
        u32 v = ldflag(voff, rs);
        if (wait_eq<0>(v, tag)) break;
        __builtin_amdgcn_s_sleep(SPINS);
    }
}
DEVI void ctl_set(u32 a, u32 v) { asm volatile("s_mov_b64 exec, 1\n\tds_write_b32 %0, %1\n\ts_mov_b64 exec, -1" :: "v"(a), "v"(v) : "memory"); }
DEVI u32x4 ctl_get(u32 a) { return *(volatile AS3 u32x4*)a; }
DEVI bool ctl_ge(u32x4 v, u32 t) { return rfl(umin(umin(v[0], v[1]), umin(v[2], v[3]))) >= t; }
DEVI void ctl_wait(u32 a, u32 t) {
#pragma nounroll
    for (u32 i = 0; i < (1u << 20); ++i) { if (ctl_ge(ctl_get(a), t)) break; __builtin_amdgcn_s_sleep(1); }
    cfence();
}
DEVI void ctl_wait8(u32 t) {
#pragma nounroll
    for (u32 i = 0; i < (1u << 20); ++i) {
        const u32x4 x = ctl_get(L_HQ), y = ctl_get(L_MC);
        if (ctl_ge(u32x4{umin(x[0], y[0]), umin(x[1], y[1]), umin(x[2], y[2]), umin(x[3], y[3])}, t)) break;
        __builtin_amdgcn_s_sleep(1);
    }
    cfence();
}
DEVI void ctl_wait1(u32 a, u32 t) {
#pragma nounroll
    for (u32 i = 0; i < (1u << 20); ++i) { if (rfl(*(volatile AS3 u32*)a) >= t) break; __builtin_amdgcn_s_sleep(1); }
    cfence();
}
DEVI bool marked(u32 m) { return __builtin_amdgcn_ballot_w64(m == 0xFFFFFFFFu) != 0ull; }
DEVI u32 mx4(u32 m, u32x4 v) { return umax(umax(umax(m, v[0]), v[1]), umax(v[2], v[3])); }

DEVI float phi(float t) { return __builtin_amdgcn_fmed3f(t + 1.f, __builtin_amdgcn_exp2f(t * LOG2E), 1.f) + 1e-6f; }
DEVI u32 phi2(float a, float b) { return pk2(phi(a), phi(b)); }
struct OF { u32x4 m[2][4]; u32x2 r[2][2]; };
template <int OFF> DEVI void ul128(u32x4& v, u32 voff, u32x4 rs, u32 soff) {
    asm volatile("buffer_load_dwordx4 %0, %1, %2, %3 offen offset:%4 sc1" : "=v"(v) : "v"(voff), "s"(rs), "s"(soff), "i"(OFF) : "memory");
}
template <int OFF> DEVI void rl128(u32x4& v, u32 voff, u32x4 rs, u32 soff) {
    asm volatile("buffer_load_dwordx4 %0, %1, %2, %3 offen offset:%4 sc1" : "+v"(v) : "v"(voff), "s"(rs), "s"(soff), "i"(OFF) : "memory");
}
DEVI u32x4 ld128(u32 voff, u32x4 rs, u32 soff) {
    u32x4 v;
    asm volatile("buffer_load_dwordx4 %0, %1, %2, %3 offen" : "=v"(v) : "v"(voff), "s"(rs), "s"(soff) : "memory");
    return v;
}
template <int OFF> DEVI void rl64(u32x2& v, u32 voff, u32x4 rs, u32 soff) {
    asm volatile("buffer_load_dwordx2 %0, %1, %2, %3 offen offset:%4 sc1" : "+v"(v) : "v"(voff), "s"(rs), "s"(soff), "i"(OFF) : "memory");
}
#define OF_OPS(o) "+v"(o.m[0][0]), "+v"(o.m[0][1]), "+v"(o.m[0][2]), "+v"(o.m[0][3]), "+v"(o.m[1][0]), "+v"(o.m[1][1]), "+v"(o.m[1][2]), "+v"(o.m[1][3]), \
                  "+v"(o.r[0][0]), "+v"(o.r[0][1]), "+v"(o.r[1][0]), "+v"(o.r[1][1])
DEVI void of_wait(OF& o) { asm volatile("s_waitcnt vmcnt(0)" : OF_OPS(o) :: "memory"); }
DEVI void of_tie(OF& o) { asm volatile("" : OF_OPS(o) :: "memory"); }

#define STAMP(i)
#define STAMPC(i)

template <int N> struct IC { static constexpr int v = N; };
template <int I, int N, class F> DEVI void sfor(F&& f) { if constexpr (I < N) { f(IC<I>{}); sfor<I + 1, N>(f); } }

#define ROTA 1
#define ROTB 3
#define UPTO {2, 4, 5, 6, 7, 8, 9, 10, 10, 10, 10}
#define HK1 3
#define HK2 5
#define HA 4
#define HB 6
#define XPM 0
#define HPRIO 3
#define QPRIO 0
#define PFQ -1
#define PFO 0
#define OLATE 1
#define FQS 1
#define CPF 0
#define ASM 0
#define FSB 6
#define HWN 200
#define HWS 1
#define PFW 0
#define WU 0
#define OAUX 0
#define XNOW 0
constexpr u64 lem(int k) { u64 m = 0; for (int l = 0; l < 64; ++l) if (k + 4 * (l >> 5) <= (l & 31)) m |= 1ull << l; return m; }
__device__ constexpr int UP[11] = UPTO;
__global__ __launch_bounds__(512, 2) void fused_kernel(const u16* __restrict__ x, const u16* __restrict__ wa, const u16* __restrict__ woa,
                                                       const u16* __restrict__ wb, const u16* __restrict__ wob, const u16* __restrict__ gw,
                                                       u16* out, char* __restrict__ ws) {
    const u32 lane = threadIdx.x & 63;
    const u32 wid = rfl(threadIdx.x >> 6);
    const u32 b = blockIdx.x, xcd = b & 7, idx = b >> 3;
    STAMP(0);
    STAMPC(62);
    if (wid == 3) {
        u64 pc; u32 len;
        asm volatile("s_getpc_b64 %0\n.Lcode_lo:\n\ts_mov_b32 %1, .Lcode_hi-.Lcode_lo\n\ts_max_u32 %1, %1, .Lcode_hm-.Lcode_lo" : "=s"(pc), "=s"(len) :: "scc");
        u32 o = (idx & 15) * 4096 + lane * 64;
        o = o < len ? o : 0;
        asm volatile("s_mov_b32 m0, %2\n\ts_nop 0\n\tglobal_load_lds_dword %0, %1" :: "v"(o), "s"(pc), "i"(L_DUMP) : "memory");
    }
    const u32 rb = (xcd & 1) * 8 + (idx >> 2), c4 = idx & 3, sg = xcd >> 1;
    const u32 ty = c4 >> 1;
    const u32 hg = ty ? 2 * sg + (c4 & 1) : sg + 4 * (c4 & 1);
    const u32 hp = 4 * sg + c4;
    const u32 wr = wid & 3;
    const u32x4 rws = mkrs(ws, WS_END);
    const __amdgpu_buffer_rsrc_t wsr = __builtin_amdgcn_make_buffer_rsrc(ws, 0, WS_END, 0x00020000);
    const u32 l15 = lane & 15, g4 = lane >> 4, l31 = lane & 31, hf = lane >> 5, l16 = lane * 16;
    const u32 me = (wid >= 4 ? L_HQ : L_MC) + wr * 4;
    const u32 oprod = WS_O + (rb * 8 + 2 * wr) * 40960 + sg * 5120;
    u32x4 qf; u32x2 qr; float gt;
    u32x4 P[3]; float g0, g1;
    u32 tag;
    const u32 r64 = xcd * 4 + (idx >> 3), c80 = idx & 7;
    u32 f2o;
    u32x4 W[25];
    bool wok;
    const u32 fso = WS_FS + (lane >> 2) * 256 + (lane & 3) * 16;
    auto wld = [&](auto I, u32 vw) __attribute__((always_inline)) { ul128<(I.v & 3) * 1024>(W[I.v], vw, rws, WS_WO + c80 * 204800 + wid * 25600 + (I.v >> 2) * 4096); };
    auto wlate = [&]() __attribute__((always_inline)) {
        if (!wok) {
#pragma nounroll
            for (u32 i = 0; i < (1u << 17); ++i) { if (wait_eq4<0>(ldflag4(fso, rws), tag)) break; __builtin_amdgcn_s_sleep(SPINS); }
            sfor<0, 25>([&](auto I) { rl128<(I.v & 3) * 1024>(W[I.v], l16, rws, WS_WO + c80 * 204800 + wid * 25600 + (I.v >> 2) * 4096); });
        }
    };
    u32 ka, kc, kr, va[2][2], mk[8];
    auto asetup = [&](u32 hh) __attribute__((always_inline)) {
        const u32 ch = 2 * (g4 & 1) + (lane & 1), kb = L_K + wr * 2560 + ((lane >> 1) & 1) * 8;
        ka = kb + hh * 1024 + ch * 256 + (((8 * hf + (l15 >> 2)) ^ (4 * ch)) * 16); kc = ka ^ 64;
        kr = kb + 2048 + ch * 128 + ((hh ^ (ch >> 1)) * 64) + (l15 >> 2) * 16;
#pragma unroll
        for (int p = 0; p < 2; ++p)
#pragma unroll
            for (int m = 0; m < 2; ++m) {
                const u32 bi = wr + p, c = 2 * m + hf;
                va[p][m] = L_V + (bi >> 1) * 5136 + (l31 < 20 ? (bi & 1) * 2560 + (l31 < 16 ? hh * 1024 + c * 256 + ((l31 ^ (4 * c)) * 16) : 2048 + c * 128 + ((hh ^ m) * 64) + (l31 - 16) * 16) : 5120);
            }
        sfor<0, 8>([&](auto J) {
            constexpr int k0 = ((2 * J.v) & 3) + 8 * ((2 * J.v) >> 2);
            mk[J.v] = (__builtin_amdgcn_inverse_ballot_w64(lem(k0)) ? 0xFFFFu : 0u) | (__builtin_amdgcn_inverse_ballot_w64(lem(k0 + 1)) ? 0xFFFF0000u : 0u);
        });
        f2o = WS_F2 + ((lane & 16) ? (r64 >> 1) * 256 + l15 * 4 : l15 * 256 + ((hp & 12) + 2 + ((hp >> 1) & 1)) * 4);
        asm volatile("" : "+v"(ka), "+v"(kc), "+v"(kr), "+v"(va[0][0]), "+v"(va[0][1]), "+v"(va[1][0]), "+v"(va[1][1]), "+v"(f2o), "+s"(out));
        asm volatile("" : "+v"(mk[0]), "+v"(mk[1]), "+v"(mk[2]), "+v"(mk[3]), "+v"(mk[4]), "+v"(mk[5]), "+v"(mk[6]), "+v"(mk[7]));
    };

    if (__builtin_expect(wid >= 4, 1)) {
        const u32 rot = (ROTA * rb + ROTB * c4) % 10;
        auto kbof = [&](u32 s) { u32 k = rot + s; return k >= 10 ? k - 10 : k; };
        const u32x4 rsx = mkrs(x, 2048 * 1280), rsw = mkrs(ty ? wb : wa, 640 * 3840);
        const u32 vx = (rb * 128 + 32 * wr + (lane >> 3)) * 1280 + (((lane & 7) ^ ((lane >> 3) & 6)) * 16);
        u32 vo[5];
#pragma unroll
        for (int i = 0; i < 5; ++i) {
            const u32 ch = 64 * i + lane, pr = (ch * 205) >> 12, j = ch - pr * 20;
            const u32 k = 32 * (wr >> 1) + 8 * (wr & 1) + (pr & 7) + 2 * (pr & 8);
            vo[i] = k * 3840 + (ty ? 1280 + 160 * hg + (j >= 10 ? 1120 : 0) : 1280 + 320 * hg) + j * 16;
        }
        auto issuep = [&](auto SC, auto A, auto B) {
            constexpr int s = decltype(SC)::v;
            const u32 kb = kbof(s);
            sfor<A.v, B.v>([&](auto I) {
                if constexpr (I.v < 4) dma<SL[s % 4] + XO + I.v * 1024>(vx, rsx, kb * 128 + I.v * 10240, wr * 4096);
                else dma<SL[s % 4] + (I.v - 4) * 1024>(vo[I.v - 4], rsw, kb * 245760, wr * WREG);
            });
        };
        auto issue = [&](auto SC) { issuep(SC, IC<0>{}, IC<9>{}); };
        STAMP(30);
        issue(IC<0>{});
        STAMP(31);
        {
            const u32 o = l16 + (wr & 1) * 1024 + rot * 128;
            dma<L_GW>(o >= 1280 ? o - 1280 : o, mkrs(gw, 1280), 0, (wr & 1) * 1024);
        }
        sfor<1, UP[0]>(issue);
        STAMP(1);
        const u32 xb0 = XO + (32 * wr + l15) * 128 + ((g4 ^ (l15 & 6)) * 16), xb1 = xb0 ^ 64;
        const u32 xc0 = xb0 + 2 * SLOT, xc1 = xb1 + 2 * SLOT, ga = L_GW + g4 * 16;
        const u32 qm0 = (wr & 1) * 5120 + (1 - (wr >> 1)) * 10240, qs0 = WS_WQ + hp * 102400 + (wr & 1) * 5120;
        auto qissue = [&](auto JC) {
            constexpr int jj = decltype(JC)::v;
            const u32 so = qs0 + kbof(2 * jj + (wr >> 1)) * 10240;
            sfor<0, 4>([&](auto I) { dmac<L_QT - 20480 * (jj + 1), I.v * 1024>(l16, rws, so, qm0); });
            dmac<L_QT - 20480 * (jj + 1) + 4096>(l16, rws, so + 4096, qm0);
        };
        const u32 fqo = WS_FQ + sg * 1024 + g4 * 256 + l15 * 4;
        bf16x8 xf[10][2][2];
        f32x4 gacc[2];
        u32 pv = 0;
        bool qok = true;
        auto wojob = [&](auto ST) {
#pragma unroll
            for (u32 j = 0; j < 2; ++j) {
                const u32 p = j * 1024 + b * 4 + wr;
                const bool br = p >= 800;
                const u32 q = br ? p - 800 : p, oct = q / 10, cl = q - oct * 10;
                const u32 kx = oct >> 2, go = oct & 3, G = kx / 5, e = kx - G * 5;
                if constexpr (!ST.v) {
                    const u32 r0 = 160 * G + (e == 4 ? 20 * go + 16 : 40 * e + (br ? 20 * (go >> 1) + 4 * (go & 1) : 4 * go));
                    const u32 step = e == 4 ? 80 : br ? 8 : 20;
                    dma<L_SCR>(p < 1600 ? (((lane >> 3) & 3) + step * hf) * 1280 + (lane & 7) * 16 : OOB, mkrs(br ? woa : wob, 640 * 1280),
                               p < 1600 ? r0 * 1280 + cl * 128 : 0, wr * 2048 + j * 1024);
                } else {
                    const u32 n = 64 * cl + lane, h = (n * 205) >> 12, d = n - 20 * h;
                    const u32 jd = p < 1600 ? WS_WO + ((h >> 2) * 2 + br) * 102400 + (kx * 5 + (d >> 2)) * 1024 + (go * 16 + 4 * (h & 3) + (d & 3)) * 16 : OOB;
                    const u32 js = L_SCR + wr * 2048 + j * 1024 + (l15 >> 2) * 128 + (4 * g4 + (lane & 3)) * 8;
                    st128(__builtin_bit_cast(u32x4, ldtr2(js, js + 512)), jd, rws, 0);
                }
            }
        };
        constexpr int XA[10] = {0, 2, 0, 0, 2, 0, 1, 0, 0, 0}, XB[10] = {0, 0, 0, PFQ == 3, PFQ == 4, PFQ == 5, 0, 0, 0, 0};
        sfor<0, 10>([&](auto SC) {
            constexpr int s = decltype(SC)::v;
            wait_lgkm<0>();
            constexpr int ex = s < 2 ? 0 : s < 4 ? XA[1] : XB[s - 3] + XA[s - 2] + XB[s - 2] + XA[s - 1] + XB[s - 1];
            if constexpr (s < 8) wait_vm<9 * (UP[s] - s - 1) + ex - (s == FSB && s < 7 ? XA[4] : 0)>();
            if constexpr (s == 9) { if (qok) wait_vm<15>(); else wait_vm<0>(); }
            bar();
            if constexpr (s == 0) __builtin_amdgcn_s_setprio(HPRIO);
            STAMP(2 + s);
            if constexpr (s == 0 || s == 2 || s == 4 || s == 7) STAMPC(s == 0 ? 51 : s == 2 ? 52 : s == 4 ? 53 : 54);
            constexpr bool up = SL[s % 4] >= 2 * SLOT, any = UP[s + 1] > UP[s];
            constexpr u32 xo = SL[s % 4] - (up ? 2 * SLOT : 0);
            const u32 a0 = up ? xc0 : xb0, a1 = up ? xc1 : xb1;
            if constexpr (s == 1) wojob(IC<0>{});
            if constexpr (s == 4) wojob(IC<1>{});
            if constexpr (s == 5) tag = rfl(lds32(L_TAG));
            if constexpr (s == 6) pv = ldflag(fqo, rws);
            if constexpr (s == 7) { qok = wait_eq<9>(pv, tag); if (qok) qissue(IC<0>{}); }
            if constexpr (s == 8) { if (qok) qissue(IC<1>{}); }
            if constexpr (s == 9) { if (qok) qissue(IC<3>{}); }
            if constexpr (any) issuep(IC<UP[s]>{}, IC<0>{}, IC<HA>{});
            xf[s][0][0] = asbf8(lds128(a0 + xo)); xf[s][0][1] = asbf8(lds128(a0 + xo + 2048));
            xf[s][1][0] = asbf8(lds128(a1 + xo)); xf[s][1][1] = asbf8(lds128(a1 + xo + 2048));
            const bf16x8 w0 = asbf8(lds128(ga + s * 128)), w1 = asbf8(lds128(ga + s * 128 + 64));
            if constexpr (any) issuep(IC<UP[s]>{}, IC<HA>{}, IC<HB>{});
            mfma16a<s == 0>(gacc[0], w0, xf[s][0][0]); mfma16a<s == 0>(gacc[1], w0, xf[s][0][1]);
            mfma16a(gacc[0], w1, xf[s][1][0]); mfma16a(gacc[1], w1, xf[s][1][1]);
            if constexpr (any) { issuep(IC<UP[s]>{}, IC<HB>{}, IC<9>{}); sfor<UP[s] + 1, UP[s + 1]>(issue); }
            if constexpr (s == PFQ) pref(lane < 25 ? lane * 128 : OOB, rws, WS_WQ + sg * 409600 + (idx * 4 + wr) * 3200);
            if constexpr (s == 8) { if (qok) qissue(IC<2>{}); }
            if constexpr (s == 9) { if (qok) qissue(IC<4>{}); }
        });
        __builtin_amdgcn_s_setprio(QPRIO);
        if (!qok) { spin(fqo, rws, tag); sfor<0, 5>(qissue); }
        asm volatile("s_nop 7\n\ts_nop 3" : "+v"(gacc[0]), "+v"(gacc[1]));
        wait_vm<20 + 2 * PFO>();
        ctl_set(me, 1);
        const u32 lhi = opaque_v(l16 + L_QT - 51200);
        ctl_wait(L_HQ, 1);
        STAMP(12);
        STAMPC(55);
        constexpr int PD = 4;
        bf16x8 wf[PD + 1];
        f32x4 qa[2][5];
        u32x4 snap, fs;
        u32 vw;
        auto wadr = [&](int n) {
            const int j = n / 10;
            return (j < 5 ? lhi + 40960 - 10240 * j : l16 + L_QT - 10240 * (j + 1)) + (n % 10) * 1024;
        };
        sfor<0, PD>([&](auto NC) { wf[NC.v] = asbf8(lds128(wadr(NC.v))); });
        sfor<0, 100>([&](auto NC) {
            constexpr int n = decltype(NC)::v, m = n + PD;
            constexpr int st = n / 10, ks = (n % 10) / 5, a = n % 5;
            const bf16x8 w = wf[n % (PD + 1)];
            if constexpr (m < 100 && m % 20 == 0) { if (!ctl_ge(snap, m / 20 + 1)) ctl_wait(L_HQ, m / 20 + 1); cfence(); }
            sched0();
            mfma16a<(n < 5)>(qa[0][a], w, xf[st][ks][0]);
            sched0();
            if constexpr (m < 100) wf[m % (PD + 1)] = asbf8(lds128(wadr(m)));
            sched0();
            mfma16a<(n < 5)>(qa[1][a], w, xf[st][ks][1]);
            sched0();
            if constexpr (n < 80 && n % 20 == 8) {
                wait_vm<15 + 2 * PFO - 5 * (n / 20)>();
                ctl_set(me, n / 20 + 2);
            }
            if constexpr (n < 80 && n % 20 == 12) snap = ctl_get(L_HQ);
            if constexpr (n == 50 && HWN < 200) fs = ldflag4(fso, rws);
            if constexpr (n == 69 && HWN < 200) { wok = wait_eq4<0>(fs, tag); vw = wok ? l16 : OOB; }
            if constexpr (n >= HWN && (n - HWN) % HWS == 0 && (n - HWN) / HWS < 25) wld(IC<(n - HWN) / HWS>{}, vw);
            sched0();
        });
        constexpr int HW0 = HWN >= 100 ? 0 : (100 - HWN + HWS - 1) / HWS;
        if constexpr (HWN < 200) sfor<HW0, 25>([&](auto I) { wld(I, vw); });
        else sfor<0, 25>([&](auto I) { undef(W[I.v]); });
        mfma_pad();
        STAMP(13);
        STAMPC(56);
        g0 = __builtin_amdgcn_rcpf(1.f + __builtin_amdgcn_exp2f(-gacc[0][0] * LOG2E));
        g1 = __builtin_amdgcn_rcpf(1.f + __builtin_amdgcn_exp2f(-gacc[1][0] * LOG2E));
        gt = (lane & 16) ? g1 : g0;
        {
            auto cvt = [&](int a, u32x4& o) {
                auto s0 = __builtin_amdgcn_permlane16_swap(pk2(qa[0][a][0] * QSCALE, qa[0][a][1] * QSCALE), pk2(qa[1][a][0] * QSCALE, qa[1][a][1] * QSCALE), false, false);
                auto s1 = __builtin_amdgcn_permlane16_swap(pk2(qa[0][a][2] * QSCALE, qa[0][a][3] * QSCALE), pk2(qa[1][a][2] * QSCALE, qa[1][a][3] * QSCALE), false, false);
                o = u32x4{s0[0], s1[0], s0[1], s1[1]};
            };
            u32x4 r, q1;
            cvt(4, q1); cvt(2, r);
            sts128(L_XQ + wr * 1536 + l16, q1);
            sts64(L_XQ + wr * 1536 + 1024 + lane * 8, u32x2{hf ? f2u(gt) : r[2], r[3]});
            ctl_set(me, 6);
            cvt(3, qf);
            qr = hf ? u32x2{0, 0} : u32x2{r[0], r[1]};
        }
#pragma unroll
        for (int t = 0; t < 2; ++t) {
            P[t] = u32x4{phi2(qa[t][0][0], qa[t][0][1]), phi2(qa[t][0][2], qa[t][0][3]), phi2(qa[t][1][0], qa[t][1][1]), phi2(qa[t][1][2], qa[t][1][3])};
            const u32 u = phi2(qa[t][2][0], qa[t][2][1]), v = phi2(qa[t][2][2], qa[t][2][3]);
            P[2][2 * t] = __builtin_amdgcn_permlane32_swap(u, u, false, false)[1];
            P[2][2 * t + 1] = __builtin_amdgcn_permlane32_swap(v, v, false, false)[1];
        }
        asm volatile("" : "+v"(P[0]), "+v"(P[1]), "+v"(P[2]), "+v"(qf), "+v"(qr));
        asetup(0);
        if constexpr (HWN < 200) wlate();
        STAMP(14);
        __builtin_amdgcn_s_setprio(0);
        ctl_wait(L_MC, 3);
        STAMP(16);
        STAMPC(57);
    } else {
        const u32 wm = wr >> 1, wn = wr & 1;
        const u32 ev = ldflag(0, rws);
        const u32 pw0 = (xcd & 1) * 128 + idx * 4 + wr;
        u32 qd[2];
#pragma unroll
        for (u32 j = 0; j < 2; ++j) {
            const u32 p = pw0 + 256 * j, isb = p >= 200, q = isb ? p - 200 : p, rg = (q * 205) >> 10;
            const u32 so = p < 400 ? rg * 61440 + sg * 320 + (q - rg * 5) * 64 : 0;
            dma<L_SCR>(p < 400 ? (lane >> 2) * 3840 + (lane & 3) * 16 : OOB, mkrs(isb ? wb : wa, 640 * 3840), so, wr * 2048 + j * 1024);
            const u32 n = (q - rg * 5) * 32 + l31, h = (n * 205) >> 12, d = n - 20 * h, o8 = 2 * rg + hf;
            const u32 t = d < 16 ? (h & 1) * 1024 + (isb ? 0 : 3072) + d * 16 : 2048 - 256 + (8 * isb + 4 * (h & 1) + d) * 16;
            qd[j] = p < 400 ? WS_WQ + (4 * sg + (h >> 1)) * 102400 + (o8 >> 2) * 5120 + (o8 & 3) * 256 + t : OOB;
        }
        const u32 krec = WS_KVP + ((2 * hg + wn) * 16 + rb) * 2048 + wm * 1024;
        if (lane < 2) sts32(L_HQ + lane * 16 + wr * 4, 0);
        u32 wb_[2][3], xb_[2][2];
        {
            const u32 q3 = lane & 3, wrow = (g4 & 1) * WREG + (8 * (g4 >> 1) + (l15 >> 2)) * 320;
            wb_[0][0] = wrow + (ty ? 80 : 160) * wn + q3 * 8;
            wb_[0][1] = wb_[0][0] + (ty ? 160 : 80);
            wb_[0][2] = wrow + 32 + (ty ? 80 * wn + ((q3 & 2) ? 0 : 160) + 40 * (q3 & 1) : 160 * wn + 40 * q3);
            xb_[0][0] = XO + (64 * wm + 16 * (l15 >> 3) + (l15 & 7)) * 128 + ((g4 ^ (l15 & 6)) * 16);
            xb_[0][1] = xb_[0][0] ^ 64;
#pragma unroll
            for (int i = 0; i < 3; ++i) wb_[1][i] = opaque_v(wb_[0][i] + 2 * SLOT);
#pragma unroll
            for (int i = 0; i < 2; ++i) xb_[1][i] = opaque_v(xb_[0][i] + 2 * SLOT);
        }
        const u32 qsrc = L_SCR + wr * 2048 + (8 * hf + (l15 >> 2)) * 64 + (g4 & 1) * 32 + (lane & 3) * 8;
        auto wad = [&](int s, int m) { return wb_[SL[s % 4] >= 2 * SLOT][(m % 5) >> 1] + SL[s % 4] % (2 * SLOT) + (m / 5) * (2 * WREG) + ((m % 5) & 1) * 40; };
        auto xad = [&](int s, int ks, int t) { return xb_[SL[s % 4] >= 2 * SLOT][ks] + SL[s % 4] % (2 * SLOT) + (t >> 1) * 4096 + (t & 1) * 1024; };
        f32x4 acc[4][5];
        STAMP(30);
        tag = wait_rfl<0>(ev) + TAGC;
        STAMP(31);
        st128(__builtin_bit_cast(u32x4, ldtr2(qsrc, qsrc + 256)), qd[0], rws, 0);
        st128(__builtin_bit_cast(u32x4, ldtr2(qsrc + 1024, qsrc + 1280)), qd[1], rws, 0);
        if (wr == 0 && lane == 0) sts32(L_TAG, tag);
        {
            bf16x8 wf[5], xa[2][4];
            STAMP(1);
            wait_lgkm<0>();
            bar();
            STAMP(2);
            sfor<0, 4>([&](auto I) { xa[0][I.v] = asbf8(lds128(xad(0, 0, I.v))); });
            wf[0] = ldtr2(wad(0, 0), wad(0, 0) + 1280);
            wf[1] = ldtr2(wad(0, 1), wad(0, 1) + 1280);
            sfor<0, 100>([&](auto NC) {
                constexpr int s = decltype(NC)::v / 10, n = decltype(NC)::v % 10, ks = n / 5, c = n % 5;
                constexpr int s2 = n >= 8 ? s + 1 : s, m = (n + 2) % 10;
                constexpr bool more = s2 < 10, first = s == 0 && ks == 0, rdw = more && (!(XPM & 1) || s == 0), rdx = !(XPM & 2) || s == 0;
                bf16x4 lo, hi;
                bf16x8 t0, t1;
                auto mm = [&](auto I) { sched0(); if constexpr (!(XPM & 4) || s == 0) mfma16a<first>(acc[I.v][c], xa[ks][I.v], wf[c]); else asm volatile("" :: "v"(xa[ks][I.v]), "v"(wf[c])); sched0(); };
                mm(IC<0>{});
                if constexpr (n == 8 && more) {
                    wait_lgkm<0>();
                    if constexpr (s == FQS) wait_vm<0>();
                    bar();
                    sched0();
                }
                if constexpr (rdw) lo = ldtr(wad(s2, m));
                mm(IC<1>{});
                if constexpr (rdw) hi = ldtr(wad(s2, m) + 1280);
                mm(IC<2>{});
                if constexpr (n >= 8 && more && rdx) t0 = asbf8(lds128(xad(s2, 0, 2 * (n - 8))));
                if constexpr (n < 4 && rdx) xa[1][n] = asbf8(lds128(xad(s, 1, n)));
                mm(IC<3>{});
                if constexpr (n >= 8 && more && rdx) { t1 = asbf8(lds128(xad(s2, 0, 2 * (n - 8) + 1))); xa[0][2 * (n - 8)] = t0; xa[0][2 * (n - 8) + 1] = t1; }
                if constexpr (s == FQS && n == 8) { if (wr == 0) stflag(tag, WS_FQ + sg * 1024 + (idx >> 3) * 256 + ((idx & 7) * 2 + (xcd & 1)) * 4, rws); }
                if constexpr (s == FSB - 1 && n == 8) { if (wr == 1) stflag(tag, WS_FS + (b >> 4) * 256 + (b & 15) * 4, rws); }
                if constexpr (rdw) wf[m % 5] = cat8(lo, hi);
                sched0();
            });
        }
        mfma_pad();
        __builtin_amdgcn_s_setprio(PRIO);
        STAMP(12);
        auto pk8 = [&](int u, int c) {
            return u32x4{pk2(acc[2 * u][c][0], acc[2 * u][c][1]), pk2(acc[2 * u][c][2], acc[2 * u][c][3]),
                         pk2(acc[2 * u + 1][c][0], acc[2 * u + 1][c][1]), pk2(acc[2 * u + 1][c][2], acc[2 * u + 1][c][3])};
        };
        auto ph8 = [&](int u, int c) {
            return u32x4{phi2(acc[2 * u][c][0], acc[2 * u][c][1]), phi2(acc[2 * u][c][2], acc[2 * u][c][3]),
                         phi2(acc[2 * u + 1][c][0], acc[2 * u + 1][c][1]), phi2(acc[2 * u + 1][c][2], acc[2 * u + 1][c][3])};
        };
        auto markers = [&]() {
            const u32x4 mk4 = {~0u, ~0u, ~0u, ~0u};
            sfor<0, 4>([&](auto I) { st128(mk4, l16, rws, oprod + c4 * 1024 + I.v * 20480); });
            st128(mk4, (g4 >> 1) * 40960 + (g4 & 1) * 20480 + l15 * 16, rws, oprod + 4096 + c4 * 256);
        };
        if (!ty) {
            const u32x4 fs = ldflag4(fso, rws);
            const u32 so = WS_T + ((4 * hg + 2 * wn) * 64 + 4 * rb + 2 * wm) * 2560;
            const u32 vm = g4 * 256 + ((l15 ^ (4 * g4)) * 16);
            const u32 vr = 2048 + g4 * 128 + ((((l15 >> 2) ^ (g4 >> 1)) & 1) * 64) + (l15 & 3) * 16 + (l15 >> 3) * 163840;
            sfor<0, 10>([&](auto I) {
                constexpr int c = decltype(I)::v >> 1, u = decltype(I)::v & 1;
                st128<(c < 4 ? (c & 1) * 1024 : 0) + u * 2560>(pk8(u, c), c < 4 ? vm : vr, rws, so + (c == 2 || c == 3 ? 163840 : 0));
            });
            markers();
            wok = wait_eq4<5>(fs, tag);
            stflag(tag, WS_FT + ((hg * 2 + wn) * 32 + 2 * rb + wm) * 128, rws);
            const u32 vw = wok ? l16 : OOB;
            sfor<0, 25>([&](auto I) { wld(I, vw); });
        } else {
            const u32x4 fs = ldflag4(fso, rws);
            markers();
            if constexpr (PFW) {
                const u32 po = WS_WO + (((idx >> 2) * 2 + (idx & 1)) * 4 + wr) * 25600;
                pref(lane * 128, rws, po); pref(lane * 128, rws, po + 8192); pref(lane * 128, rws, po + 16384); pref(lane < 8 ? lane * 128 : OOB, rws, po + 24576);
            }
            f32x4 Q0 = {0, 0, 0, 0}, Q1 = Q0, X0 = Q0, X1 = Q0, Y0 = Q0, Y1 = Q0, U = Q0;
#pragma unroll
            for (int u = 0; u < 2; ++u) {
                const bf16x8 v0 = asbf8(pk8(u, 2)), v1 = asbf8(pk8(u, 3));
                const u32x4 rr = pk8(u, 4);
                const bf16x8 rbv = asbf8(l15 < 8 ? rr : u32x4{ONES, ONES, ONES, ONES});
                const bf16x8 k0 = asbf8(ph8(u, 0));
                Q0 = mfma16(k0, v0, Q0); X0 = mfma16(k0, rbv, X0);
                const bf16x8 k1 = asbf8(ph8(u, 1));
                Q1 = mfma16(k1, v1, Q1); X1 = mfma16(k1, rbv, X1);
                const bf16x8 ra = asbf8(ph8(u, 4));
                Y0 = mfma16(ra, v0, Y0); Y1 = mfma16(ra, v1, Y1); U = mfma16(ra, rbv, U);
                if (u == WU) {
                    sched0();
                    wok = wait_eq4<5 + 4 * PFW>(fs, tag);
                    const u32 vw = wok ? l16 : OOB;
                    sfor<0, 25>([&](auto I) { wld(I, vw); });
                    sched0();
                }
            }
            f32x4 X = (l15 & 4) ? X1 : X0, Z;
#pragma unroll
            for (int r = 0; r < 4; ++r) Z[r] = u2f(__builtin_amdgcn_permlane32_swap(f2u((g4 & 1) ? Y1[r] : Y0[r]), f2u(U[r]), false, false)[1]);
            sts128(L_SCR + wr * 2048 + l16, __builtin_bit_cast(u32x4, wm ? Q0 : X));
            sts128(L_SCR + wr * 2048 + 1024 + l16, __builtin_bit_cast(u32x4, wm ? Q1 : Z));
            ctl_set(me, 1);
            ctl_wait1(L_MC + (wr ^ 2) * 4, 1);
            const f32x4 a = (wm ? X : Q0) + __builtin_bit_cast(f32x4, lds128(L_SCR + (wr ^ 2) * 2048 + l16));
            const f32x4 c = (wm ? Z : Q1) + __builtin_bit_cast(f32x4, lds128(L_SCR + (wr ^ 2) * 2048 + 1024 + l16));
            st128(u32x4{pk2(a[0], a[1]), pk2(a[2], a[3]), pk2(c[0], c[1]), pk2(c[2], c[3])}, l16, rws, krec);
        }
        STAMP(13);
        if constexpr (CPF) {
            u64 pc; u32 len; int rel;
            asm volatile("s_getpc_b64 %0\n.Lcode_p2:\n\ts_mov_b32 %2, .Lcode_at-.Lcode_p2\n\ts_mov_b32 %1, .Lcode_hi-.Lcode_at" : "=s"(pc), "=s"(len), "=s"(rel));
            pc += (long long)rel;
            u32 o = wr * 8192 + lane * 128;
            o = o < len ? o : 0;
            asm volatile("s_mov_b32 m0, %2\n\ts_nop 0\n\tglobal_load_lds_dword %0, %1" :: "v"(o), "s"(pc), "i"(L_DUMP) : "memory");
        }
        {
            const int g0 = 2 * (int)rb - 4 + 3 * (int)wn;
            int gl = g0 + (int)(lane < 2 ? lane : 2);
            gl = gl < 0 ? 0 : gl;
            spin(WS_FT + ((wm * 8 + (hp >> 1)) * 32 + gl) * 128, rws, tag);
            ctl_set(me, 2);
            ctl_wait8(2);
            if (wr == 0) stflag(tag, WS_F2 + rb * 256 + hp * 4, rws);
            STAMP(14);
#pragma unroll
            for (int i = 0; i < 3; ++i) {
                const u32 mb = wm ? L_V + (3 * wn + i) * 5136 : L_K + (3 * wn + i) * 5120;
                if (g0 + i >= 0) {
                    const u32 so = WS_T + ((wm * 16 + hp) * 32 + g0 + i) * 5120;
                    dmac<0, 0>(l16, rws, so, mb); dmac<0, 1024>(l16, rws, so, mb); dmac<0, 2048>(l16, rws, so, mb); dmac<0, 3072>(l16, rws, so, mb);
                    dmac<4096, 0>(l16, rws, so + 4096, mb);
                } else {
#pragma unroll
                    for (int k = 0; k < 5; ++k) sts128(mb + k * 1024 + l16, u32x4{0, 0, 0, 0});
                }
            }
            if (wm && lane < 12) sts32(L_V + (3 * wn + (lane >> 2)) * 5136 + 5120 + (lane & 3) * 4, g0 + (int)(lane >> 2) >= 0 ? ONES : 0);
            asetup(1);
            asm volatile("" : "=v"(P[0]), "=v"(P[1]), "=v"(P[2]));
            if constexpr (HWN >= 200) wlate();
            wait_vm<0>();
            ctl_set(me, 3);
            if constexpr (HWN < 200) wlate();
            STAMP(15);
        }
        ctl_wait(L_HQ, 6);
        qf = lds128(L_XQ + wr * 1536 + l16);
        const u32x2 r = lds64(L_XQ + wr * 1536 + 1024 + lane * 8);
        qr = hf ? u32x2{0, 0} : r;
        gt = u2f(lds32(L_XQ + wr * 1536 + 1280 + l31 * 8));
        ctl_wait(L_MC, 3);
        if (b == 0 && wr == 0) stflag(tag, WS_E, rws);
        __builtin_amdgcn_s_setprio(0);
        STAMP(16);
        asm volatile(".Lcode_hm:");
    }
    asm volatile(".Lcode_at:");
    auto recs = [&]() {
        const u32 vo = hf * 2048 + l31 * 16, so = WS_KVP + hp * 32768 + wr * 512;
        sfor<0, 8>([&](auto I) { dmac<L_RC + I.v * 1024>(vo, rws, so + I.v * 4096, wr * 8192); });
    };
    u32 pv;
    bool rok = false;
    {
        const u32 hh = wid < 4;
        f32x16 zero;
#pragma unroll
        for (int i = 0; i < 16; ++i) zero[i] = 0.f;
        const bf16x8 q16 = asbf8(qf);
        const bf16x4 q4 = __builtin_bit_cast(bf16x4, qr);
        auto qk = [&](u32 o) { return mfma32k8(ldtr(kr + o), q4, mfma32(ldtr2(ka + o, kc + o), q16, zero)); };
        f32x16 o = zero;
        sched0();
        bf16x8 j16 = ldtr2(ka + 2560, kc + 2560), v2, v3;
        bf16x4 j4 = ldtr(kr + 2560);
        bf16x8 k16 = ldtr2(ka + 5120, kc + 5120);
        bf16x4 k4 = ldtr(kr + 5120);
        bf16x8 v0 = asbf8(lds128(va[1][0])), v1 = asbf8(lds128(va[1][1]));
        sched0();
        f32x16 S = mfma32k8(j4, q4, mfma32(j16, q16, zero)), Sn, S8;
        sfor<1, 9>([&](auto I) {
            constexpr int it = decltype(I)::v, i = it & 7, nx = (it + 1) & 7;
            auto wl = [&](auto J) {
                if constexpr (HWN >= 200) { if (wid >= 4) rl128<(J.v & 3) * 1024>(W[J.v], l16, rws, WS_WO + c80 * 204800 + wid * 25600 + (J.v >> 2) * 4096); }
            };
            float e[16];
            u32 p[8];
            auto ex = [&](auto A, auto B) {
                sfor<A.v, B.v>([&](auto R) {
                    if constexpr (i == 0) e[R.v] = __builtin_amdgcn_exp2f(__builtin_amdgcn_inverse_ballot_w64(lem((R.v & 3) + 8 * (R.v >> 2))) ? S8[R.v] : S[R.v]);
                    else e[R.v] = __builtin_amdgcn_exp2f(S[R.v]);
                });
                sched0();
            };
            auto cv = [&](int a) {
#pragma unroll
                for (int j = a; j < a + 4; ++j) p[j] = pk2(e[2 * j], e[2 * j + 1]);
                sched0();
            };
            sched0();
            if constexpr ((ASM >> it) & 1) STAMP(35 + it);
            if constexpr (it == HK1) pv = ldflag(f2o, rws);
            if constexpr (it == HK2) {
                if (wid < 4) { rok = wait_eq<0>(pv, tag); if (rok) recs(); }
                else rok = wait_eq<(HWN >= 200 ? 3 * (HK2 - HK1) : 0)>(pv, tag);
            }
            if constexpr (it == 1) wl(IC<0>{});
            sched0();
            if constexpr (i != 0) Sn = mfma32(k16, q16, zero);
            sched0();
            ex(IC<0>{}, IC<8>{});
            if constexpr (i != 0) Sn = mfma32k8(k4, q4, Sn);
            sched0();
            if constexpr (it < 7) { k16 = ldtr2(ka + ((it + 2) & 7) * 2560, kc + ((it + 2) & 7) * 2560); k4 = ldtr(kr + ((it + 2) & 7) * 2560); }
            if constexpr (it == 5) { j16 = ldtr2(ka + 8 * 2560, kc + 8 * 2560); j4 = ldtr(kr + 8 * 2560); }
            if constexpr (it == 7) { v2 = asbf8(lds128(va[0][0] + 4 * 5136)); v3 = asbf8(lds128(va[0][1] + 4 * 5136)); }
            wl(IC<3 * it - 2>{});
            sched0();
            cv(0);
            if constexpr (i == 0) { o = mfma32(v2, asbf8(u32x4{p[0] & mk[0], p[1] & mk[1], p[2] & mk[2], p[3] & mk[3]}), o); sched0(); }
            ex(IC<8>{}, IC<12>{});
            if constexpr (i == 0) o = mfma32(v0, asbf8(u32x4{p[0] & ~mk[0], p[1] & ~mk[1], p[2] & ~mk[2], p[3] & ~mk[3]}), o);
            else o = mfma32(v0, asbf8(u32x4{p[0], p[1], p[2], p[3]}), o);
            sched0();
            wl(IC<3 * it - 1>{});
            if constexpr (it == 6) S8 = mfma32(j16, q16, zero);
            sched0();
            ex(IC<12>{}, IC<16>{});
            cv(4);
            if constexpr (i == 0) {
                o = mfma32(v3, asbf8(u32x4{p[4] & mk[4], p[5] & mk[5], p[6] & mk[6], p[7] & mk[7]}), o);
                o = mfma32(v1, asbf8(u32x4{p[4] & ~mk[4], p[5] & ~mk[5], p[6] & ~mk[6], p[7] & ~mk[7]}), o);
            } else o = mfma32(v1, asbf8(u32x4{p[4], p[5], p[6], p[7]}), o);
            sched0();
            if constexpr (it == 6) S8 = mfma32k8(j4, q4, S8);
            if constexpr (i != 0) { v0 = asbf8(lds128(va[nx & 1][0] + (nx >> 1) * 5136)); v1 = asbf8(lds128(va[nx & 1][1] + (nx >> 1) * 5136)); }
            wl(IC<3 * it>{});
            sched0();
            if constexpr (i != 0) S = Sn;
        });
        if constexpr ((ASM >> 9) & 1) STAMP(44);
        const int qq = (int)l31 - 4 * (int)hf;
        float am = 0.f;
#pragma unroll
        for (int r = 0; r < 12; r += 2) am = fmaxf(fmaxf(am, fabsf(o[r])), fabsf(o[r + 1]));
        const bool fine = o[12] > 0x1p-30f && o[12] < 0x1p30f && am < 0x1p120f;
        if (__builtin_amdgcn_ballot_w64(!fine) != 0ull) {
            const int i0 = rb >= 2 ? 0 : 8 - 4 * (int)rb - (int)wr;
            auto sc = [&](int i) {
                f32x16 s = qk(i * 2560);
#pragma unroll
                for (int r = 0; r < 16; ++r) {
                    const bool le = (r & 3) + 8 * (r >> 2) <= qq;
                    const bool ok = i == 0 ? !le : (i == 8 ? le : true);
                    s[r] = ok ? s[r] : -__builtin_inff();
                }
                return s;
            };
            float m = -__builtin_inff();
#pragma nounroll
            for (int i = i0; i < 9; ++i) {
                const f32x16 s = sc(i);
#pragma unroll
                for (int r = 0; r < 16; ++r) m = fmaxf(m, s[r]);
            }
            auto sw = __builtin_amdgcn_permlane32_swap(f2u(m), f2u(m), false, false);
            m = fmaxf(u2f(sw[0]), u2f(sw[1]));
            o = zero;
#pragma nounroll
            for (int i = i0; i < 9; ++i) {
                const f32x16 s = sc(i);
                u32 p[8];
#pragma unroll
                for (int j = 0; j < 8; ++j) p[j] = pk2(__builtin_amdgcn_exp2f(s[2 * j] - m), __builtin_amdgcn_exp2f(s[2 * j + 1] - m));
                const u32 vo_ = (i >> 1) * 5136;
                o = mfma32(asbf8(lds128(((i & 1) ? va[1][0] : va[0][0]) + vo_)), asbf8(u32x4{p[0], p[1], p[2], p[3]}), o);
                o = mfma32(asbf8(lds128(((i & 1) ? va[1][1] : va[0][1]) + vo_)), asbf8(u32x4{p[4], p[5], p[6], p[7]}), o);
            }
        }
        const float rs = __builtin_amdgcn_rcpf(o[12]) * gt;
        const u32 ob = oprod + 20480 + (l31 >> 4) * 40960;
        u32x4 v = {pk2m(o[0] * rs, o[1] * rs), pk2m(o[2] * rs, o[3] * rs), pk2m(o[4] * rs, o[5] * rs), pk2m(o[6] * rs, o[7] * rs)};
        __builtin_amdgcn_raw_buffer_store_b128(v, wsr, ob + c4 * 1024 + (2 * hh + hf) * 256 + l15 * 16, 0, 16);
        u32x2 vt = {pk2m(o[8] * rs, o[9] * rs), pk2m(o[10] * rs, o[11] * rs)};
        __builtin_amdgcn_raw_buffer_store_b64(vt, wsr, hf ? OOB : ob + 4096 + (2 * c4 + hh) * 128 + l15 * 8, 0, 16);
        cfence();
    }
    STAMP(17);
    STAMPC(58);
    if (!rok) spin(f2o, rws, tag);
    if (wid < 4) {
        if (rok) wait_vm<2>(); else { recs(); wait_vm<0>(); }
        float s[8];
#pragma unroll
        for (int i = 0; i < 8; ++i) {
            const u32x4 v = lds128(L_RC + wr * 8192 + i * 1024 + l16);
#pragma unroll
            for (int k = 0; k < 4; ++k) { s[2 * k] = i ? s[2 * k] + blo(v[k]) : blo(v[k]); s[2 * k + 1] = i ? s[2 * k + 1] + bhi(v[k]) : bhi(v[k]); }
        }
        float t[4];
#pragma unroll
        for (int k = 0; k < 4; ++k) {
            auto sw = __builtin_amdgcn_permlane32_swap(f2u(s[k]), f2u(s[4 + k]), false, false);
            t[k] = u2f(sw[0]) + u2f(sw[1]);
        }
        sts64(L_REC + wr * 512 + l31 * 16 + hf * 8, u32x2{pk2(t[0], t[1]), pk2(t[2], t[3])});
    } else {
        ctl_wait(L_MC, 10);
        STAMP(23);
        const u32x4 m = lds128(L_REC + l16), n = lds128(L_REC + 1024 + l16);
        const bool m1 = l15 & 4;
        const bf16x8 a0 = asbf8(u32x4{m[0], m[1], g4 == 0 ? n[2] : 0, g4 == 0 ? n[3] : 0});
        const bf16x8 a1 = asbf8(u32x4{m[2], m[3], g4 == 1 ? n[2] : 0, g4 == 1 ? n[3] : 0});
        const bf16x8 x0 = asbf8(u32x4{m1 ? 0 : n[0], m1 ? 0 : n[1], (g4 == 2 && !m1) ? n[2] : 0, (g4 == 2 && !m1) ? n[3] : 0});
        const bf16x8 x1 = asbf8(u32x4{m1 ? n[0] : 0, m1 ? n[1] : 0, (g4 == 3 && m1) ? n[2] : 0, (g4 == 3 && m1) ? n[3] : 0});
#pragma unroll
        for (int t2 = 0; t2 < 2; ++t2) {
            const bf16x8 b0 = asbf8(u32x4{P[t2][0], P[t2][1], P[2][2 * t2], P[2][2 * t2 + 1]});
            const bf16x8 b1 = asbf8(u32x4{P[t2][2], P[t2][3], P[2][2 * t2], P[2][2 * t2 + 1]});
            const f32x4 z = {0, 0, 0, 0};
            const f32x4 xx = mfma16(x1, b1, mfma16(x0, b0, z));
            const f32x4 n0 = mfma16(a0, b0, z), n1 = mfma16(a1, b1, z);
            const u32 rc = f2u(__builtin_amdgcn_rcpf(xx[0] + 1e-6f) * (1.f - (t2 ? g1 : g0)));
            const u32 rs = __builtin_amdgcn_permlane32_swap(rc, rc, false, false)[1];
            auto sw = __builtin_amdgcn_permlane16_swap(rs, rs, false, false);
            const float r0 = u2f(sw[0]), r1 = u2f(sw[1]), rr = u2f(rs);
            const u32 so = oprod + t2 * 40960;
            st128(u32x4{pk2m(n0[0] * r0, n0[1] * r0), pk2m(n0[2] * r0, n0[3] * r0), pk2m(n1[0] * r1, n1[1] * r1), pk2m(n1[2] * r1, n1[3] * r1)}, l16, rws, so + c4 * 1024);
            __builtin_amdgcn_raw_buffer_store_b64(u32x2{pk2m(xx[0] * rr, xx[1] * rr), pk2m(xx[2] * rr, xx[3] * rr)}, wsr, hf ? OOB : lane * 8, so + 4096 + c4 * 256, 16);
        }
        cfence();
    }
    ctl_set(me, 10);
    STAMP(24);
    {
        const u32 vr = lane * 8;
        auto ot = [&](u32 x) { return WS_O + (r64 * 4 + (wr ^ x)) * 40960 + wid * 5120; };
        auto oload = [&](OF& o, u32 s0, u32 s1) {
#pragma unroll
            for (int e = 0; e < 4; ++e) {
                o.m[0][e] = __builtin_amdgcn_raw_buffer_load_b128(wsr, l16 + e * 1024, s0, 16);
                o.m[1][e] = __builtin_amdgcn_raw_buffer_load_b128(wsr, l16 + e * 1024, s1, 16);
            }
#pragma unroll
            for (int h = 0; h < 2; ++h) {
                o.r[0][h] = __builtin_amdgcn_raw_buffer_load_b64(wsr, vr + h * 512, s0 + 4096, 16);
                o.r[1][h] = __builtin_amdgcn_raw_buffer_load_b64(wsr, vr + h * 512, s1 + 4096, 16);
            }
            cfence();
        };
        auto pass = [&](OF& o, f32x4 (&ac)[2][5], u32 s0, u32 s1) {
            u32 nx = 0, bad = 31;
#pragma unroll
            for (int i = 0; i < 10; ++i) asm volatile("" : "=v"(ac[i / 5][i % 5]));
#pragma nounroll
            for (u32 it = 0; it < (1u << 17); ++it) {
                const u32 chk = bad;
                bad = 32;
                sfor<0, 4>([&](auto E) {
                    if (chk & (1u << E.v)) {
                        if (marked(mx4(mx4(0, o.m[0][E.v]), o.m[1][E.v]))) {
                            rl128<E.v * 1024>(o.m[0][E.v], l16, rws, s0); rl128<E.v * 1024>(o.m[1][E.v], l16, rws, s1);
                            bad |= 1u << E.v;
                        }
                    }
                });
                if (marked(umax(umax(umax(o.r[0][0][0], o.r[0][0][1]), umax(o.r[0][1][0], o.r[0][1][1])), umax(umax(o.r[1][0][0], o.r[1][0][1]), umax(o.r[1][1][0], o.r[1][1][1]))))) {
                    rl64<0>(o.r[0][0], vr, rws, s0 + 4096); rl64<0>(o.r[1][0], vr, rws, s1 + 4096);
                    rl64<512>(o.r[0][1], vr, rws, s0 + 4096); rl64<512>(o.r[1][1], vr, rws, s1 + 4096);
                    bad |= 16;
                }
                const u32 lim = __builtin_ctz(bad);
                sfor<0, 5>([&](auto E) {
                    constexpr int e = decltype(E)::v;
                    if (e >= nx && e < lim) {
#pragma unroll
                        for (int t = 0; t < 2; ++t) {
                            const bf16x8 f = asbf8(e < 4 ? o.m[t][e & 3] : u32x4{o.r[t][0][0], o.r[t][0][1], o.r[t][1][0], o.r[t][1][1]});
#pragma unroll
                            for (int c = 0; c < 5; ++c) mfma16b<e == 0>(ac[t][c], asbf8(W[e * 5 + c]), f);
                        }
                    }
                });
                nx = lim;
                if (lim == 5) break;
                of_wait(o);
            }
            mfma_pad();
        };
        f32x4 ac[2][5];
        OF oa, ob;
        const u32 pw = (wid >= 4 ? wr * 20480 : L_K - 5120 + wr * 15360) + l16;
        if (OLATE) ctl_wait8(10);
        STAMP(25);
        oload(oa, ot(2), ot(3));
        oload(ob, ot(0), ot(1));
        of_tie(oa);
        pass(oa, ac, ot(2), ot(3));
        STAMP(18);
        ctl_wait8(10);
#pragma unroll
        for (int i = 0; i < 10; ++i) sts128(pw + 10240 + i * 1024, __builtin_bit_cast(u32x4, ac[i / 5][i % 5]));
        ctl_set(me, 11);
        of_tie(ob);
        pass(ob, ac, ot(0), ot(1));
        STAMP(19);
#pragma unroll
        for (int c = 0; c < 5; ++c) sts128(pw + 5120 + c * 1024, __builtin_bit_cast(u32x4, ac[1][c]));
        if (wid >= 4) {
#pragma unroll
            for (int c = 0; c < 5; ++c) sts128(pw + c * 1024, __builtin_bit_cast(u32x4, ac[0][c]));
            ctl_set(me, 12);
        } else {
            ctl_set(me, 12);
            auto add = [&](u32 a) {
#pragma unroll
                for (int c = 0; c < 5; ++c) ac[0][c] += __builtin_bit_cast(f32x4, lds128(a + l16 + c * 1024));
            };
            ctl_wait(L_MC, 12);
#pragma unroll
            for (u32 x = 1; x < 4; ++x) add(L_K - 5120 + (wr ^ x) * 15360 + x * 5120);
            STAMP(20);
            ctl_wait(L_HQ, 11);
#pragma unroll
            for (u32 x = 2; x < 4; ++x) add((wr ^ x) * 20480 + x * 5120);
            ctl_wait(L_HQ, 12);
            STAMP(21);
#pragma unroll
            for (u32 x = 0; x < 2; ++x) add((wr ^ x) * 20480 + x * 5120);
            u32 r[10];
#pragma unroll
            for (int c = 0; c < 5; ++c) { r[2 * c] = pk2(ac[0][c][0], ac[0][c][1]); r[2 * c + 1] = pk2(ac[0][c][2], ac[0][c][3]); }
            const __amdgpu_buffer_rsrc_t ors = __builtin_amdgcn_make_buffer_rsrc(out, 0, 2048 * 1280, 0x00020000);
            const u32 vo = l15 * 1280 + g4 * 40 + (r64 * 64 + wr * 16) * 1280 + c80 * 160;
            __builtin_amdgcn_raw_buffer_store_b128(u32x4{r[0], r[1], r[2], r[3]}, ors, vo, 0, OAUX);
            __builtin_amdgcn_raw_buffer_store_b128(u32x4{r[4], r[5], r[6], r[7]}, ors, vo + 16, 0, OAUX);
            __builtin_amdgcn_raw_buffer_store_b64(u32x2{r[8], r[9]}, ors, vo + 32, 0, OAUX);
        }
    }
    STAMP(22);
    STAMPC(63);
    asm volatile(".Lcode_hi:");
}

extern "C" void kernel_launch(void* const* d_in, const int* in_sizes, int n_in, void* d_out, int out_size, void* d_ws,
                              size_t ws_size, hipStream_t stream) {
    (void)in_sizes; (void)n_in; (void)out_size; (void)ws_size;
    (void)hipFuncSetAttribute((const void*)fused_kernel, hipFuncAttributeMaxDynamicSharedMemorySize, LDS_BYTES);
    fused_kernel<<<256, 512, LDS_BYTES, stream>>>((const u16*)d_in[0], (const u16*)d_in[1], (const u16*)d_in[2], (const u16*)d_in[4],
                                                  (const u16*)d_in[5], (const u16*)d_in[6], (u16*)d_out, (char*)d_ws);
}
```

```cpp
#include <hip/hip_runtime.h>
#include <stdint.h>
#pragma clang diagnostic ignored "-Wint-to-pointer-cast"

#define SPINS 1
#define PRIO 3
typedef unsigned short u16;
typedef unsigned int u32;
typedef unsigned long long u64;
typedef __attribute__((ext_vector_type(8))) __bf16 bf16x8;
typedef __attribute__((ext_vector_type(4))) __bf16 bf16x4;
typedef __attribute__((ext_vector_type(2))) __bf16 bf16x2;
typedef __attribute__((ext_vector_type(2))) float f32x2;
typedef __attribute__((ext_vector_type(4))) float f32x4;
typedef __attribute__((ext_vector_type(16))) float f32x16;
typedef __attribute__((ext_vector_type(4))) u32 u32x4;
typedef __attribute__((ext_vector_type(2))) u32 u32x2;
typedef __attribute__((ext_vector_type(4))) short s16x4;
#define AS3 __attribute__((address_space(3)))
#define DEVI __device__ __forceinline__

constexpr u32 WS_E   = 0;
constexpr u32 WS_FS  = 4096;
constexpr u32 WS_F2  = 12288;
constexpr u32 WS_FQ  = 8192;
constexpr u32 WS_FT  = 16384;
constexpr u32 WS_T   = 81920;
constexpr u32 WS_KVP = WS_T + 2 * 16 * 64 * 2560;
constexpr u32 WS_O   = WS_KVP + 16 * 16 * 2048;
constexpr u32 WS_WO  = WS_O + 128 * 40960;
constexpr u32 WS_WQ  = WS_WO + 16 * 102400;
constexpr u32 WS_END = WS_WQ + 16 * 102400;
constexpr u32 OOB = 0x80000000u;

constexpr u32 WREG  = 5152;
constexpr u32 XO    = 20608;
constexpr u32 SLOT  = 36992;
__device__ constexpr u32 SL[4] = {0, 3 * SLOT, 2 * SLOT, SLOT};
constexpr u32 L_SCR = 4 * SLOT;
constexpr u32 L_GW  = L_SCR + 8192;
constexpr u32 L_DUMP = 163328;
constexpr u32 L_REC = 0;
constexpr u32 L_XQ  = 2048;
constexpr u32 L_QT  = 3 * SLOT;
constexpr u32 L_K   = 101376;
constexpr u32 L_V   = 132096;
constexpr u32 L_RC  = 8192;
constexpr u32 L_HQ  = 162944, L_MC = L_HQ + 16, L_TAG = L_HQ + 32;
constexpr u32 LDS_BYTES = 163840;
static_assert(L_GW + 2048 <= L_HQ && L_DUMP >= L_TAG + 4 && L_DUMP + 256 <= LDS_BYTES && L_V + 6 * 5136 <= L_HQ && L_XQ + 6144 <= L_QT - 102400, "lds");

constexpr float LOG2E = 1.4426950408889634f;
constexpr float QSCALE = 0.22360679774997896f * 1.4426950408889634f;
constexpr u32 TAGC = 0x7F4A7C15u;
constexpr u32 ONES = 0x3F803F80u;

DEVI u32 pk2(float a, float b) { f32x2 v = {a, b}; bf16x2 r = __builtin_convertvector(v, bf16x2); return __builtin_bit_cast(u32, r); }
DEVI u32 f2u(float f) { return __builtin_bit_cast(u32, f); }
DEVI float u2f(u32 u) { return __builtin_bit_cast(float, u); }
DEVI float blo(u32 u) { return u2f(u << 16); }
DEVI float bhi(u32 u) { return u2f(u & 0xFFFF0000u); }
DEVI u32 umin(u32 a, u32 b) { return a < b ? a : b; }
DEVI u32 umax(u32 a, u32 b) { return a > b ? a : b; }
DEVI u32 pk2m(float a, float b) { return umin(pk2(a, b), 0xFFFFFFFEu); }
DEVI bf16x8 asbf8(u32x4 v) { return __builtin_bit_cast(bf16x8, v); }
DEVI f32x4 mfma16(bf16x8 a, bf16x8 b, f32x4 c) { return __builtin_amdgcn_mfma_f32_16x16x32_bf16(a, b, c, 0, 0, 0); }
template <bool FIRST = false> DEVI void mfma16a(f32x4& c, bf16x8 a, bf16x8 b) {
    if constexpr (FIRST) asm volatile("v_mfma_f32_16x16x32_bf16 %0, %1, %2, 0" : "=&v"(c) : "v"(a), "v"(b));
    else asm volatile("v_mfma_f32_16x16x32_bf16 %0, %1, %2, %0" : "+v"(c) : "v"(a), "v"(b));
}
template <bool FIRST> DEVI void mfma16b(f32x4& c, bf16x8 a, bf16x8 b) {
    if constexpr (FIRST) asm volatile("v_mfma_f32_16x16x32_bf16 %0, %1, %2, 0" : "+v"(c) : "v"(a), "v"(b));
    else asm volatile("v_mfma_f32_16x16x32_bf16 %0, %1, %2, %0" : "+v"(c) : "v"(a), "v"(b));
}
DEVI void mfma_pad() { __builtin_amdgcn_sched_barrier(0); asm volatile("s_nop 7\n\ts_nop 3" ::: "memory"); __builtin_amdgcn_sched_barrier(0); }
DEVI f32x16 mfma32(bf16x8 a, bf16x8 b, f32x16 c) { return __builtin_amdgcn_mfma_f32_32x32x16_bf16(a, b, c, 0, 0, 0); }
DEVI f32x16 mfma32k8(bf16x4 a, bf16x4 b, f32x16 c) {
    return __builtin_amdgcn_mfma_f32_32x32x8bf16_1k(__builtin_bit_cast(s16x4, a), __builtin_bit_cast(s16x4, b), c, 0, 0, 0);
}
DEVI bf16x4 ldtr(u32 a) { return __builtin_amdgcn_ds_read_tr16_b64_v4bf16((AS3 bf16x4*)a); }
DEVI bf16x8 cat8(bf16x4 x, bf16x4 y) { return __builtin_shufflevector(x, y, 0, 1, 2, 3, 4, 5, 6, 7); }
DEVI bf16x8 ldtr2(u32 a, u32 b) { bf16x4 x = ldtr(a), y = ldtr(b); return cat8(x, y); }
DEVI u32x4 lds128(u32 a) { return *(AS3 u32x4*)a; }
DEVI u32x2 lds64(u32 a) { return *(AS3 u32x2*)a; }
DEVI u32 lds32(u32 a) { return *(AS3 u32*)a; }
DEVI void sts128(u32 a, u32x4 v) { *(AS3 u32x4*)a = v; }
DEVI void sts64(u32 a, u32x2 v) { *(AS3 u32x2*)a = v; }
DEVI void sts32(u32 a, u32 v) { *(AS3 u32*)a = v; }
DEVI u32 rfl(u32 v) { return __builtin_amdgcn_readfirstlane(v); }
DEVI u32 opaque_s(u32 v) { asm volatile("" : "+s"(v)); return v; }
DEVI u32 opaque_v(u32 v) { asm volatile("" : "+v"(v)); return v; }
DEVI void undef(u32x4& v) { asm volatile("" : "=v"(v)); }
DEVI void cfence() { asm volatile("" ::: "memory"); }

DEVI u32x4 mkrs(const void* p, u32 bytes) { return u32x4{(u32)(uintptr_t)p, (u32)((uintptr_t)p >> 32) & 0xFFFFu, bytes, 0x00020000u}; }
template <int MO, int OFF = 0> DEVI void dma(u32 voff, u32x4 rs, u32 soff, u32 mb) {
    asm volatile("s_add_u32 m0, %3, %5\n\ts_nop 0\n\tbuffer_load_dwordx4 %0, %1, %2 offen offset:%4 lds"
                 :: "v"(voff), "s"(rs), "s"(soff), "s"(mb), "i"(OFF), "i"(MO) : "memory", "scc");
}
template <int MO, int OFF = 0> DEVI void dmac(u32 voff, u32x4 rs, u32 soff, u32 mb) {
    asm volatile("s_add_u32 m0, %3, %5\n\ts_nop 0\n\tbuffer_load_dwordx4 %0, %1, %2 offen offset:%4 sc1 lds"
                 :: "v"(voff), "s"(rs), "s"(soff), "s"(mb), "i"(OFF), "i"(MO) : "memory", "scc");
}
DEVI void pref(u32 voff, u32x4 rs, u32 soff) {
    asm volatile("s_mov_b32 m0, %3\n\ts_nop 0\n\tbuffer_load_dword %0, %1, %2 offen sc1 lds" :: "v"(voff), "s"(rs), "s"(soff), "i"(L_DUMP) : "memory");
}
DEVI u32 ldflag(u32 voff, u32x4 rs) {
    u32 v;
    asm volatile("buffer_load_dword %0, %1, %2, 0 offen sc1" : "=v"(v) : "v"(voff), "s"(rs) : "memory");
    return v;
}
DEVI u32x4 ldflag4(u32 voff, u32x4 rs) {
    u32x4 v;
    asm volatile("buffer_load_dwordx4 %0, %1, %2, 0 offen sc1" : "=v"(v) : "v"(voff), "s"(rs) : "memory");
    return v;
}
DEVI void stflag(u32 val, u32 soff, u32x4 rs) {
    asm volatile("s_mov_b64 exec, 1\n\tbuffer_store_dword %0, off, %1, %2 sc1\n\ts_mov_b64 exec, -1" :: "v"(val), "s"(rs), "s"(soff) : "memory");
}
template <int OFF = 0> DEVI void st128(u32x4 v, u32 voff, u32x4 rs, u32 soff) {
    asm volatile("buffer_store_dwordx4 %0, %1, %2, %3 offen offset:%4 sc1\n\ts_nop 1" :: "v"(v), "v"(voff), "s"(rs), "s"(soff), "i"(OFF) : "memory");
}
template <int OFF = 0> DEVI void st128n(u32x4 v, u32 voff, u32x4 rs, u32 soff) {
    asm volatile("buffer_store_dwordx4 %0, %1, %2, %3 offen offset:%4\n\ts_nop 1" :: "v"(v), "v"(voff), "s"(rs), "s"(soff), "i"(OFF) : "memory");
}
template <int N> DEVI void wait_vm() { asm volatile("s_waitcnt vmcnt(%0)" ::"i"(N) : "memory"); }
template <int N> DEVI u32 wait_rfl(u32 v) { u32 r; asm volatile("s_waitcnt vmcnt(%2)\n\tv_readfirstlane_b32 %0, %1" : "=s"(r) : "v"(v), "i"(N) : "memory"); return r; }
template <int N> DEVI bool wait_eq(u32 v, u32 tag) {
    u64 m; asm volatile("s_waitcnt vmcnt(%3)\n\tv_cmp_ne_u32_e64 %0, %2, %1" : "=s"(m) : "v"(v), "s"(tag), "i"(N) : "memory");
    return m == 0ull;
}
template <int N> DEVI bool wait_eq4(u32x4 v, u32 tag) {
    asm volatile("s_waitcnt vmcnt(%1)" : "+v"(v) : "i"(N) : "memory");
    return __builtin_amdgcn_ballot_w64(((v[0] ^ tag) | (v[1] ^ tag) | (v[2] ^ tag) | (v[3] ^ tag)) != 0u) == 0ull;
}
template <int N> DEVI void wait_lgkm() { asm volatile("s_waitcnt lgkmcnt(%0)" ::"i"(N) : "memory"); }
DEVI void bar() { asm volatile("s_barrier" ::: "memory"); }
DEVI void sched0() { __builtin_amdgcn_sched_barrier(0); }
DEVI void spin(u32 voff, u32x4 rs, u32 tag) {
#pragma nounroll
    for (u32 i = 0; i < (1u << 17); ++i) {
        u32 v = ldflag(voff, rs);
        if (wait_eq<0>(v, tag)) break;
        __builtin_amdgcn_s_sleep(SPINS);
    }
}
DEVI void ctl_set(u32 a, u32 v) { asm volatile("s_mov_b64 exec, 1\n\tds_write_b32 %0, %1\n\ts_mov_b64 exec, -1" :: "v"(a), "v"(v) : "memory"); }
DEVI u32x4 ctl_get(u32 a) { return *(volatile AS3 u32x4*)a; }
DEVI bool ctl_ge(u32x4 v, u32 t) { return rfl(umin(umin(v[0], v[1]), umin(v[2], v[3]))) >= t; }
DEVI void ctl_wait(u32 a, u32 t) {
#pragma nounroll
    for (u32 i = 0; i < (1u << 20); ++i) { if (ctl_ge(ctl_get(a), t)) break; __builtin_amdgcn_s_sleep(1); }
    cfence();
}
DEVI void ctl_wait8(u32 t) {
#pragma nounroll
    for (u32 i = 0; i < (1u << 20); ++i) {
        const u32x4 x = ctl_get(L_HQ), y = ctl_get(L_MC);
        if (ctl_ge(u32x4{umin(x[0], y[0]), umin(x[1], y[1]), umin(x[2], y[2]), umin(x[3], y[3])}, t)) break;
        __builtin_amdgcn_s_sleep(1);
    }
    cfence();
}
DEVI void ctl_wait1(u32 a, u32 t) {
#pragma nounroll
    for (u32 i = 0; i < (1u << 20); ++i) { if (rfl(*(volatile AS3 u32*)a) >= t) break; __builtin_amdgcn_s_sleep(1); }
    cfence();
}
DEVI bool marked(u32 m) { return __builtin_amdgcn_ballot_w64(m == 0xFFFFFFFFu) != 0ull; }
DEVI u32 mx4(u32 m, u32x4 v) { return umax(umax(umax(m, v[0]), v[1]), umax(v[2], v[3])); }

DEVI float phi(float t) { return __builtin_amdgcn_fmed3f(t + 1.f, __builtin_amdgcn_exp2f(t * LOG2E), 1.f) + 1e-6f; }
DEVI u32 phi2(float a, float b) { return pk2(phi(a), phi(b)); }
struct OF { u32x4 m[2][4]; u32x2 r[2][2]; };
template <int OFF> DEVI void ul128(u32x4& v, u32 voff, u32x4 rs, u32 soff) {
    asm volatile("buffer_load_dwordx4 %0, %1, %2, %3 offen offset:%4 sc1" : "=v"(v) : "v"(voff), "s"(rs), "s"(soff), "i"(OFF) : "memory");
}
template <int OFF> DEVI void rl128(u32x4& v, u32 voff, u32x4 rs, u32 soff) {
    asm volatile("buffer_load_dwordx4 %0, %1, %2, %3 offen offset:%4 sc1" : "+v"(v) : "v"(voff), "s"(rs), "s"(soff), "i"(OFF) : "memory");
}
DEVI u32x4 ld128(u32 voff, u32x4 rs, u32 soff) {
    u32x4 v;
    asm volatile("buffer_load_dwordx4 %0, %1, %2, %3 offen" : "=v"(v) : "v"(voff), "s"(rs), "s"(soff) : "memory");
    return v;
}
template <int OFF> DEVI void rl64(u32x2& v, u32 voff, u32x4 rs, u32 soff) {
    asm volatile("buffer_load_dwordx2 %0, %1, %2, %3 offen offset:%4 sc1" : "+v"(v) : "v"(voff), "s"(rs), "s"(soff), "i"(OFF) : "memory");
}
#define OF_OPS(o) "+v"(o.m[0][0]), "+v"(o.m[0][1]), "+v"(o.m[0][2]), "+v"(o.m[0][3]), "+v"(o.m[1][0]), "+v"(o.m[1][1]), "+v"(o.m[1][2]), "+v"(o.m[1][3]), \
                  "+v"(o.r[0][0]), "+v"(o.r[0][1]), "+v"(o.r[1][0]), "+v"(o.r[1][1])
DEVI void of_wait(OF& o) { asm volatile("s_waitcnt vmcnt(0)" : OF_OPS(o) :: "memory"); }
DEVI void of_tie(OF& o) { asm volatile("" : OF_OPS(o) :: "memory"); }

#define STAMP(i)
#define STAMPC(i)

#define SPINTO(k)
template <int N> struct IC { static constexpr int v = N; };
template <int I, int N, class F> DEVI void sfor(F&& f) { if constexpr (I < N) { f(IC<I>{}); sfor<I + 1, N>(f); } }

#define ROTA 1
#define ROTB 3
#define UPTO {2, 4, 5, 6, 7, 8, 9, 10, 10, 10, 10}
#define HK1 3
#define HK2 5
#define HA 4
#define HB 6
#define XPM 0
#define HPRIO 3
#define QPRIO 0
#define PFQ -1
#define PFO 0
#define OLATE 1
#define FQS 1
#define CPF 0
#define ASM 0
#define FSB 6
#define HWN 200
#define HWS 1
#define PFW 0
#define WU 0
#define OAUX 0
#define XNOW 0
constexpr u64 lem(int k) { u64 m = 0; for (int l = 0; l < 64; ++l) if (k + 4 * (l >> 5) <= (l & 31)) m |= 1ull << l; return m; }
__device__ constexpr int UP[11] = UPTO;
__global__ __launch_bounds__(512, 2) void fused_kernel(const u16* __restrict__ x, const u16* __restrict__ wa, const u16* __restrict__ woa,
                                                       const u16* __restrict__ wb, const u16* __restrict__ wob, const u16* __restrict__ gw,
                                                       u16* out, char* __restrict__ ws) {
    const u32 lane = threadIdx.x & 63;
    const u32 wid = rfl(threadIdx.x >> 6);
    const u32 b = blockIdx.x, xcd = b & 7, idx = b >> 3;
    STAMP(0);
    STAMPC(62);
    if (wid == 3) {
        u64 pc; u32 len;
        asm volatile("s_getpc_b64 %0\n.Lcode_lo:\n\ts_mov_b32 %1, .Lcode_hi-.Lcode_lo\n\ts_max_u32 %1, %1, .Lcode_hm-.Lcode_lo" : "=s"(pc), "=s"(len) :: "scc");
        u32 o = (idx & 15) * 4096 + lane * 64;
        o = o < len ? o : 0;
        asm volatile("s_mov_b32 m0, %2\n\ts_nop 0\n\tglobal_load_lds_dword %0, %1" :: "v"(o), "s"(pc), "i"(L_DUMP) : "memory");
    }
    const u32 rb = (xcd & 1) * 8 + (idx >> 2), c4 = idx & 3, sg = xcd >> 1;
    const u32 ty = c4 >> 1;
    const u32 hg = ty ? 2 * sg + (c4 & 1) : sg + 4 * (c4 & 1);
    const u32 hp = 4 * sg + c4;
    const u32 wr = wid & 3;
    const u32x4 rws = mkrs(ws, WS_END);
    const __amdgpu_buffer_rsrc_t wsr = __builtin_amdgcn_make_buffer_rsrc(ws, 0, WS_END, 0x00020000);
    const u32 l15 = lane & 15, g4 = lane >> 4, l31 = lane & 31, hf = lane >> 5, l16 = lane * 16;
    const u32 me = (wid >= 4 ? L_HQ : L_MC) + wr * 4;
    const u32 oprod = WS_O + (rb * 8 + 2 * wr) * 40960 + sg * 5120;
    u32x4 qf; u32x2 qr; float gt;
    u32x4 P[3]; float g0, g1;
    u32 tag;
    const u32 r64 = xcd * 4 + (idx >> 3), c80 = idx & 7;
    u32 f2o;
    u32x4 W[25];
    bool wok;
    const u32 fso = WS_FS + (lane >> 2) * 256 + (lane & 3) * 16;
    auto wld = [&](auto I, u32 vw) __attribute__((always_inline)) { ul128<(I.v & 3) * 1024>(W[I.v], vw, rws, WS_WO + c80 * 204800 + wid * 25600 + (I.v >> 2) * 4096); };
    auto wlate = [&]() __attribute__((always_inline)) {
        if (!wok) {
#pragma nounroll
            for (u32 i = 0; i < (1u << 17); ++i) { if (wait_eq4<0>(ldflag4(fso, rws), tag)) break; __builtin_amdgcn_s_sleep(SPINS); }
            sfor<0, 25>([&](auto I) { rl128<(I.v & 3) * 1024>(W[I.v], l16, rws, WS_WO + c80 * 204800 + wid * 25600 + (I.v >> 2) * 4096); });
        }
    };
    u32 ka, kc, kr, va[2][2], mk[8];
    auto asetup = [&](u32 hh) __attribute__((always_inline)) {
        const u32 ch = 2 * (g4 & 1) + (lane & 1), kb = L_K + wr * 2560 + ((lane >> 1) & 1) * 8;
        ka = kb + hh * 1024 + ch * 256 + (((8 * hf + (l15 >> 2)) ^ (4 * ch)) * 16); kc = ka ^ 64;
        kr = kb + 2048 + ch * 128 + ((hh ^ (ch >> 1)) * 64) + (l15 >> 2) * 16;
#pragma unroll
        for (int p = 0; p < 2; ++p)
#pragma unroll
            for (int m = 0; m < 2; ++m) {
                const u32 bi = wr + p, c = 2 * m + hf;
                va[p][m] = L_V + (bi >> 1) * 5136 + (l31 < 20 ? (bi & 1) * 2560 + (l31 < 16 ? hh * 1024 + c * 256 + ((l31 ^ (4 * c)) * 16) : 2048 + c * 128 + ((hh ^ m) * 64) + (l31 - 16) * 16) : 5120);
            }
        sfor<0, 8>([&](auto J) {
            constexpr int k0 = ((2 * J.v) & 3) + 8 * ((2 * J.v) >> 2);
            mk[J.v] = (__builtin_amdgcn_inverse_ballot_w64(lem(k0)) ? 0xFFFFu : 0u) | (__builtin_amdgcn_inverse_ballot_w64(lem(k0 + 1)) ? 0xFFFF0000u : 0u);
        });
        f2o = WS_F2 + ((lane & 16) ? (r64 >> 1) * 256 + l15 * 4 : l15 * 256 + ((hp & 12) + 2 + ((hp >> 1) & 1)) * 4);
        asm volatile("" : "+v"(ka), "+v"(kc), "+v"(kr), "+v"(va[0][0]), "+v"(va[0][1]), "+v"(va[1][0]), "+v"(va[1][1]), "+v"(f2o), "+s"(out));
        asm volatile("" : "+v"(mk[0]), "+v"(mk[1]), "+v"(mk[2]), "+v"(mk[3]), "+v"(mk[4]), "+v"(mk[5]), "+v"(mk[6]), "+v"(mk[7]));
    };

    if (__builtin_expect(wid >= 4, 1)) {
        const u32 rot = (ROTA * rb + ROTB * c4) % 10;
        auto kbof = [&](u32 s) { u32 k = rot + s; return k >= 10 ? k - 10 : k; };
        const u32x4 rsx = mkrs(x, 2048 * 1280), rsw = mkrs(ty ? wb : wa, 640 * 3840);
        const u32 vx = (rb * 128 + 32 * wr + (lane >> 3)) * 1280 + (((lane & 7) ^ ((lane >> 3) & 6)) * 16);
        u32 vo[5];
#pragma unroll
        for (int i = 0; i < 5; ++i) {
            const u32 ch = 64 * i + lane, pr = (ch * 205) >> 12, j = ch - pr * 20;
            const u32 k = 32 * (wr >> 1) + 8 * (wr & 1) + (pr & 7) + 2 * (pr & 8);
            vo[i] = k * 3840 + (ty ? 1280 + 160 * hg + (j >= 10 ? 1120 : 0) : 1280 + 320 * hg) + j * 16;
        }
        auto issuep = [&](auto SC, auto A, auto B) {
            constexpr int s = decltype(SC)::v;
            const u32 kb = kbof(s);
            sfor<A.v, B.v>([&](auto I) {
                if constexpr (I.v < 4) dma<SL[s % 4] + XO + I.v * 1024>(vx, rsx, kb * 128 + I.v * 10240, wr * 4096);
                else dma<SL[s % 4] + (I.v - 4) * 1024>(vo[I.v - 4], rsw, kb * 245760, wr * WREG);
            });
        };
        auto issue = [&](auto SC) { issuep(SC, IC<0>{}, IC<9>{}); };
        STAMP(30);
        issue(IC<0>{});
        STAMP(31);
        {
            const u32 o = l16 + (wr & 1) * 1024 + rot * 128;
            dma<L_GW>(o >= 1280 ? o - 1280 : o, mkrs(gw, 1280), 0, (wr & 1) * 1024);
        }
        sfor<1, UP[0]>(issue);
        STAMP(1);
        const u32 xb0 = XO + (32 * wr + l15) * 128 + ((g4 ^ (l15 & 6)) * 16), xb1 = xb0 ^ 64;
        const u32 xc0 = xb0 + 2 * SLOT, xc1 = xb1 + 2 * SLOT, ga = L_GW + g4 * 16;
        const u32 qm0 = (wr & 1) * 5120 + (1 - (wr >> 1)) * 10240, qs0 = WS_WQ + hp * 102400 + (wr & 1) * 5120;
        auto qissue = [&](auto JC) {
            constexpr int jj = decltype(JC)::v;
            const u32 so = qs0 + kbof(2 * jj + (wr >> 1)) * 10240;
            sfor<0, 4>([&](auto I) { dmac<L_QT - 20480 * (jj + 1), I.v * 1024>(l16, rws, so, qm0); });
            dmac<L_QT - 20480 * (jj + 1) + 4096>(l16, rws, so + 4096, qm0);
        };
        const u32 fqo = WS_FQ + sg * 1024 + g4 * 256 + l15 * 4;
        bf16x8 xf[10][2][2];
        f32x4 gacc[2];
        u32 pv = 0;
        bool qok = true;
        auto wojob = [&](auto ST) {
#pragma unroll
            for (u32 j = 0; j < 2; ++j) {
                const u32 p = j * 1024 + b * 4 + wr;
                const bool br = p >= 800;
                const u32 q = br ? p - 800 : p, oct = q / 10, cl = q - oct * 10;
                const u32 kx = oct >> 2, go = oct & 3, G = kx / 5, e = kx - G * 5;
                if constexpr (!ST.v) {
                    const u32 r0 = 160 * G + (e == 4 ? 20 * go + 16 : 40 * e + (br ? 20 * (go >> 1) + 4 * (go & 1) : 4 * go));
                    const u32 step = e == 4 ? 80 : br ? 8 : 20;
                    dma<L_SCR>(p < 1600 ? (((lane >> 3) & 3) + step * hf) * 1280 + (lane & 7) * 16 : OOB, mkrs(br ? woa : wob, 640 * 1280),
                               p < 1600 ? r0 * 1280 + cl * 128 : 0, wr * 2048 + j * 1024);
                } else {
                    const u32 n = 64 * cl + lane, h = (n * 205) >> 12, d = n - 20 * h;
                    const u32 jd = p < 1600 ? WS_WO + ((h >> 2) * 2 + br) * 102400 + (kx * 5 + (d >> 2)) * 1024 + (go * 16 + 4 * (h & 3) + (d & 3)) * 16 : OOB;
                    const u32 js = L_SCR + wr * 2048 + j * 1024 + (l15 >> 2) * 128 + (4 * g4 + (lane & 3)) * 8;
                    st128(__builtin_bit_cast(u32x4, ldtr2(js, js + 512)), jd, rws, 0);
                }
            }
        };
        constexpr int XA[10] = {0, 2, 0, 0, 2, 0, 1, 0, 0, 0}, XB[10] = {0, 0, 0, PFQ == 3, PFQ == 4, PFQ == 5, 0, 0, 0, 0};
        sfor<0, 10>([&](auto SC) {
            constexpr int s = decltype(SC)::v;
            wait_lgkm<0>();
            constexpr int ex = s < 2 ? 0 : s < 4 ? XA[1] : XB[s - 3] + XA[s - 2] + XB[s - 2] + XA[s - 1] + XB[s - 1];
            if constexpr (s < 8) wait_vm<9 * (UP[s] - s - 1) + ex - (s == FSB && s < 7 ? XA[4] : 0)>();
            if constexpr (s == 9) { if (qok) wait_vm<15>(); else wait_vm<0>(); }
            bar();
            if constexpr (s == 0) __builtin_amdgcn_s_setprio(HPRIO);
            if constexpr (s == 0) SPINTO(2);
            if constexpr (s == 4) SPINTO(6);
            STAMP(2 + s);
            if constexpr (s == 0 || s == 2 || s == 4 || s == 7) STAMPC(s == 0 ? 51 : s == 2 ? 52 : s == 4 ? 53 : 54);
            constexpr bool up = SL[s % 4] >= 2 * SLOT, any = UP[s + 1] > UP[s];
            constexpr u32 xo = SL[s % 4] - (up ? 2 * SLOT : 0);
            const u32 a0 = up ? xc0 : xb0, a1 = up ? xc1 : xb1;
            if constexpr (s == 1) wojob(IC<0>{});
            if constexpr (s == 4) wojob(IC<1>{});
            if constexpr (s == 5) tag = rfl(lds32(L_TAG));
            if constexpr (s == 6) pv = ldflag(fqo, rws);
            if constexpr (s == 7) { qok = wait_eq<9>(pv, tag); if (qok) qissue(IC<0>{}); }
            if constexpr (s == 8) { if (qok) qissue(IC<1>{}); }
            if constexpr (s == 9) { if (qok) qissue(IC<3>{}); }
            if constexpr (any) issuep(IC<UP[s]>{}, IC<0>{}, IC<HA>{});
            xf[s][0][0] = asbf8(lds128(a0 + xo)); xf[s][0][1] = asbf8(lds128(a0 + xo + 2048));
            xf[s][1][0] = asbf8(lds128(a1 + xo)); xf[s][1][1] = asbf8(lds128(a1 + xo + 2048));
            const bf16x8 w0 = asbf8(lds128(ga + s * 128)), w1 = asbf8(lds128(ga + s * 128 + 64));
            if constexpr (any) issuep(IC<UP[s]>{}, IC<HA>{}, IC<HB>{});
            mfma16a<s == 0>(gacc[0], w0, xf[s][0][0]); mfma16a<s == 0>(gacc[1], w0, xf[s][0][1]);
            mfma16a(gacc[0], w1, xf[s][1][0]); mfma16a(gacc[1], w1, xf[s][1][1]);
            if constexpr (any) { issuep(IC<UP[s]>{}, IC<HB>{}, IC<9>{}); sfor<UP[s] + 1, UP[s + 1]>(issue); }
            if constexpr (s == PFQ) pref(lane < 25 ? lane * 128 : OOB, rws, WS_WQ + sg * 409600 + (idx * 4 + wr) * 3200);
            if constexpr (s == 8) { if (qok) qissue(IC<2>{}); }
            if constexpr (s == 9) { if (qok) qissue(IC<4>{}); }
        });
        __builtin_amdgcn_s_setprio(QPRIO);
        if (!qok) { spin(fqo, rws, tag); sfor<0, 5>(qissue); }
        asm volatile("s_nop 7\n\ts_nop 3" : "+v"(gacc[0]), "+v"(gacc[1]));
        wait_vm<20 + 2 * PFO>();
        ctl_set(me, 1);
        const u32 lhi = opaque_v(l16 + L_QT - 51200);
        ctl_wait(L_HQ, 1);
        SPINTO(12);
        STAMP(12);
        STAMPC(55);
        constexpr int PD = 4;
        bf16x8 wf[PD + 1];
        f32x4 qa[2][5];
        u32x4 snap, fs;
        u32 vw;
        auto wadr = [&](int n) {
            const int j = n / 10;
            return (j < 5 ? lhi + 40960 - 10240 * j : l16 + L_QT - 10240 * (j + 1)) + (n % 10) * 1024;
        };
        sfor<0, PD>([&](auto NC) { wf[NC.v] = asbf8(lds128(wadr(NC.v))); });
        sfor<0, 100>([&](auto NC) {
            constexpr int n = decltype(NC)::v, m = n + PD;
            constexpr int st = n / 10, ks = (n % 10) / 5, a = n % 5;
            const bf16x8 w = wf[n % (PD + 1)];
            if constexpr (m < 100 && m % 20 == 0) { if (!ctl_ge(snap, m / 20 + 1)) ctl_wait(L_HQ, m / 20 + 1); cfence(); }
            sched0();
            mfma16a<(n < 5)>(qa[0][a], w, xf[st][ks][0]);
            sched0();
            if constexpr (m < 100) wf[m % (PD + 1)] = asbf8(lds128(wadr(m)));
            sched0();
            mfma16a<(n < 5)>(qa[1][a], w, xf[st][ks][1]);
            sched0();
            if constexpr (n < 80 && n % 20 == 8) {
                wait_vm<15 + 2 * PFO - 5 * (n / 20)>();
                ctl_set(me, n / 20 + 2);
            }
            if constexpr (n < 80 && n % 20 == 12) snap = ctl_get(L_HQ);
            if constexpr (n == 50 && HWN < 200) fs = ldflag4(fso, rws);
            if constexpr (n == 69 && HWN < 200) { wok = wait_eq4<0>(fs, tag); vw = wok ? l16 : OOB; }
            if constexpr (n >= HWN && (n - HWN) % HWS == 0 && (n - HWN) / HWS < 25) wld(IC<(n - HWN) / HWS>{}, vw);
            sched0();
        });
        constexpr int HW0 = HWN >= 100 ? 0 : (100 - HWN + HWS - 1) / HWS;
        if constexpr (HWN < 200) sfor<HW0, 25>([&](auto I) { wld(I, vw); });
        else sfor<0, 25>([&](auto I) { undef(W[I.v]); });
        mfma_pad();
        STAMP(13);
        STAMPC(56);
        g0 = __builtin_amdgcn_rcpf(1.f + __builtin_amdgcn_exp2f(-gacc[0][0] * LOG2E));
        g1 = __builtin_amdgcn_rcpf(1.f + __builtin_amdgcn_exp2f(-gacc[1][0] * LOG2E));
        gt = (lane & 16) ? g1 : g0;
        {
            auto cvt = [&](int a, u32x4& o) {
                auto s0 = __builtin_amdgcn_permlane16_swap(pk2(qa[0][a][0] * QSCALE, qa[0][a][1] * QSCALE), pk2(qa[1][a][0] * QSCALE, qa[1][a][1] * QSCALE), false, false);
                auto s1 = __builtin_amdgcn_permlane16_swap(pk2(qa[0][a][2] * QSCALE, qa[0][a][3] * QSCALE), pk2(qa[1][a][2] * QSCALE, qa[1][a][3] * QSCALE), false, false);
                o = u32x4{s0[0], s1[0], s0[1], s1[1]};
            };
            u32x4 r, q1;
            cvt(4, q1); cvt(2, r);
            sts128(L_XQ + wr * 1536 + l16, q1);
            sts64(L_XQ + wr * 1536 + 1024 + lane * 8, u32x2{hf ? f2u(gt) : r[2], r[3]});
            ctl_set(me, 6);
            cvt(3, qf);
            qr = hf ? u32x2{0, 0} : u32x2{r[0], r[1]};
        }
#pragma unroll
        for (int t = 0; t < 2; ++t) {
            P[t] = u32x4{phi2(qa[t][0][0], qa[t][0][1]), phi2(qa[t][0][2], qa[t][0][3]), phi2(qa[t][1][0], qa[t][1][1]), phi2(qa[t][1][2], qa[t][1][3])};
            const u32 u = phi2(qa[t][2][0], qa[t][2][1]), v = phi2(qa[t][2][2], qa[t][2][3]);
            P[2][2 * t] = __builtin_amdgcn_permlane32_swap(u, u, false, false)[1];
            P[2][2 * t + 1] = __builtin_amdgcn_permlane32_swap(v, v, false, false)[1];
        }
        asm volatile("" : "+v"(P[0]), "+v"(P[1]), "+v"(P[2]), "+v"(qf), "+v"(qr));
        asetup(0);
        if constexpr (HWN < 200) wlate();
        STAMP(14);
        __builtin_amdgcn_s_setprio(0);
        ctl_wait(L_MC, 3);
        SPINTO(16);
        STAMP(16);
        STAMPC(57);
    } else {
        const u32 wm = wr >> 1, wn = wr & 1;
        const u32 ev = ldflag(0, rws);
        const u32 pw0 = (xcd & 1) * 128 + idx * 4 + wr;
        u32 qd[2];
#pragma unroll
        for (u32 j = 0; j < 2; ++j) {
            const u32 p = pw0 + 256 * j, isb = p >= 200, q = isb ? p - 200 : p, rg = (q * 205) >> 10;
            const u32 so = p < 400 ? rg * 61440 + sg * 320 + (q - rg * 5) * 64 : 0;
            dma<L_SCR>(p < 400 ? (lane >> 2) * 3840 + (lane & 3) * 16 : OOB, mkrs(isb ? wb : wa, 640 * 3840), so, wr * 2048 + j * 1024);
            const u32 n = (q - rg * 5) * 32 + l31, h = (n * 205) >> 12, d = n - 20 * h, o8 = 2 * rg + hf;
            const u32 t = d < 16 ? (h & 1) * 1024 + (isb ? 0 : 3072) + d * 16 : 2048 - 256 + (8 * isb + 4 * (h & 1) + d) * 16;
            qd[j] = p < 400 ? WS_WQ + (4 * sg + (h >> 1)) * 102400 + (o8 >> 2) * 5120 + (o8 & 3) * 256 + t : OOB;
        }
        const u32 krec = WS_KVP + ((2 * hg + wn) * 16 + rb) * 2048 + wm * 1024;
        if (lane < 2) sts32(L_HQ + lane * 16 + wr * 4, 0);
        u32 wb_[2][3], xb_[2][2];
        {
            const u32 q3 = lane & 3, wrow = (g4 & 1) * WREG + (8 * (g4 >> 1) + (l15 >> 2)) * 320;
            wb_[0][0] = wrow + (ty ? 80 : 160) * wn + q3 * 8;
            wb_[0][1] = wb_[0][0] + (ty ? 160 : 80);
            wb_[0][2] = wrow + 32 + (ty ? 80 * wn + ((q3 & 2) ? 0 : 160) + 40 * (q3 & 1) : 160 * wn + 40 * q3);
            xb_[0][0] = XO + (64 * wm + 16 * (l15 >> 3) + (l15 & 7)) * 128 + ((g4 ^ (l15 & 6)) * 16);
            xb_[0][1] = xb_[0][0] ^ 64;
#pragma unroll
            for (int i = 0; i < 3; ++i) wb_[1][i] = opaque_v(wb_[0][i] + 2 * SLOT);
#pragma unroll
            for (int i = 0; i < 2; ++i) xb_[1][i] = opaque_v(xb_[0][i] + 2 * SLOT);
        }
        const u32 qsrc = L_SCR + wr * 2048 + (8 * hf + (l15 >> 2)) * 64 + (g4 & 1) * 32 + (lane & 3) * 8;
        auto wad = [&](int s, int m) { return wb_[SL[s % 4] >= 2 * SLOT][(m % 5) >> 1] + SL[s % 4] % (2 * SLOT) + (m / 5) * (2 * WREG) + ((m % 5) & 1) * 40; };
        auto xad = [&](int s, int ks, int t) { return xb_[SL[s % 4] >= 2 * SLOT][ks] + SL[s % 4] % (2 * SLOT) + (t >> 1) * 4096 + (t & 1) * 1024; };
        f32x4 acc[4][5];
        STAMP(30);
        tag = wait_rfl<0>(ev) + TAGC;
        STAMP(31);
        st128(__builtin_bit_cast(u32x4, ldtr2(qsrc, qsrc + 256)), qd[0], rws, 0);
        st128(__builtin_bit_cast(u32x4, ldtr2(qsrc + 1024, qsrc + 1280)), qd[1], rws, 0);
        if (wr == 0 && lane == 0) sts32(L_TAG, tag);
        {
            bf16x8 wf[5], xa[2][4];
            STAMP(1);
            wait_lgkm<0>();
            bar();
            SPINTO(2);
            STAMP(2);
            sfor<0, 4>([&](auto I) { xa[0][I.v] = asbf8(lds128(xad(0, 0, I.v))); });
            wf[0] = ldtr2(wad(0, 0), wad(0, 0) + 1280);
            wf[1] = ldtr2(wad(0, 1), wad(0, 1) + 1280);
            sfor<0, 100>([&](auto NC) {
                constexpr int s = decltype(NC)::v / 10, n = decltype(NC)::v % 10, ks = n / 5, c = n % 5;
                constexpr int s2 = n >= 8 ? s + 1 : s, m = (n + 2) % 10;
                constexpr bool more = s2 < 10, first = s == 0 && ks == 0, rdw = more && (!(XPM & 1) || s == 0), rdx = !(XPM & 2) || s == 0;
                bf16x4 lo, hi;
                bf16x8 t0, t1;
                auto mm = [&](auto I) { sched0(); if constexpr (!(XPM & 4) || s == 0) mfma16a<first>(acc[I.v][c], xa[ks][I.v], wf[c]); else asm volatile("" :: "v"(xa[ks][I.v]), "v"(wf[c])); sched0(); };
                mm(IC<0>{});
                if constexpr (n == 8 && more) {
                    wait_lgkm<0>();
                    if constexpr (s == FQS) wait_vm<0>();
                    bar();
                    sched0();
                }
                if constexpr (rdw) lo = ldtr(wad(s2, m));
                mm(IC<1>{});
                if constexpr (rdw) hi = ldtr(wad(s2, m) + 1280);
                mm(IC<2>{});
                if constexpr (n >= 8 && more && rdx) t0 = asbf8(lds128(xad(s2, 0, 2 * (n - 8))));
                if constexpr (n < 4 && rdx) xa[1][n] = asbf8(lds128(xad(s, 1, n)));
                mm(IC<3>{});
                if constexpr (n >= 8 && more && rdx) { t1 = asbf8(lds128(xad(s2, 0, 2 * (n - 8) + 1))); xa[0][2 * (n - 8)] = t0; xa[0][2 * (n - 8) + 1] = t1; }
                if constexpr (s == FQS && n == 8) { if (wr == 0) stflag(tag, WS_FQ + sg * 1024 + (idx >> 3) * 256 + ((idx & 7) * 2 + (xcd & 1)) * 4, rws); }
                if constexpr (s == FSB - 1 && n == 8) { if (wr == 1) stflag(tag, WS_FS + (b >> 4) * 256 + (b & 15) * 4, rws); }
                if constexpr (rdw) wf[m % 5] = cat8(lo, hi);
                sched0();
            });
        }
        mfma_pad();
        __builtin_amdgcn_s_setprio(PRIO);
        SPINTO(12);
        STAMP(12);
        auto pk8 = [&](int u, int c) {
            return u32x4{pk2(acc[2 * u][c][0], acc[2 * u][c][1]), pk2(acc[2 * u][c][2], acc[2 * u][c][3]),
                         pk2(acc[2 * u + 1][c][0], acc[2 * u + 1][c][1]), pk2(acc[2 * u + 1][c][2], acc[2 * u + 1][c][3])};
        };
        auto ph8 = [&](int u, int c) {
            return u32x4{phi2(acc[2 * u][c][0], acc[2 * u][c][1]), phi2(acc[2 * u][c][2], acc[2 * u][c][3]),
                         phi2(acc[2 * u + 1][c][0], acc[2 * u + 1][c][1]), phi2(acc[2 * u + 1][c][2], acc[2 * u + 1][c][3])};
        };
        auto markers = [&]() {
            const u32x4 mk4 = {~0u, ~0u, ~0u, ~0u};
            sfor<0, 4>([&](auto I) { st128(mk4, l16, rws, oprod + c4 * 1024 + I.v * 20480); });
            st128(mk4, (g4 >> 1) * 40960 + (g4 & 1) * 20480 + l15 * 16, rws, oprod + 4096 + c4 * 256);
        };
        if (!ty) {
            const u32x4 fs = ldflag4(fso, rws);
            const u32 so = WS_T + ((4 * hg + 2 * wn) * 64 + 4 * rb + 2 * wm) * 2560;
            const u32 vm = g4 * 256 + ((l15 ^ (4 * g4)) * 16);
            const u32 vr = 2048 + g4 * 128 + ((((l15 >> 2) ^ (g4 >> 1)) & 1) * 64) + (l15 & 3) * 16 + (l15 >> 3) * 163840;
            sfor<0, 10>([&](auto I) {
                constexpr int c = decltype(I)::v >> 1, u = decltype(I)::v & 1;
                st128<(c < 4 ? (c & 1) * 1024 : 0) + u * 2560>(pk8(u, c), c < 4 ? vm : vr, rws, so + (c == 2 || c == 3 ? 163840 : 0));
            });
            markers();
            wok = wait_eq4<5>(fs, tag);
            stflag(tag, WS_FT + ((hg * 2 + wn) * 32 + 2 * rb + wm) * 128, rws);
            const u32 vw = wok ? l16 : OOB;
            sfor<0, 25>([&](auto I) { wld(I, vw); });
        } else {
            const u32x4 fs = ldflag4(fso, rws);
            markers();
            if constexpr (PFW) {
                const u32 po = WS_WO + (((idx >> 2) * 2 + (idx & 1)) * 4 + wr) * 25600;
                pref(lane * 128, rws, po); pref(lane * 128, rws, po + 8192); pref(lane * 128, rws, po + 16384); pref(lane < 8 ? lane * 128 : OOB, rws, po + 24576);
            }
            f32x4 Q0 = {0, 0, 0, 0}, Q1 = Q0, X0 = Q0, X1 = Q0, Y0 = Q0, Y1 = Q0, U = Q0;
#pragma unroll
            for (int u = 0; u < 2; ++u) {
                const bf16x8 v0 = asbf8(pk8(u, 2)), v1 = asbf8(pk8(u, 3));
                const u32x4 rr = pk8(u, 4);
                const bf16x8 rbv = asbf8(l15 < 8 ? rr : u32x4{ONES, ONES, ONES, ONES});
                const bf16x8 k0 = asbf8(ph8(u, 0));
                Q0 = mfma16(k0, v0, Q0); X0 = mfma16(k0, rbv, X0);
                const bf16x8 k1 = asbf8(ph8(u, 1));
                Q1 = mfma16(k1, v1, Q1); X1 = mfma16(k1, rbv, X1);
                const bf16x8 ra = asbf8(ph8(u, 4));
                Y0 = mfma16(ra, v0, Y0); Y1 = mfma16(ra, v1, Y1); U = mfma16(ra, rbv, U);
                if (u == WU) {
                    sched0();
                    wok = wait_eq4<5 + 4 * PFW>(fs, tag);
                    const u32 vw = wok ? l16 : OOB;
                    sfor<0, 25>([&](auto I) { wld(I, vw); });
                    sched0();
                }
            }
            f32x4 X = (l15 & 4) ? X1 : X0, Z;
#pragma unroll
            for (int r = 0; r < 4; ++r) Z[r] = u2f(__builtin_amdgcn_permlane32_swap(f2u((g4 & 1) ? Y1[r] : Y0[r]), f2u(U[r]), false, false)[1]);
            sts128(L_SCR + wr * 2048 + l16, __builtin_bit_cast(u32x4, wm ? Q0 : X));
            sts128(L_SCR + wr * 2048 + 1024 + l16, __builtin_bit_cast(u32x4, wm ? Q1 : Z));
            ctl_set(me, 1);
            ctl_wait1(L_MC + (wr ^ 2) * 4, 1);
            const f32x4 a = (wm ? X : Q0) + __builtin_bit_cast(f32x4, lds128(L_SCR + (wr ^ 2) * 2048 + l16));
            const f32x4 c = (wm ? Z : Q1) + __builtin_bit_cast(f32x4, lds128(L_SCR + (wr ^ 2) * 2048 + 1024 + l16));
            st128(u32x4{pk2(a[0], a[1]), pk2(a[2], a[3]), pk2(c[0], c[1]), pk2(c[2], c[3])}, l16, rws, krec);
        }
        STAMP(13);
        if constexpr (CPF) {
            u64 pc; u32 len; int rel;
            asm volatile("s_getpc_b64 %0\n.Lcode_p2:\n\ts_mov_b32 %2, .Lcode_at-.Lcode_p2\n\ts_mov_b32 %1, .Lcode_hi-.Lcode_at" : "=s"(pc), "=s"(len), "=s"(rel));
            pc += (long long)rel;
            u32 o = wr * 8192 + lane * 128;
            o = o < len ? o : 0;
            asm volatile("s_mov_b32 m0, %2\n\ts_nop 0\n\tglobal_load_lds_dword %0, %1" :: "v"(o), "s"(pc), "i"(L_DUMP) : "memory");
        }
        {
            const int g0 = 2 * (int)rb - 4 + 3 * (int)wn;
            int gl = g0 + (int)(lane < 2 ? lane : 2);
            gl = gl < 0 ? 0 : gl;
            spin(WS_FT + ((wm * 8 + (hp >> 1)) * 32 + gl) * 128, rws, tag);
            ctl_set(me, 2);
            ctl_wait8(2);
            if (wr == 0) stflag(tag, WS_F2 + rb * 256 + hp * 4, rws);
            STAMP(14);
#pragma unroll
            for (int i = 0; i < 3; ++i) {
                const u32 mb = wm ? L_V + (3 * wn + i) * 5136 : L_K + (3 * wn + i) * 5120;
                if (g0 + i >= 0) {
                    const u32 so = WS_T + ((wm * 16 + hp) * 32 + g0 + i) * 5120;
                    dmac<0, 0>(l16, rws, so, mb); dmac<0, 1024>(l16, rws, so, mb); dmac<0, 2048>(l16, rws, so, mb); dmac<0, 3072>(l16, rws, so, mb);
                    dmac<4096, 0>(l16, rws, so + 4096, mb);
                } else {
#pragma unroll
                    for (int k = 0; k < 5; ++k) sts128(mb + k * 1024 + l16, u32x4{0, 0, 0, 0});
                }
            }
            if (wm && lane < 12) sts32(L_V + (3 * wn + (lane >> 2)) * 5136 + 5120 + (lane & 3) * 4, g0 + (int)(lane >> 2) >= 0 ? ONES : 0);
            asetup(1);
            asm volatile("" : "=v"(P[0]), "=v"(P[1]), "=v"(P[2]));
            if constexpr (HWN >= 200) wlate();
            wait_vm<0>();
            ctl_set(me, 3);
            if constexpr (HWN < 200) wlate();
            STAMP(15);
        }
        ctl_wait(L_HQ, 6);
        qf = lds128(L_XQ + wr * 1536 + l16);
        const u32x2 r = lds64(L_XQ + wr * 1536 + 1024 + lane * 8);
        qr = hf ? u32x2{0, 0} : r;
        gt = u2f(lds32(L_XQ + wr * 1536 + 1280 + l31 * 8));
        ctl_wait(L_MC, 3);
        if (b == 0 && wr == 0) stflag(tag, WS_E, rws);
        __builtin_amdgcn_s_setprio(0);
        SPINTO(16);
        STAMP(16);
        asm volatile(".Lcode_hm:");
    }
    asm volatile(".Lcode_at:");
    auto recs = [&]() {
        const u32 vo = hf * 2048 + l31 * 16, so = WS_KVP + hp * 32768 + wr * 512;
        sfor<0, 8>([&](auto I) { dmac<L_RC + I.v * 1024>(vo, rws, so + I.v * 4096, wr * 8192); });
    };
    u32 pv;
    bool rok = false;
    {
        const u32 hh = wid < 4;
        f32x16 zero;
#pragma unroll
        for (int i = 0; i < 16; ++i) zero[i] = 0.f;
        const bf16x8 q16 = asbf8(qf);
        const bf16x4 q4 = __builtin_bit_cast(bf16x4, qr);
        auto qk = [&](u32 o) { return mfma32k8(ldtr(kr + o), q4, mfma32(ldtr2(ka + o, kc + o), q16, zero)); };
        f32x16 o = zero;
        sched0();
        bf16x8 j16 = ldtr2(ka + 2560, kc + 2560), v2, v3;
        bf16x4 j4 = ldtr(kr + 2560);
        bf16x8 k16 = ldtr2(ka + 5120, kc + 5120);
        bf16x4 k4 = ldtr(kr + 5120);
        bf16x8 v0 = asbf8(lds128(va[1][0])), v1 = asbf8(lds128(va[1][1]));
        sched0();
        f32x16 S = mfma32k8(j4, q4, mfma32(j16, q16, zero)), Sn, S8;
        sfor<1, 9>([&](auto I) {
            constexpr int it = decltype(I)::v, i = it & 7, nx = (it + 1) & 7;
            auto wl = [&](auto J) {
                if constexpr (HWN >= 200) { if (wid >= 4) rl128<(J.v & 3) * 1024>(W[J.v], l16, rws, WS_WO + c80 * 204800 + wid * 25600 + (J.v >> 2) * 4096); }
            };
            float e[16];
            u32 p[8];
            auto ex = [&](auto A, auto B) {
                sfor<A.v, B.v>([&](auto R) {
                    if constexpr (i == 0) e[R.v] = __builtin_amdgcn_exp2f(__builtin_amdgcn_inverse_ballot_w64(lem((R.v & 3) + 8 * (R.v >> 2))) ? S8[R.v] : S[R.v]);
                    else e[R.v] = __builtin_amdgcn_exp2f(S[R.v]);
                });
                sched0();
            };
            auto cv = [&](int a) {
#pragma unroll
                for (int j = a; j < a + 4; ++j) p[j] = pk2(e[2 * j], e[2 * j + 1]);
                sched0();
            };
            sched0();
            if constexpr ((ASM >> it) & 1) STAMP(35 + it);
            if constexpr (it == HK1) pv = ldflag(f2o, rws);
            if constexpr (it == HK2) {
                if (wid < 4) { rok = wait_eq<0>(pv, tag); if (rok) recs(); }
                else rok = wait_eq<(HWN >= 200 ? 3 * (HK2 - HK1) : 0)>(pv, tag);
            }
            if constexpr (it == 1) wl(IC<0>{});
            sched0();
            if constexpr (i != 0) Sn = mfma32(k16, q16, zero);
            sched0();
            ex(IC<0>{}, IC<8>{});
            if constexpr (i != 0) Sn = mfma32k8(k4, q4, Sn);
            sched0();
            if constexpr (it < 7) { k16 = ldtr2(ka + ((it + 2) & 7) * 2560, kc + ((it + 2) & 7) * 2560); k4 = ldtr(kr + ((it + 2) & 7) * 2560); }
            if constexpr (it == 5) { j16 = ldtr2(ka + 8 * 2560, kc + 8 * 2560); j4 = ldtr(kr + 8 * 2560); }
            if constexpr (it == 7) { v2 = asbf8(lds128(va[0][0] + 4 * 5136)); v3 = asbf8(lds128(va[0][1] + 4 * 5136)); }
            wl(IC<3 * it - 2>{});
            sched0();
            cv(0);
            if constexpr (i == 0) { o = mfma32(v2, asbf8(u32x4{p[0] & mk[0], p[1] & mk[1], p[2] & mk[2], p[3] & mk[3]}), o); sched0(); }
            ex(IC<8>{}, IC<12>{});
            if constexpr (i == 0) o = mfma32(v0, asbf8(u32x4{p[0] & ~mk[0], p[1] & ~mk[1], p[2] & ~mk[2], p[3] & ~mk[3]}), o);
            else o = mfma32(v0, asbf8(u32x4{p[0], p[1], p[2], p[3]}), o);
            sched0();
            wl(IC<3 * it - 1>{});
            if constexpr (it == 6) S8 = mfma32(j16, q16, zero);
            sched0();
            ex(IC<12>{}, IC<16>{});
            cv(4);
            if constexpr (i == 0) {
                o = mfma32(v3, asbf8(u32x4{p[4] & mk[4], p[5] & mk[5], p[6] & mk[6], p[7] & mk[7]}), o);
                o = mfma32(v1, asbf8(u32x4{p[4] & ~mk[4], p[5] & ~mk[5], p[6] & ~mk[6], p[7] & ~mk[7]}), o);
            } else o = mfma32(v1, asbf8(u32x4{p[4], p[5], p[6], p[7]}), o);
            sched0();
            if constexpr (it == 6) S8 = mfma32k8(j4, q4, S8);
            if constexpr (i != 0) { v0 = asbf8(lds128(va[nx & 1][0] + (nx >> 1) * 5136)); v1 = asbf8(lds128(va[nx & 1][1] + (nx >> 1) * 5136)); }
            wl(IC<3 * it>{});
            sched0();
            if constexpr (i != 0) S = Sn;
        });
        if constexpr ((ASM >> 9) & 1) STAMP(44);
        const int qq = (int)l31 - 4 * (int)hf;
        float am = 0.f;
#pragma unroll
        for (int r = 0; r < 12; r += 2) am = fmaxf(fmaxf(am, fabsf(o[r])), fabsf(o[r + 1]));
        const bool fine = o[12] > 0x1p-30f && o[12] < 0x1p30f && am < 0x1p120f;
        if (__builtin_amdgcn_ballot_w64(!fine) != 0ull) {
            const int i0 = rb >= 2 ? 0 : 8 - 4 * (int)rb - (int)wr;
            auto sc = [&](int i) {
                f32x16 s = qk(i * 2560);
#pragma unroll
                for (int r = 0; r < 16; ++r) {
                    const bool le = (r & 3) + 8 * (r >> 2) <= qq;
                    const bool ok = i == 0 ? !le : (i == 8 ? le : true);
                    s[r] = ok ? s[r] : -__builtin_inff();
                }
                return s;
            };
            float m = -__builtin_inff();
#pragma nounroll
            for (int i = i0; i < 9; ++i) {
                const f32x16 s = sc(i);
#pragma unroll
                for (int r = 0; r < 16; ++r) m = fmaxf(m, s[r]);
            }
            auto sw = __builtin_amdgcn_permlane32_swap(f2u(m), f2u(m), false, false);
            m = fmaxf(u2f(sw[0]), u2f(sw[1]));
            o = zero;
#pragma nounroll
            for (int i = i0; i < 9; ++i) {
                const f32x16 s = sc(i);
                u32 p[8];
#pragma unroll
                for (int j = 0; j < 8; ++j) p[j] = pk2(__builtin_amdgcn_exp2f(s[2 * j] - m), __builtin_amdgcn_exp2f(s[2 * j + 1] - m));
                const u32 vo_ = (i >> 1) * 5136;
                o = mfma32(asbf8(lds128(((i & 1) ? va[1][0] : va[0][0]) + vo_)), asbf8(u32x4{p[0], p[1], p[2], p[3]}), o);
                o = mfma32(asbf8(lds128(((i & 1) ? va[1][1] : va[0][1]) + vo_)), asbf8(u32x4{p[4], p[5], p[6], p[7]}), o);
            }
        }
        const float rs = __builtin_amdgcn_rcpf(o[12]) * gt;
        const u32 ob = oprod + 20480 + (l31 >> 4) * 40960;
        u32x4 v = {pk2m(o[0] * rs, o[1] * rs), pk2m(o[2] * rs, o[3] * rs), pk2m(o[4] * rs, o[5] * rs), pk2m(o[6] * rs, o[7] * rs)};
        __builtin_amdgcn_raw_buffer_store_b128(v, wsr, ob + c4 * 1024 + (2 * hh + hf) * 256 + l15 * 16, 0, 16);
        u32x2 vt = {pk2m(o[8] * rs, o[9] * rs), pk2m(o[10] * rs, o[11] * rs)};
        __builtin_amdgcn_raw_buffer_store_b64(vt, wsr, hf ? OOB : ob + 4096 + (2 * c4 + hh) * 128 + l15 * 8, 0, 16);
        cfence();
    }
    STAMP(17);
    STAMPC(58);
    if (!rok) spin(f2o, rws, tag);
    if (wid < 4) {
        if (rok) wait_vm<2>(); else { recs(); wait_vm<0>(); }
        float s[8];
#pragma unroll
        for (int i = 0; i < 8; ++i) {
            const u32x4 v = lds128(L_RC + wr * 8192 + i * 1024 + l16);
#pragma unroll
            for (int k = 0; k < 4; ++k) { s[2 * k] = i ? s[2 * k] + blo(v[k]) : blo(v[k]); s[2 * k + 1] = i ? s[2 * k + 1] + bhi(v[k]) : bhi(v[k]); }
        }
        float t[4];
#pragma unroll
        for (int k = 0; k < 4; ++k) {
            auto sw = __builtin_amdgcn_permlane32_swap(f2u(s[k]), f2u(s[4 + k]), false, false);
            t[k] = u2f(sw[0]) + u2f(sw[1]);
        }
        sts64(L_REC + wr * 512 + l31 * 16 + hf * 8, u32x2{pk2(t[0], t[1]), pk2(t[2], t[3])});
    } else {
        ctl_wait(L_MC, 10);
        STAMP(23);
        const u32x4 m = lds128(L_REC + l16), n = lds128(L_REC + 1024 + l16);
        const bool m1 = l15 & 4;
        const bf16x8 a0 = asbf8(u32x4{m[0], m[1], g4 == 0 ? n[2] : 0, g4 == 0 ? n[3] : 0});
        const bf16x8 a1 = asbf8(u32x4{m[2], m[3], g4 == 1 ? n[2] : 0, g4 == 1 ? n[3] : 0});
        const bf16x8 x0 = asbf8(u32x4{m1 ? 0 : n[0], m1 ? 0 : n[1], (g4 == 2 && !m1) ? n[2] : 0, (g4 == 2 && !m1) ? n[3] : 0});
        const bf16x8 x1 = asbf8(u32x4{m1 ? n[0] : 0, m1 ? n[1] : 0, (g4 == 3 && m1) ? n[2] : 0, (g4 == 3 && m1) ? n[3] : 0});
#pragma unroll
        for (int t2 = 0; t2 < 2; ++t2) {
            const bf16x8 b0 = asbf8(u32x4{P[t2][0], P[t2][1], P[2][2 * t2], P[2][2 * t2 + 1]});
            const bf16x8 b1 = asbf8(u32x4{P[t2][2], P[t2][3], P[2][2 * t2], P[2][2 * t2 + 1]});
            const f32x4 z = {0, 0, 0, 0};
            const f32x4 xx = mfma16(x1, b1, mfma16(x0, b0, z));
            const f32x4 n0 = mfma16(a0, b0, z), n1 = mfma16(a1, b1, z);
            const u32 rc = f2u(__builtin_amdgcn_rcpf(xx[0] + 1e-6f) * (1.f - (t2 ? g1 : g0)));
            const u32 rs = __builtin_amdgcn_permlane32_swap(rc, rc, false, false)[1];
            auto sw = __builtin_amdgcn_permlane16_swap(rs, rs, false, false);
            const float r0 = u2f(sw[0]), r1 = u2f(sw[1]), rr = u2f(rs);
            const u32 so = oprod + t2 * 40960;
            st128(u32x4{pk2m(n0[0] * r0, n0[1] * r0), pk2m(n0[2] * r0, n0[3] * r0), pk2m(n1[0] * r1, n1[1] * r1), pk2m(n1[2] * r1, n1[3] * r1)}, l16, rws, so + c4 * 1024);
            __builtin_amdgcn_raw_buffer_store_b64(u32x2{pk2m(xx[0] * rr, xx[1] * rr), pk2m(xx[2] * rr, xx[3] * rr)}, wsr, hf ? OOB : lane * 8, so + 4096 + c4 * 256, 16);
        }
        cfence();
    }
    ctl_set(me, 10);
    STAMP(24);
    {
        const u32 vr = lane * 8;
        auto ot = [&](u32 x) { return WS_O + (r64 * 4 + (wr ^ x)) * 40960 + wid * 5120; };
        auto oload = [&](OF& o, u32 s0, u32 s1) {
#pragma unroll
            for (int e = 0; e < 4; ++e) {
                o.m[0][e] = __builtin_amdgcn_raw_buffer_load_b128(wsr, l16 + e * 1024, s0, 16);
                o.m[1][e] = __builtin_amdgcn_raw_buffer_load_b128(wsr, l16 + e * 1024, s1, 16);
            }
#pragma unroll
            for (int h = 0; h < 2; ++h) {
                o.r[0][h] = __builtin_amdgcn_raw_buffer_load_b64(wsr, vr + h * 512, s0 + 4096, 16);
                o.r[1][h] = __builtin_amdgcn_raw_buffer_load_b64(wsr, vr + h * 512, s1 + 4096, 16);
            }
            cfence();
        };
        auto pass = [&](OF& o, f32x4 (&ac)[2][5], u32 s0, u32 s1) {
            u32 nx = 0, bad = 31;
#pragma unroll
            for (int i = 0; i < 10; ++i) asm volatile("" : "=v"(ac[i / 5][i % 5]));
#pragma nounroll
            for (u32 it = 0; it < (1u << 17); ++it) {
                const u32 chk = bad;
                bad = 32;
                sfor<0, 4>([&](auto E) {
                    if (chk & (1u << E.v)) {
                        if (marked(mx4(mx4(0, o.m[0][E.v]), o.m[1][E.v]))) {
                            rl128<E.v * 1024>(o.m[0][E.v], l16, rws, s0); rl128<E.v * 1024>(o.m[1][E.v], l16, rws, s1);
                            bad |= 1u << E.v;
                        }
                    }
                });
                if (marked(umax(umax(umax(o.r[0][0][0], o.r[0][0][1]), umax(o.r[0][1][0], o.r[0][1][1])), umax(umax(o.r[1][0][0], o.r[1][0][1]), umax(o.r[1][1][0], o.r[1][1][1]))))) {
                    rl64<0>(o.r[0][0], vr, rws, s0 + 4096); rl64<0>(o.r[1][0], vr, rws, s1 + 4096);
                    rl64<512>(o.r[0][1], vr, rws, s0 + 4096); rl64<512>(o.r[1][1], vr, rws, s1 + 4096);
                    bad |= 16;
                }
                const u32 lim = __builtin_ctz(bad);
                sfor<0, 5>([&](auto E) {
                    constexpr int e = decltype(E)::v;
                    if (e >= nx && e < lim) {
#pragma unroll
                        for (int t = 0; t < 2; ++t) {
                            const bf16x8 f = asbf8(e < 4 ? o.m[t][e & 3] : u32x4{o.r[t][0][0], o.r[t][0][1], o.r[t][1][0], o.r[t][1][1]});
#pragma unroll
                            for (int c = 0; c < 5; ++c) mfma16b<e == 0>(ac[t][c], asbf8(W[e * 5 + c]), f);
                        }
                    }
                });
                nx = lim;
                if (lim == 5) break;
                of_wait(o);
            }
            mfma_pad();
        };
        f32x4 ac[2][5];
        OF oa, ob;
        const u32 pw = (wid >= 4 ? wr * 20480 : L_K - 5120 + wr * 15360) + l16;
        if (OLATE) ctl_wait8(10);
        SPINTO(25);
        STAMP(25);
        oload(oa, ot(2), ot(3));
        oload(ob, ot(0), ot(1));
        of_tie(oa);
        pass(oa, ac, ot(2), ot(3));
        STAMP(18);
        ctl_wait8(10);
#pragma unroll
        for (int i = 0; i < 10; ++i) sts128(pw + 10240 + i * 1024, __builtin_bit_cast(u32x4, ac[i / 5][i % 5]));
        ctl_set(me, 11);
        of_tie(ob);
        pass(ob, ac, ot(0), ot(1));
        STAMP(19);
#pragma unroll
        for (int c = 0; c < 5; ++c) sts128(pw + 5120 + c * 1024, __builtin_bit_cast(u32x4, ac[1][c]));
        if (wid >= 4) {
#pragma unroll
            for (int c = 0; c < 5; ++c) sts128(pw + c * 1024, __builtin_bit_cast(u32x4, ac[0][c]));
            ctl_set(me, 12);
        } else {
            ctl_set(me, 12);
            auto add = [&](u32 a) {
#pragma unroll
                for (int c = 0; c < 5; ++c) ac[0][c] += __builtin_bit_cast(f32x4, lds128(a + l16 + c * 1024));
            };
            ctl_wait(L_MC, 12);
#pragma unroll
            for (u32 x = 1; x < 4; ++x) add(L_K - 5120 + (wr ^ x) * 15360 + x * 5120);
            STAMP(20);
            ctl_wait(L_HQ, 11);
#pragma unroll
            for (u32 x = 2; x < 4; ++x) add((wr ^ x) * 20480 + x * 5120);
            ctl_wait(L_HQ, 12);
            STAMP(21);
#pragma unroll
            for (u32 x = 0; x < 2; ++x) add((wr ^ x) * 20480 + x * 5120);
            u32 r[10];
#pragma unroll
            for (int c = 0; c < 5; ++c) { r[2 * c] = pk2(ac[0][c][0], ac[0][c][1]); r[2 * c + 1] = pk2(ac[0][c][2], ac[0][c][3]); }
            const __amdgpu_buffer_rsrc_t ors = __builtin_amdgcn_make_buffer_rsrc(out, 0, 2048 * 1280, 0x00020000);
            const u32 vo = l15 * 1280 + g4 * 40 + (r64 * 64 + wr * 16) * 1280 + c80 * 160;
            __builtin_amdgcn_raw_buffer_store_b128(u32x4{r[0], r[1], r[2], r[3]}, ors, vo, 0, OAUX);
            __builtin_amdgcn_raw_buffer_store_b128(u32x4{r[4], r[5], r[6], r[7]}, ors, vo + 16, 0, OAUX);
            __builtin_amdgcn_raw_buffer_store_b64(u32x2{r[8], r[9]}, ors, vo + 32, 0, OAUX);
        }
    }
    SPINTO(22);
    STAMP(22);
    STAMPC(63);
    asm volatile(".Lcode_hi:");
}

extern "C" void kernel_launch(void* const* d_in, const int* in_sizes, int n_in, void* d_out, int out_size, void* d_ws,
                              size_t ws_size, hipStream_t stream) {
    (void)in_sizes; (void)n_in; (void)out_size; (void)ws_size;
    (void)hipFuncSetAttribute((const void*)fused_kernel, hipFuncAttributeMaxDynamicSharedMemorySize, LDS_BYTES);
    fused_kernel<<<256, 512, LDS_BYTES, stream>>>((const u16*)d_in[0], (const u16*)d_in[1], (const u16*)d_in[2], (const u16*)d_in[4],
                                                  (const u16*)d_in[5], (const u16*)d_in[6], (u16*)d_out, (char*)d_ws);
}
```

```cpp
#include <hip/hip_runtime.h>
#include <stdint.h>
#pragma clang diagnostic ignored "-Wint-to-pointer-cast"

#define SPINS 1
#define PRIO 3
typedef unsigned short u16;
typedef unsigned int u32;
typedef unsigned long long u64;
typedef __attribute__((ext_vector_type(8))) __bf16 bf16x8;
typedef __attribute__((ext_vector_type(4))) __bf16 bf16x4;
typedef __attribute__((ext_vector_type(2))) __bf16 bf16x2;
typedef __attribute__((ext_vector_type(2))) float f32x2;
typedef __attribute__((ext_vector_type(4))) float f32x4;
typedef __attribute__((ext_vector_type(16))) float f32x16;
typedef __attribute__((ext_vector_type(4))) u32 u32x4;
typedef __attribute__((ext_vector_type(2))) u32 u32x2;
typedef __attribute__((ext_vector_type(4))) short s16x4;
#define AS3 __attribute__((address_space(3)))
#define DEVI __device__ __forceinline__

constexpr u32 WS_E   = 0;
constexpr u32 WS_FS  = 4096;
constexpr u32 WS_F2  = 12288;
constexpr u32 WS_FQ  = 8192;
constexpr u32 WS_FT  = 16384;
constexpr u32 WS_T   = 81920;
constexpr u32 WS_KVP = WS_T + 2 * 16 * 64 * 2560;
constexpr u32 WS_O   = WS_KVP + 16 * 16 * 2048;
constexpr u32 WS_WO  = WS_O + 128 * 40960;
constexpr u32 WS_WQ  = WS_WO + 16 * 102400;
constexpr u32 WS_END = WS_WQ + 16 * 102400;
constexpr u32 OOB = 0x80000000u;

constexpr u32 WREG  = 5152;
constexpr u32 XO    = 20608;
constexpr u32 SLOT  = 36992;
__device__ constexpr u32 SL[4] = {0, 3 * SLOT, 2 * SLOT, SLOT};
constexpr u32 L_SCR = 4 * SLOT;
constexpr u32 L_GW  = L_SCR + 8192;
constexpr u32 L_DUMP = 163328;
constexpr u32 L_REC = 0;
constexpr u32 L_XQ  = 2048;
constexpr u32 L_QT  = 3 * SLOT;
constexpr u32 L_K   = 101376;
constexpr u32 L_V   = 132096;
constexpr u32 L_RC  = 8192;
constexpr u32 L_HQ  = 162944, L_MC = L_HQ + 16, L_TAG = L_HQ + 32;
constexpr u32 LDS_BYTES = 163840;
static_assert(L_GW + 2048 <= L_HQ && L_DUMP >= L_TAG + 4 && L_DUMP + 256 <= LDS_BYTES && L_V + 6 * 5136 <= L_HQ && L_XQ + 6144 <= L_QT - 102400, "lds");

constexpr float LOG2E = 1.4426950408889634f;
constexpr float QSCALE = 0.22360679774997896f * 1.4426950408889634f;
constexpr u32 TAGC = 0x7F4A7C15u;
constexpr u32 ONES = 0x3F803F80u;

DEVI u32 pk2(float a, float b) { f32x2 v = {a, b}; bf16x2 r = __builtin_convertvector(v, bf16x2); return __builtin_bit_cast(u32, r); }
DEVI u32 f2u(float f) { return __builtin_bit_cast(u32, f); }
DEVI float u2f(u32 u) { return __builtin_bit_cast(float, u); }
DEVI float blo(u32 u) { return u2f(u << 16); }
DEVI float bhi(u32 u) { return u2f(u & 0xFFFF0000u); }
DEVI u32 umin(u32 a, u32 b) { return a < b ? a : b; }
DEVI u32 umax(u32 a, u32 b) { return a > b ? a : b; }
DEVI u32 pk2m(float a, float b) { return umin(pk2(a, b), 0xFFFFFFFEu); }
DEVI bf16x8 asbf8(u32x4 v) { return __builtin_bit_cast(bf16x8, v); }
DEVI f32x4 mfma16(bf16x8 a, bf16x8 b, f32x4 c) { return __builtin_amdgcn_mfma_f32_16x16x32_bf16(a, b, c, 0, 0, 0); }
template <bool FIRST = false> DEVI void mfma16a(f32x4& c, bf16x8 a, bf16x8 b) {
    if constexpr (FIRST) asm volatile("v_mfma_f32_16x16x32_bf16 %0, %1, %2, 0" : "=&v"(c) : "v"(a), "v"(b));
    else asm volatile("v_mfma_f32_16x16x32_bf16 %0, %1, %2, %0" : "+v"(c) : "v"(a), "v"(b));
}
template <bool FIRST> DEVI void mfma16b(f32x4& c, bf16x8 a, bf16x8 b) {
    if constexpr (FIRST) asm volatile("v_mfma_f32_16x16x32_bf16 %0, %1, %2, 0" : "+v"(c) : "v"(a), "v"(b));
    else asm volatile("v_mfma_f32_16x16x32_bf16 %0, %1, %2, %0" : "+v"(c) : "v"(a), "v"(b));
}
DEVI void mfma_pad() { __builtin_amdgcn_sched_barrier(0); asm volatile("s_nop 7\n\ts_nop 3" ::: "memory"); __builtin_amdgcn_sched_barrier(0); }
DEVI f32x16 mfma32(bf16x8 a, bf16x8 b, f32x16 c) { return __builtin_amdgcn_mfma_f32_32x32x16_bf16(a, b, c, 0, 0, 0); }
DEVI f32x16 mfma32k8(bf16x4 a, bf16x4 b, f32x16 c) {
    return __builtin_amdgcn_mfma_f32_32x32x8bf16_1k(__builtin_bit_cast(s16x4, a), __builtin_bit_cast(s16x4, b), c, 0, 0, 0);
}
DEVI bf16x4 ldtr(u32 a) { return __builtin_amdgcn_ds_read_tr16_b64_v4bf16((AS3 bf16x4*)a); }
DEVI bf16x8 cat8(bf16x4 x, bf16x4 y) { return __builtin_shufflevector(x, y, 0, 1, 2, 3, 4, 5, 6, 7); }
DEVI bf16x8 ldtr2(u32 a, u32 b) { bf16x4 x = ldtr(a), y = ldtr(b); return cat8(x, y); }
DEVI u32x4 lds128(u32 a) { return *(AS3 u32x4*)a; }
DEVI u32x2 lds64(u32 a) { return *(AS3 u32x2*)a; }
DEVI u32 lds32(u32 a) { return *(AS3 u32*)a; }
DEVI void sts128(u32 a, u32x4 v) { *(AS3 u32x4*)a = v; }
DEVI void sts64(u32 a, u32x2 v) { *(AS3 u32x2*)a = v; }
DEVI void sts32(u32 a, u32 v) { *(AS3 u32*)a = v; }
DEVI u32 rfl(u32 v) { return __builtin_amdgcn_readfirstlane(v); }
DEVI u32 opaque_s(u32 v) { asm volatile("" : "+s"(v)); return v; }
DEVI u32 opaque_v(u32 v) { asm volatile("" : "+v"(v)); return v; }
DEVI void undef(u32x4& v) { asm volatile("" : "=v"(v)); }
DEVI void cfence() { asm volatile("" ::: "memory"); }

DEVI u32x4 mkrs(const void* p, u32 bytes) { return u32x4{(u32)(uintptr_t)p, (u32)((uintptr_t)p >> 32) & 0xFFFFu, bytes, 0x00020000u}; }
template <int MO, int OFF = 0> DEVI void dma(u32 voff, u32x4 rs, u32 soff, u32 mb) {
    asm volatile("s_add_u32 m0, %3, %5\n\ts_nop 0\n\tbuffer_load_dwordx4 %0, %1, %2 offen offset:%4 lds"
                 :: "v"(voff), "s"(rs), "s"(soff), "s"(mb), "i"(OFF), "i"(MO) : "memory", "scc");
}
template <int MO, int OFF = 0> DEVI void dmac(u32 voff, u32x4 rs, u32 soff, u32 mb) {
    asm volatile("s_add_u32 m0, %3, %5\n\ts_nop 0\n\tbuffer_load_dwordx4 %0, %1, %2 offen offset:%4 sc1 lds"
                 :: "v"(voff), "s"(rs), "s"(soff), "s"(mb), "i"(OFF), "i"(MO) : "memory", "scc");
}
DEVI void pref(u32 voff, u32x4 rs, u32 soff) {
    asm volatile("s_mov_b32 m0, %3\n\ts_nop 0\n\tbuffer_load_dword %0, %1, %2 offen sc1 lds" :: "v"(voff), "s"(rs), "s"(soff), "i"(L_DUMP) : "memory");
}
DEVI u32 ldflag(u32 voff, u32x4 rs) {
    u32 v;
    asm volatile("buffer_load_dword %0, %1, %2, 0 offen sc1" : "=v"(v) : "v"(voff), "s"(rs) : "memory");
    return v;
}
DEVI u32x4 ldflag4(u32 voff, u32x4 rs) {
    u32x4 v;
    asm volatile("buffer_load_dwordx4 %0, %1, %2, 0 offen sc1" : "=v"(v) : "v"(voff), "s"(rs) : "memory");
    return v;
}
DEVI void stflag(u32 val, u32 soff, u32x4 rs) {
    asm volatile("s_mov_b64 exec, 1\n\tbuffer_store_dword %0, off, %1, %2 sc1\n\ts_mov_b64 exec, -1" :: "v"(val), "s"(rs), "s"(soff) : "memory");
}
template <int OFF = 0> DEVI void st128(u32x4 v, u32 voff, u32x4 rs, u32 soff) {
    asm volatile("buffer_store_dwordx4 %0, %1, %2, %3 offen offset:%4 sc1\n\ts_nop 1" :: "v"(v), "v"(voff), "s"(rs), "s"(soff), "i"(OFF) : "memory");
}
template <int OFF = 0> DEVI void st128n(u32x4 v, u32 voff, u32x4 rs, u32 soff) {
    asm volatile("buffer_store_dwordx4 %0, %1, %2, %3 offen offset:%4\n\ts_nop 1" :: "v"(v), "v"(voff), "s"(rs), "s"(soff), "i"(OFF) : "memory");
}
template <int N> DEVI void wait_vm() { asm volatile("s_waitcnt vmcnt(%0)" ::"i"(N) : "memory"); }
template <int N> DEVI u32 wait_rfl(u32 v) { u32 r; asm volatile("s_waitcnt vmcnt(%2)\n\tv_readfirstlane_b32 %0, %1" : "=s"(r) : "v"(v), "i"(N) : "memory"); return r; }
template <int N> DEVI bool wait_eq(u32 v, u32 tag) {
    u64 m; asm volatile("s_waitcnt vmcnt(%3)\n\tv_cmp_ne_u32_e64 %0, %2, %1" : "=s"(m) : "v"(v), "s"(tag), "i"(N) : "memory");
    return m == 0ull;
}
template <int N> DEVI bool wait_eq4(u32x4 v, u32 tag) {
    asm volatile("s_waitcnt vmcnt(%1)" : "+v"(v) : "i"(N) : "memory");
    return __builtin_amdgcn_ballot_w64(((v[0] ^ tag) | (v[1] ^ tag) | (v[2] ^ tag) | (v[3] ^ tag)) != 0u) == 0ull;
}
template <int N> DEVI void wait_lgkm() { asm volatile("s_waitcnt lgkmcnt(%0)" ::"i"(N) : "memory"); }
DEVI void bar() { asm volatile("s_barrier" ::: "memory"); }
DEVI void sched0() { __builtin_amdgcn_sched_barrier(0); }
DEVI void spin(u32 voff, u32x4 rs, u32 tag) {
#pragma nounroll
    for (u32 i = 0; i < (1u << 17); ++i) {
        u32 v = ldflag(voff, rs);
        if (wait_eq<0>(v, tag)) break;
        __builtin_amdgcn_s_sleep(SPINS);
    }
}
DEVI void ctl_set(u32 a, u32 v) { asm volatile("s_mov_b64 exec, 1\n\tds_write_b32 %0, %1\n\ts_mov_b64 exec, -1" :: "v"(a), "v"(v) : "memory"); }
DEVI u32x4 ctl_get(u32 a) { return *(volatile AS3 u32x4*)a; }
DEVI bool ctl_ge(u32x4 v, u32 t) { return rfl(umin(umin(v[0], v[1]), umin(v[2], v[3]))) >= t; }
DEVI void ctl_wait(u32 a, u32 t) {
#pragma nounroll
    for (u32 i = 0; i < (1u << 20); ++i) { if (ctl_ge(ctl_get(a), t)) break; __builtin_amdgcn_s_sleep(1); }
    cfence();
}
DEVI void ctl_wait8(u32 t) {
#pragma nounroll
    for (u32 i = 0; i < (1u << 20); ++i) {
        const u32x4 x = ctl_get(L_HQ), y = ctl_get(L_MC);
        if (ctl_ge(u32x4{umin(x[0], y[0]), umin(x[1], y[1]), umin(x[2], y[2]), umin(x[3], y[3])}, t)) break;
        __builtin_amdgcn_s_sleep(1);
    }
    cfence();
}
DEVI void ctl_wait1(u32 a, u32 t) {
#pragma nounroll
    for (u32 i = 0; i < (1u << 20); ++i) { if (rfl(*(volatile AS3 u32*)a) >= t) break; __builtin_amdgcn_s_sleep(1); }
    cfence();
}
DEVI bool marked(u32 m) { return __builtin_amdgcn_ballot_w64(m == 0xFFFFFFFFu) != 0ull; }
DEVI u32 mx4(u32 m, u32x4 v) { return umax(umax(umax(m, v[0]), v[1]), umax(v[2], v[3])); }

DEVI float phi(float t) { return __builtin_amdgcn_fmed3f(t + 1.f, __builtin_amdgcn_exp2f(t * LOG2E), 1.f) + 1e-6f; }
DEVI u32 phi2(float a, float b) { return pk2(phi(a), phi(b)); }
template <int OFF> DEVI void ul128(u32x4& v, u32 voff, u32x4 rs, u32 soff) {
    asm volatile("buffer_load_dwordx4 %0, %1, %2, %3 offen offset:%4 sc1" : "=v"(v) : "v"(voff), "s"(rs), "s"(soff), "i"(OFF) : "memory");
}
template <int OFF> DEVI void rl128(u32x4& v, u32 voff, u32x4 rs, u32 soff) {
    asm volatile("buffer_load_dwordx4 %0, %1, %2, %3 offen offset:%4 sc1" : "+v"(v) : "v"(voff), "s"(rs), "s"(soff), "i"(OFF) : "memory");
}
DEVI u32x4 ld128(u32 voff, u32x4 rs, u32 soff) {
    u32x4 v;
    asm volatile("buffer_load_dwordx4 %0, %1, %2, %3 offen" : "=v"(v) : "v"(voff), "s"(rs), "s"(soff) : "memory");
    return v;
}
template <int OFF> DEVI void rl64(u32x2& v, u32 voff, u32x4 rs, u32 soff) {
    asm volatile("buffer_load_dwordx2 %0, %1, %2, %3 offen offset:%4 sc1" : "+v"(v) : "v"(voff), "s"(rs), "s"(soff), "i"(OFF) : "memory");
}
template <int N> DEVI void wait5(u32x4 (&o)[5]) {
    asm volatile("s_waitcnt vmcnt(%5)" : "+v"(o[0]), "+v"(o[1]), "+v"(o[2]), "+v"(o[3]), "+v"(o[4]) : "i"(N) : "memory");
}
DEVI void tief(f32x4& v) { asm volatile("" : "+v"(v)); }

#define STAMP(i)
#define STAMPC(i)

#define SPINTO(k)
template <int N> struct IC { static constexpr int v = N; };
template <int I, int N, class F> DEVI void sfor(F&& f) { if constexpr (I < N) { f(IC<I>{}); sfor<I + 1, N>(f); } }

#define ROTA 1
#define ROTB 3
#define UPTO {2, 4, 5, 6, 7, 8, 9, 10, 10, 10, 10}
#define HK1 3
#define HK2 5
#define HA 4
#define HB 6
#define XPM 0
#define HPRIO 3
#define QPRIO 0
#define PFQ -1
#define PFO 0
#define OLATE 1
#define FQS 1
#define CPF 0
#define ASM 0
#define FSB 6
#define HWN 200
#define HWS 1
#define PFW 0
#define WU 0
#define OAUX 0
#define MKS -1
#define SWP 4
#define NUP 2
#define LD0 1
#define LDS_ 2
#define CK0 18
#define TPM 1
#define TPH 0
#define W1L 0
#define QD0 10
#define QDS 2
#define MKA 1
#define EFC 0
constexpr int qcnt(int n) { return QD0 < 0 ? 10 : n < QD0 ? 0 : (n - QD0) / QDS + 1 > 10 ? 10 : (n - QD0) / QDS + 1; }
#define WA 25
#define XNOW 0
constexpr u64 lem(int k) { u64 m = 0; for (int l = 0; l < 64; ++l) if (k + 4 * (l >> 5) <= (l & 31)) m |= 1ull << l; return m; }
__device__ constexpr int UP[11] = UPTO;
__global__ __launch_bounds__(512, 2) void fused_kernel(const u16* __restrict__ x, const u16* __restrict__ wa, const u16* __restrict__ woa,
                                                       const u16* __restrict__ wb, const u16* __restrict__ wob, const u16* __restrict__ gw,
                                                       u16* out, char* __restrict__ ws) {
    const u32 lane = threadIdx.x & 63;
    const u32 wid = rfl(threadIdx.x >> 6);
    const u32 b = blockIdx.x, xcd = b & 7, idx = b >> 3;
    STAMP(0);
    STAMPC(62);
    if (wid == 3) {
        u64 pc; u32 len;
        asm volatile("s_getpc_b64 %0\n.Lcode_lo:\n\ts_mov_b32 %1, .Lcode_hi-.Lcode_lo\n\ts_max_u32 %1, %1, .Lcode_hm-.Lcode_lo" : "=s"(pc), "=s"(len) :: "scc");
        u32 o = (idx & 15) * 4096 + lane * 64;
        o = o < len ? o : 0;
        asm volatile("s_mov_b32 m0, %2\n\ts_nop 0\n\tglobal_load_lds_dword %0, %1" :: "v"(o), "s"(pc), "i"(L_DUMP) : "memory");
    }
    const u32 rb = (xcd & 1) * 8 + (idx >> 2), c4 = idx & 3, sg = xcd >> 1;
    const u32 ty = c4 >> 1;
    const u32 hg = ty ? 2 * sg + (c4 & 1) : sg + 4 * (c4 & 1);
    const u32 hp = 4 * sg + c4;
    const u32 wr = wid & 3;
    const u32x4 rws = mkrs(ws, WS_END);
    const __amdgpu_buffer_rsrc_t wsr = __builtin_amdgcn_make_buffer_rsrc(ws, 0, WS_END, 0x00020000);
    const u32 l15 = lane & 15, g4 = lane >> 4, l31 = lane & 31, hf = lane >> 5, l16 = lane * 16;
    const u32 me = (wid >= 4 ? L_HQ : L_MC) + wr * 4;
    const u32 oprod = WS_O + (rb * 8 + 2 * wr) * 40960 + sg * 5120;
    u32x4 qf; u32x2 qr; float gt;
    u32x4 P[3]; float g0, g1;
    u32 tag;
    const u32 r64 = xcd * 4 + (idx >> 3), c80 = idx & 7;
    u32 f2o;
    u32x4 W[25];
    bool wok;
    const u32 fso = WS_FS + (lane >> 2) * 256 + (lane & 3) * 16;
    auto wld = [&](auto I, u32 vw) __attribute__((always_inline)) { ul128<(I.v & 3) * 1024>(W[I.v], vw, rws, WS_WO + c80 * 204800 + (wid ^ SWP) * 25600 + (I.v >> 2) * 4096); };
    auto wlate = [&]() __attribute__((always_inline)) {
        if (!wok) {
#pragma nounroll
            for (u32 i = 0; i < (1u << 17); ++i) { if (wait_eq4<0>(ldflag4(fso, rws), tag)) break; __builtin_amdgcn_s_sleep(SPINS); }
            sfor<0, WA>([&](auto I) { rl128<(I.v & 3) * 1024>(W[I.v], l16, rws, WS_WO + c80 * 204800 + (wid ^ SWP) * 25600 + (I.v >> 2) * 4096); });
        }
    };
    u32 ka, kc, kr, va[2][2], mk[8];
    auto asetup = [&](u32 hh) __attribute__((always_inline)) {
        const u32 ch = 2 * (g4 & 1) + (lane & 1), kb = L_K + wr * 2560 + ((lane >> 1) & 1) * 8;
        ka = kb + hh * 1024 + ch * 256 + (((8 * hf + (l15 >> 2)) ^ (4 * ch)) * 16); kc = ka ^ 64;
        kr = kb + 2048 + ch * 128 + ((hh ^ (ch >> 1)) * 64) + (l15 >> 2) * 16;
#pragma unroll
        for (int p = 0; p < 2; ++p)
#pragma unroll
            for (int m = 0; m < 2; ++m) {
                const u32 bi = wr + p, c = 2 * m + hf;
                va[p][m] = L_V + (bi >> 1) * 5136 + (l31 < 20 ? (bi & 1) * 2560 + (l31 < 16 ? hh * 1024 + c * 256 + ((l31 ^ (4 * c)) * 16) : 2048 + c * 128 + ((hh ^ m) * 64) + (l31 - 16) * 16) : 5120);
            }
        sfor<0, 8>([&](auto J) {
            constexpr int k0 = ((2 * J.v) & 3) + 8 * ((2 * J.v) >> 2);
            mk[J.v] = (__builtin_amdgcn_inverse_ballot_w64(lem(k0)) ? 0xFFFFu : 0u) | (__builtin_amdgcn_inverse_ballot_w64(lem(k0 + 1)) ? 0xFFFF0000u : 0u);
        });
        f2o = WS_F2 + ((lane & 16) ? (r64 >> 1) * 256 + l15 * 4 : l15 * 256 + ((hp & 12) + 2 + ((hp >> 1) & 1)) * 4);
        asm volatile("" : "+v"(ka), "+v"(kc), "+v"(kr), "+v"(va[0][0]), "+v"(va[0][1]), "+v"(va[1][0]), "+v"(va[1][1]), "+v"(f2o), "+s"(out));
        asm volatile("" : "+v"(mk[0]), "+v"(mk[1]), "+v"(mk[2]), "+v"(mk[3]), "+v"(mk[4]), "+v"(mk[5]), "+v"(mk[6]), "+v"(mk[7]));
    };

    if (__builtin_expect(wid >= 4, 1)) {
        const u32 rot = (ROTA * rb + ROTB * c4) % 10;
        auto kbof = [&](u32 s) { u32 k = rot + s; return k >= 10 ? k - 10 : k; };
        const u32x4 rsx = mkrs(x, 2048 * 1280), rsw = mkrs(ty ? wb : wa, 640 * 3840);
        const u32 vx = (rb * 128 + 32 * wr + (lane >> 3)) * 1280 + (((lane & 7) ^ ((lane >> 3) & 6)) * 16);
        u32 vo[5];
#pragma unroll
        for (int i = 0; i < 5; ++i) {
            const u32 ch = 64 * i + lane, pr = (ch * 205) >> 12, j = ch - pr * 20;
            const u32 k = 32 * (wr >> 1) + 8 * (wr & 1) + (pr & 7) + 2 * (pr & 8);
            vo[i] = k * 3840 + (ty ? 1280 + 160 * hg + (j >= 10 ? 1120 : 0) : 1280 + 320 * hg) + j * 16;
        }
        auto issuep = [&](auto SC, auto A, auto B) {
            constexpr int s = decltype(SC)::v;
            const u32 kb = kbof(s);
            sfor<A.v, B.v>([&](auto I) {
                if constexpr (I.v < 4) dma<SL[s % 4] + XO + I.v * 1024>(vx, rsx, kb * 128 + I.v * 10240, wr * 4096);
                else dma<SL[s % 4] + (I.v - 4) * 1024>(vo[I.v - 4], rsw, kb * 245760, wr * WREG);
            });
        };
        auto issue = [&](auto SC) { issuep(SC, IC<0>{}, IC<9>{}); };
        STAMP(30);
        issue(IC<0>{});
        STAMP(31);
        {
            const u32 o = l16 + (wr & 1) * 1024 + rot * 128;
            dma<L_GW>(o >= 1280 ? o - 1280 : o, mkrs(gw, 1280), 0, (wr & 1) * 1024);
        }
        sfor<1, UP[0]>(issue);
        STAMP(1);
        const u32 xb0 = XO + (32 * wr + l15) * 128 + ((g4 ^ (l15 & 6)) * 16), xb1 = xb0 ^ 64;
        const u32 xc0 = xb0 + 2 * SLOT, xc1 = xb1 + 2 * SLOT, ga = L_GW + g4 * 16;
        const u32 qm0 = (wr & 1) * 5120 + (1 - (wr >> 1)) * 10240, qs0 = WS_WQ + hp * 102400 + (wr & 1) * 5120;
        auto qissue = [&](auto JC) {
            constexpr int jj = decltype(JC)::v;
            const u32 so = qs0 + kbof(2 * jj + (wr >> 1)) * 10240;
            sfor<0, 4>([&](auto I) { dmac<L_QT - 20480 * (jj + 1), I.v * 1024>(l16, rws, so, qm0); });
            dmac<L_QT - 20480 * (jj + 1) + 4096>(l16, rws, so + 4096, qm0);
        };
        auto qone = [&](auto KC) {
            constexpr int jj = 3 + decltype(KC)::v / 5, i = decltype(KC)::v % 5;
            const u32 so = qs0 + kbof(2 * jj + (wr >> 1)) * 10240;
            if constexpr (i < 4) dmac<L_QT - 20480 * (jj + 1), i * 1024>(l16, rws, so, qm0);
            else dmac<L_QT - 20480 * (jj + 1) + 4096>(l16, rws, so + 4096, qm0);
        };
        const u32 fqo = WS_FQ + sg * 1024 + g4 * 256 + l15 * 4;
        bf16x8 xf[10][2][2];
        f32x4 gacc[2];
        u32 pv = 0;
        bool qok = true;
        auto wojob = [&](auto ST) {
#pragma unroll
            for (u32 j = 0; j < 2; ++j) {
                const u32 p = j * 1024 + b * 4 + wr;
                const bool br = p >= 800;
                const u32 q = br ? p - 800 : p, oct = q / 10, cl = q - oct * 10;
                const u32 kx = oct >> 2, go = oct & 3, G = kx / 5, e = kx - G * 5;
                if constexpr (!ST.v) {
                    const u32 r0 = 160 * G + (e == 4 ? 40 * go + 16 : 40 * e + (br ? 20 * (go >> 1) + 4 * (go & 1) : 4 * go));
                    const u32 step = e != 4 && br ? 8 : 20;
                    dma<L_SCR>(p < 1600 ? (((lane >> 3) & 3) + step * hf) * 1280 + (lane & 7) * 16 : OOB, mkrs(br ? woa : wob, 640 * 1280),
                               p < 1600 ? r0 * 1280 + cl * 128 : 0, wr * 2048 + j * 1024);
                } else {
                    const u32 n = 64 * cl + lane, h = (n * 205) >> 12, d = n - 20 * h;
                    const u32 jd = p < 1600 ? WS_WO + ((h >> 2) * 2 + br) * 102400 + (kx * 5 + (d >> 2)) * 1024 + (go * 16 + 4 * (h & 3) + (d & 3)) * 16 : OOB;
                    const u32 js = L_SCR + wr * 2048 + j * 1024 + (l15 >> 2) * 128 + (4 * g4 + (lane & 3)) * 8;
                    st128(__builtin_bit_cast(u32x4, ldtr2(js, js + 512)), jd, rws, 0);
                }
            }
        };
        constexpr int XA[10] = {0, 2, 0, 0, 2, 0, 1, 0, 0, 0}, XB[10] = {0, 0, 0, PFQ == 3, PFQ == 4, PFQ == 5, 0, 0, 0, 0};
        sfor<0, 10>([&](auto SC) {
            constexpr int s = decltype(SC)::v;
            wait_lgkm<0>();
            constexpr int ex = s < 2 ? 0 : s < 4 ? XA[1] : XB[s - 3] + XA[s - 2] + XB[s - 2] + XA[s - 1] + XB[s - 1];
            if constexpr (s < 8) wait_vm<9 * (UP[s] - s - 1) + ex - (s == FSB && s < 7 ? XA[4] : 0)>();
            if constexpr (s == 9) { if (qok) wait_vm<15>(); else wait_vm<0>(); }
            bar();
            if constexpr (s == 0) __builtin_amdgcn_s_setprio(HPRIO);
            if constexpr (s == 0) SPINTO(2);
            if constexpr (s == 4) SPINTO(6);
            STAMP(2 + s);
            if constexpr (s == 0 || s == 2 || s == 4 || s == 7) STAMPC(s == 0 ? 51 : s == 2 ? 52 : s == 4 ? 53 : 54);
            constexpr bool up = SL[s % 4] >= 2 * SLOT, any = UP[s + 1] > UP[s];
            constexpr u32 xo = SL[s % 4] - (up ? 2 * SLOT : 0);
            const u32 a0 = up ? xc0 : xb0, a1 = up ? xc1 : xb1;
            if constexpr (s == 1) wojob(IC<0>{});
            if constexpr (s == 4) wojob(IC<1>{});
            if constexpr (s == 5) tag = rfl(lds32(L_TAG));
            if constexpr (s == 6) pv = ldflag(fqo, rws);
            if constexpr (s == 7) { qok = wait_eq<9>(pv, tag); if (qok) qissue(IC<0>{}); }
            if constexpr (s == 8) { if (qok) qissue(IC<1>{}); }
            if constexpr (s == 9 && QD0 < 0) { if (qok) qissue(IC<3>{}); }
            if constexpr (any) issuep(IC<UP[s]>{}, IC<0>{}, IC<HA>{});
            xf[s][0][0] = asbf8(lds128(a0 + xo)); xf[s][0][1] = asbf8(lds128(a0 + xo + 2048));
            xf[s][1][0] = asbf8(lds128(a1 + xo)); xf[s][1][1] = asbf8(lds128(a1 + xo + 2048));
            const bf16x8 w0 = asbf8(lds128(ga + s * 128)), w1 = asbf8(lds128(ga + s * 128 + 64));
            if constexpr (any) issuep(IC<UP[s]>{}, IC<HA>{}, IC<HB>{});
            mfma16a<s == 0>(gacc[0], w0, xf[s][0][0]); mfma16a<s == 0>(gacc[1], w0, xf[s][0][1]);
            mfma16a(gacc[0], w1, xf[s][1][0]); mfma16a(gacc[1], w1, xf[s][1][1]);
            if constexpr (any) { issuep(IC<UP[s]>{}, IC<HB>{}, IC<9>{}); sfor<UP[s] + 1, UP[s + 1]>(issue); }
            if constexpr (s == PFQ) pref(lane < 25 ? lane * 128 : OOB, rws, WS_WQ + sg * 409600 + (idx * 4 + wr) * 3200);
            if constexpr (s == 8) { if (qok) qissue(IC<2>{}); }
            if constexpr (s == 9 && QD0 < 0) { if (qok) qissue(IC<4>{}); }
        });
        __builtin_amdgcn_s_setprio(QPRIO);
        if (!qok) { spin(fqo, rws, tag); sfor<0, (QD0 < 0 ? 5 : 3)>(qissue); }
        asm volatile("s_nop 7\n\ts_nop 3" : "+v"(gacc[0]), "+v"(gacc[1]));
        wait_vm<10 + qcnt(-1)>();
        ctl_set(me, 1);
        const u32 lhi = opaque_v(l16 + L_QT - 51200);
        ctl_wait(L_HQ, 1);
        SPINTO(12);
        STAMP(12);
        STAMPC(55);
        constexpr int PD = 4;
        bf16x8 wf[PD + 1];
        f32x4 qa[2][5];
        u32x4 snap, fs;
        u32 vw;
        auto wadr = [&](int n) {
            const int j = n / 10;
            return (j < 5 ? lhi + 40960 - 10240 * j : l16 + L_QT - 10240 * (j + 1)) + (n % 10) * 1024;
        };
        sfor<0, PD>([&](auto NC) { wf[NC.v] = asbf8(lds128(wadr(NC.v))); });
        sfor<0, 100>([&](auto NC) {
            constexpr int n = decltype(NC)::v, m = n + PD;
            constexpr int st = n / 10, ks = (n % 10) / 5, a = n % 5;
            const bf16x8 w = wf[n % (PD + 1)];
            if constexpr (m < 100 && m % 20 == 0) { if (!ctl_ge(snap, m / 20 + 1)) ctl_wait(L_HQ, m / 20 + 1); cfence(); }
            sched0();
            mfma16a<(n < 5)>(qa[0][a], w, xf[st][ks][0]);
            sched0();
            if constexpr (m < 100) wf[m % (PD + 1)] = asbf8(lds128(wadr(m)));
            sched0();
            mfma16a<(n < 5)>(qa[1][a], w, xf[st][ks][1]);
            sched0();
            if constexpr (QD0 >= 0 && qcnt(n) > qcnt(n - 1)) qone(IC<qcnt(n) - 1>{});
            if constexpr (n < 80 && n % 20 == 8) {
                static_assert(5 + qcnt(n) - 5 * (n / 20) >= 0, "q schedule");
                wait_vm<5 + qcnt(n) - 5 * (n / 20)>();
                ctl_set(me, n / 20 + 2);
            }
            if constexpr (n < 80 && n % 20 == 12) snap = ctl_get(L_HQ);
            if constexpr (n == 50 && HWN < 200) fs = ldflag4(fso, rws);
            if constexpr (n == 69 && HWN < 200) { wok = wait_eq4<0>(fs, tag); vw = wok ? l16 : OOB; }
            if constexpr (n >= HWN && (n - HWN) % HWS == 0 && (n - HWN) / HWS < 25) wld(IC<(n - HWN) / HWS>{}, vw);
            sched0();
        });
        constexpr int HW0 = HWN >= 100 ? 0 : (100 - HWN + HWS - 1) / HWS;
        if constexpr (HWN < 200) sfor<HW0, 25>([&](auto I) { wld(I, vw); });
        else sfor<0, 25>([&](auto I) { undef(W[I.v]); });
        mfma_pad();
        STAMP(13);
        STAMPC(56);
        g0 = __builtin_amdgcn_rcpf(1.f + __builtin_amdgcn_exp2f(-gacc[0][0] * LOG2E));
        g1 = __builtin_amdgcn_rcpf(1.f + __builtin_amdgcn_exp2f(-gacc[1][0] * LOG2E));
        gt = (lane & 16) ? g1 : g0;
        {
            auto cvt = [&](int a, u32x4& o) {
                auto s0 = __builtin_amdgcn_permlane16_swap(pk2(qa[0][a][0] * QSCALE, qa[0][a][1] * QSCALE), pk2(qa[1][a][0] * QSCALE, qa[1][a][1] * QSCALE), false, false);
                auto s1 = __builtin_amdgcn_permlane16_swap(pk2(qa[0][a][2] * QSCALE, qa[0][a][3] * QSCALE), pk2(qa[1][a][2] * QSCALE, qa[1][a][3] * QSCALE), false, false);
                o = u32x4{s0[0], s1[0], s0[1], s1[1]};
            };
            u32x4 r, q1;
            cvt(4, q1); cvt(2, r);
            sts128(L_XQ + wr * 1536 + l16, q1);
            sts64(L_XQ + wr * 1536 + 1024 + lane * 8, u32x2{hf ? f2u(gt) : r[2], r[3]});
            ctl_set(me, 6);
            cvt(3, qf);
            qr = hf ? u32x2{0, 0} : u32x2{r[0], r[1]};
        }
#pragma unroll
        for (int t = 0; t < 2; ++t) {
            P[t] = u32x4{phi2(qa[t][0][0], qa[t][0][1]), phi2(qa[t][0][2], qa[t][0][3]), phi2(qa[t][1][0], qa[t][1][1]), phi2(qa[t][1][2], qa[t][1][3])};
            const u32 u = phi2(qa[t][2][0], qa[t][2][1]), v = phi2(qa[t][2][2], qa[t][2][3]);
            P[2][2 * t] = __builtin_amdgcn_permlane32_swap(u, u, false, false)[1];
            P[2][2 * t + 1] = __builtin_amdgcn_permlane32_swap(v, v, false, false)[1];
        }
        asm volatile("" : "+v"(P[0]), "+v"(P[1]), "+v"(P[2]), "+v"(qf), "+v"(qr));
        asetup(0);
        if constexpr (HWN < 200) wlate();
        STAMP(14);
        __builtin_amdgcn_s_setprio(0);
        ctl_wait(L_MC, 3);
        SPINTO(16);
        STAMP(16);
        STAMPC(57);
    } else {
        const u32 wm = wr >> 1, wn = wr & 1;
        const u32 ev = ldflag(0, rws);
        const u32 pw0 = (xcd & 1) * 128 + idx * 4 + wr;
        u32 qd[2];
#pragma unroll
        for (u32 j = 0; j < 2; ++j) {
            const u32 p = pw0 + 256 * j, isb = p >= 200, q = isb ? p - 200 : p, rg = (q * 205) >> 10;
            const u32 so = p < 400 ? rg * 61440 + sg * 320 + (q - rg * 5) * 64 : 0;
            dma<L_SCR>(p < 400 ? (lane >> 2) * 3840 + (lane & 3) * 16 : OOB, mkrs(isb ? wb : wa, 640 * 3840), so, wr * 2048 + j * 1024);
            const u32 n = (q - rg * 5) * 32 + l31, h = (n * 205) >> 12, d = n - 20 * h, o8 = 2 * rg + hf;
            const u32 t = d < 16 ? (h & 1) * 1024 + (isb ? 0 : 3072) + d * 16 : 2048 - 256 + (8 * isb + 4 * (h & 1) + d) * 16;
            qd[j] = p < 400 ? WS_WQ + (4 * sg + (h >> 1)) * 102400 + (o8 >> 2) * 5120 + (o8 & 3) * 256 + t : OOB;
        }
        const u32 krec = WS_KVP + ((2 * hg + wn) * 16 + rb) * 2048 + wm * 1024;
        if (lane < 2) sts32(L_HQ + lane * 16 + wr * 4, 0);
        u32 wb_[2][3], xb_[2][2];
        {
            const u32 q3 = lane & 3, wrow = (g4 & 1) * WREG + (8 * (g4 >> 1) + (l15 >> 2)) * 320;
            wb_[0][0] = wrow + (ty ? 80 : 160) * wn + q3 * 8;
            wb_[0][1] = wb_[0][0] + (ty ? 160 : 80);
            wb_[0][2] = wrow + 32 + (ty ? 80 * wn + ((q3 & 2) ? 0 : 160) + 40 * (q3 & 1) : 160 * wn + 40 * q3);
            xb_[0][0] = XO + (64 * wm + 16 * (l15 >> 3) + (l15 & 7)) * 128 + ((g4 ^ (l15 & 6)) * 16);
            xb_[0][1] = xb_[0][0] ^ 64;
#pragma unroll
            for (int i = 0; i < 3; ++i) wb_[1][i] = opaque_v(wb_[0][i] + 2 * SLOT);
#pragma unroll
            for (int i = 0; i < 2; ++i) xb_[1][i] = opaque_v(xb_[0][i] + 2 * SLOT);
        }
        const u32 qsrc = L_SCR + wr * 2048 + (8 * hf + (l15 >> 2)) * 64 + (g4 & 1) * 32 + (lane & 3) * 8;
        auto wad = [&](int s, int m) { return wb_[SL[s % 4] >= 2 * SLOT][(m % 5) >> 1] + SL[s % 4] % (2 * SLOT) + (m / 5) * (2 * WREG) + ((m % 5) & 1) * 40; };
        auto xad = [&](int s, int ks, int t) { return xb_[SL[s % 4] >= 2 * SLOT][ks] + SL[s % 4] % (2 * SLOT) + (t >> 1) * 4096 + (t & 1) * 1024; };
        f32x4 acc[4][5];
        STAMP(30);
        tag = wait_rfl<0>(ev) + TAGC;
        STAMP(31);
        st128(__builtin_bit_cast(u32x4, ldtr2(qsrc, qsrc + 256)), qd[0], rws, 0);
        st128(__builtin_bit_cast(u32x4, ldtr2(qsrc + 1024, qsrc + 1280)), qd[1], rws, 0);
        auto marker = [&](int i) {
            const u32x4 mk4 = {~0u, ~0u, ~0u, ~0u};
            if (i < 4) st128(mk4, l16, rws, oprod + c4 * 1024 + i * 20480);
            else st128(mk4, (g4 >> 1) * 40960 + (g4 & 1) * 20480 + l15 * 16, rws, oprod + 4096 + c4 * 256);
        };
        auto markers = [&]() { if constexpr (MKS < 0) sfor<0, 5>([&](auto I) { marker(I.v); }); };
        constexpr int MKN = MKS < 0 ? 5 : 0;
        if (wr == 0 && lane == 0) sts32(L_TAG, tag);
        {
            bf16x8 wf[5], xa[2][4];
            STAMP(1);
            wait_lgkm<0>();
            bar();
            SPINTO(2);
            STAMP(2);
            sfor<0, 4>([&](auto I) { xa[0][I.v] = asbf8(lds128(xad(0, 0, I.v))); });
            wf[0] = ldtr2(wad(0, 0), wad(0, 0) + 1280);
            wf[1] = ldtr2(wad(0, 1), wad(0, 1) + 1280);
            sfor<0, 100>([&](auto NC) {
                constexpr int s = decltype(NC)::v / 10, n = decltype(NC)::v % 10, ks = n / 5, c = n % 5;
                constexpr int s2 = n >= 8 ? s + 1 : s, m = (n + 2) % 10;
                constexpr bool more = s2 < 10, first = s == 0 && ks == 0, rdw = more && (!(XPM & 1) || s == 0), rdx = !(XPM & 2) || s == 0;
                bf16x4 lo, hi;
                bf16x8 t0, t1;
                auto mm = [&](auto I) { sched0(); if constexpr (!(XPM & 4) || s == 0) mfma16a<first>(acc[I.v][c], xa[ks][I.v], wf[c]); else asm volatile("" :: "v"(xa[ks][I.v]), "v"(wf[c])); sched0(); };
                mm(IC<0>{});
                if constexpr (n == 8 && more) {
                    wait_lgkm<0>();
                    if constexpr (s == FQS) wait_vm<0>();
                    bar();
                    sched0();
                }
                if constexpr (rdw) lo = ldtr(wad(s2, m));
                mm(IC<1>{});
                if constexpr (rdw) hi = ldtr(wad(s2, m) + 1280);
                mm(IC<2>{});
                if constexpr (n >= 8 && more && rdx) t0 = asbf8(lds128(xad(s2, 0, 2 * (n - 8))));
                if constexpr (n < 4 && rdx) xa[1][n] = asbf8(lds128(xad(s, 1, n)));
                mm(IC<3>{});
                if constexpr (n >= 8 && more && rdx) { t1 = asbf8(lds128(xad(s2, 0, 2 * (n - 8) + 1))); xa[0][2 * (n - 8)] = t0; xa[0][2 * (n - 8) + 1] = t1; }
                if constexpr (s == MKS && n % 2 == 0) marker(n / 2);
                if constexpr (s == FQS && n == 8) { if (wr == 0) stflag(tag, WS_FQ + sg * 1024 + (idx >> 3) * 256 + ((idx & 7) * 2 + (xcd & 1)) * 4, rws); }
                if constexpr (s == FSB - 1 && n == 8) { if (wr == 1) stflag(tag, WS_FS + (b >> 4) * 256 + (b & 15) * 4, rws); }
                if constexpr (rdw) wf[m % 5] = cat8(lo, hi);
                sched0();
            });
        }
        mfma_pad();
        __builtin_amdgcn_s_setprio(PRIO);
        SPINTO(12);
        STAMP(12);
        auto pk8 = [&](int u, int c) {
            return u32x4{pk2(acc[2 * u][c][0], acc[2 * u][c][1]), pk2(acc[2 * u][c][2], acc[2 * u][c][3]),
                         pk2(acc[2 * u + 1][c][0], acc[2 * u + 1][c][1]), pk2(acc[2 * u + 1][c][2], acc[2 * u + 1][c][3])};
        };
        auto ph8 = [&](int u, int c) {
            return u32x4{phi2(acc[2 * u][c][0], acc[2 * u][c][1]), phi2(acc[2 * u][c][2], acc[2 * u][c][3]),
                         phi2(acc[2 * u + 1][c][0], acc[2 * u + 1][c][1]), phi2(acc[2 * u + 1][c][2], acc[2 * u + 1][c][3])};
        };
        if (!ty) {
            const u32x4 fs = ldflag4(fso, rws);
            const u32 so = WS_T + ((4 * hg + 2 * wn) * 64 + 4 * rb + 2 * wm) * 2560;
            const u32 vm = g4 * 256 + ((l15 ^ (4 * g4)) * 16);
            const u32 vr = 2048 + g4 * 128 + ((((l15 >> 2) ^ (g4 >> 1)) & 1) * 64) + (l15 & 3) * 16 + (l15 >> 3) * 163840;
            sfor<0, 10>([&](auto I) {
                constexpr int c = decltype(I)::v >> 1, u = decltype(I)::v & 1;
                st128<(c < 4 ? (c & 1) * 1024 : 0) + u * 2560>(pk8(u, c), c < 4 ? vm : vr, rws, so + (c == 2 || c == 3 ? 163840 : 0));
            });
            STAMP(32);
            if constexpr (!MKA) markers();
            STAMP(33);
            wok = wait_eq4<MKA ? 0 : MKN>(fs, tag);
            stflag(tag, WS_FT + ((hg * 2 + wn) * 32 + 2 * rb + wm) * 128, rws);
            if constexpr (MKA) markers();
            STAMP(34);
            const u32 vw = wok ? l16 : OOB;
            sfor<0, WA>([&](auto I) { wld(I, vw); });
        } else {
            const u32x4 fs = ldflag4(fso, rws);
            markers();
            STAMP(33);
            if constexpr (PFW) {
                const u32 po = WS_WO + (((idx >> 2) * 2 + (idx & 1)) * 4 + wr) * 25600;
                pref(lane * 128, rws, po); pref(lane * 128, rws, po + 8192); pref(lane * 128, rws, po + 16384); pref(lane < 8 ? lane * 128 : OOB, rws, po + 24576);
            }
            f32x4 Q0 = {0, 0, 0, 0}, Q1 = Q0, X0 = Q0, X1 = Q0, Y0 = Q0, Y1 = Q0, U = Q0;
#pragma unroll
            for (int u = 0; u < 2; ++u) {
                const bf16x8 v0 = asbf8(pk8(u, 2)), v1 = asbf8(pk8(u, 3));
                const u32x4 rr = pk8(u, 4);
                const bf16x8 rbv = asbf8(l15 < 8 ? rr : u32x4{ONES, ONES, ONES, ONES});
                const bf16x8 k0 = asbf8(ph8(u, 0));
                Q0 = mfma16(k0, v0, Q0); X0 = mfma16(k0, rbv, X0);
                const bf16x8 k1 = asbf8(ph8(u, 1));
                Q1 = mfma16(k1, v1, Q1); X1 = mfma16(k1, rbv, X1);
                const bf16x8 ra = asbf8(ph8(u, 4));
                Y0 = mfma16(ra, v0, Y0); Y1 = mfma16(ra, v1, Y1); U = mfma16(ra, rbv, U);
                if (u == WU) {
                    sched0();
                    STAMP(34);
                    wok = wait_eq4<MKN + 4 * PFW>(fs, tag);
                    const u32 vw = wok ? l16 : OOB;
                    sfor<0, WA>([&](auto I) { wld(I, vw); });
                    STAMP(32);
                    sched0();
                }
            }
            f32x4 X = (l15 & 4) ? X1 : X0, Z;
#pragma unroll
            for (int r = 0; r < 4; ++r) Z[r] = u2f(__builtin_amdgcn_permlane32_swap(f2u((g4 & 1) ? Y1[r] : Y0[r]), f2u(U[r]), false, false)[1]);
            sts128(L_SCR + wr * 2048 + l16, __builtin_bit_cast(u32x4, wm ? Q0 : X));
            sts128(L_SCR + wr * 2048 + 1024 + l16, __builtin_bit_cast(u32x4, wm ? Q1 : Z));
            ctl_set(me, 1);
            ctl_wait1(L_MC + (wr ^ 2) * 4, 1);
            const f32x4 a = (wm ? X : Q0) + __builtin_bit_cast(f32x4, lds128(L_SCR + (wr ^ 2) * 2048 + l16));
            const f32x4 c = (wm ? Z : Q1) + __builtin_bit_cast(f32x4, lds128(L_SCR + (wr ^ 2) * 2048 + 1024 + l16));
            st128(u32x4{pk2(a[0], a[1]), pk2(a[2], a[3]), pk2(c[0], c[1]), pk2(c[2], c[3])}, l16, rws, krec);
        }
        STAMP(13);
        if constexpr (CPF) {
            u64 pc; u32 len; int rel;
            asm volatile("s_getpc_b64 %0\n.Lcode_p2:\n\ts_mov_b32 %2, .Lcode_at-.Lcode_p2\n\ts_mov_b32 %1, .Lcode_hi-.Lcode_at" : "=s"(pc), "=s"(len), "=s"(rel));
            pc += (long long)rel;
            u32 o = wr * 8192 + lane * 128;
            o = o < len ? o : 0;
            asm volatile("s_mov_b32 m0, %2\n\ts_nop 0\n\tglobal_load_lds_dword %0, %1" :: "v"(o), "s"(pc), "i"(L_DUMP) : "memory");
        }
        {
            const int g0 = 2 * (int)rb - 4 + 3 * (int)wn;
            int gl = g0 + (int)(lane < 2 ? lane : 2);
            gl = gl < 0 ? 0 : gl;
            spin(WS_FT + ((wm * 8 + (hp >> 1)) * 32 + gl) * 128, rws, tag);
            ctl_set(me, 2);
            ctl_wait8(2);
            if (wr == 0) stflag(tag, WS_F2 + rb * 256 + hp * 4, rws);
            STAMP(14);
#pragma unroll
            for (int i = 0; i < 3; ++i) {
                const u32 mb = wm ? L_V + (3 * wn + i) * 5136 : L_K + (3 * wn + i) * 5120;
                if (g0 + i >= 0) {
                    const u32 so = WS_T + ((wm * 16 + hp) * 32 + g0 + i) * 5120;
                    dmac<0, 0>(l16, rws, so, mb); dmac<0, 1024>(l16, rws, so, mb); dmac<0, 2048>(l16, rws, so, mb); dmac<0, 3072>(l16, rws, so, mb);
                    dmac<4096, 0>(l16, rws, so + 4096, mb);
                } else {
#pragma unroll
                    for (int k = 0; k < 5; ++k) sts128(mb + k * 1024 + l16, u32x4{0, 0, 0, 0});
                }
            }
            if (wm && lane < 12) sts32(L_V + (3 * wn + (lane >> 2)) * 5136 + 5120 + (lane & 3) * 4, g0 + (int)(lane >> 2) >= 0 ? ONES : 0);
            wlate();
            sfor<WA, 25>([&](auto I) { wld(I, l16); });
            asetup(1);
            asm volatile("" : "=v"(P[0]), "=v"(P[1]), "=v"(P[2]));
            wait_vm<25 - WA>();
            ctl_set(me, 3);
            STAMP(15);
        }
        ctl_wait(L_HQ, 6);
        qf = lds128(L_XQ + wr * 1536 + l16);
        const u32x2 r = lds64(L_XQ + wr * 1536 + 1024 + lane * 8);
        qr = hf ? u32x2{0, 0} : r;
        gt = u2f(lds32(L_XQ + wr * 1536 + 1280 + l31 * 8));
        ctl_wait(L_MC, 3);
        if (b == 0 && wr == 0) stflag(tag, WS_E, rws);
        __builtin_amdgcn_s_setprio(0);
        SPINTO(16);
        STAMP(16);
        asm volatile(".Lcode_hm:");
    }
    asm volatile(".Lcode_at:");
    auto recs = [&]() {
        const u32 vo = hf * 2048 + l31 * 16, so = WS_KVP + hp * 32768 + wr * 512;
        sfor<0, 8>([&](auto I) { dmac<L_RC + I.v * 1024>(vo, rws, so + I.v * 4096, wr * 8192); });
    };
    u32 pv;
    bool rok = false;
    {
        const u32 hh = wid < 4;
        f32x16 zero;
#pragma unroll
        for (int i = 0; i < 16; ++i) zero[i] = 0.f;
        const bf16x8 q16 = asbf8(qf);
        const bf16x4 q4 = __builtin_bit_cast(bf16x4, qr);
        auto qk = [&](u32 o) { return mfma32k8(ldtr(kr + o), q4, mfma32(ldtr2(ka + o, kc + o), q16, zero)); };
        f32x16 o = zero;
        sched0();
        bf16x8 j16 = ldtr2(ka + 2560, kc + 2560), v2, v3;
        bf16x4 j4 = ldtr(kr + 2560);
        bf16x8 k16 = ldtr2(ka + 5120, kc + 5120);
        bf16x4 k4 = ldtr(kr + 5120);
        bf16x8 v0 = asbf8(lds128(va[1][0])), v1 = asbf8(lds128(va[1][1]));
        sched0();
        f32x16 S = mfma32k8(j4, q4, mfma32(j16, q16, zero)), Sn, S8;
        sfor<1, 9>([&](auto I) {
            constexpr int it = decltype(I)::v, i = it & 7, nx = (it + 1) & 7;
            auto wl = [&](auto J) {
                if constexpr (HWN >= 200) { if (wid >= 4) rl128<(J.v & 3) * 1024>(W[J.v], l16, rws, WS_WO + c80 * 204800 + (wid ^ SWP) * 25600 + (J.v >> 2) * 4096); }
            };
            float e[16];
            u32 p[8];
            auto ex = [&](auto A, auto B) {
                sfor<A.v, B.v>([&](auto R) {
                    if constexpr (i == 0) e[R.v] = __builtin_amdgcn_exp2f(__builtin_amdgcn_inverse_ballot_w64(lem((R.v & 3) + 8 * (R.v >> 2))) ? S8[R.v] : S[R.v]);
                    else e[R.v] = __builtin_amdgcn_exp2f(S[R.v]);
                });
                sched0();
            };
            auto cv = [&](int a) {
#pragma unroll
                for (int j = a; j < a + 4; ++j) p[j] = pk2(e[2 * j], e[2 * j + 1]);
                sched0();
            };
            sched0();
            if constexpr ((ASM >> it) & 1) STAMP(35 + it);
            if constexpr (it == HK1) pv = ldflag(f2o, rws);
            if constexpr (it == HK2) {
                if (wid < 4) { rok = wait_eq<0>(pv, tag); if (rok) recs(); }
                else rok = wait_eq<(HWN >= 200 ? 3 * (HK2 - HK1) : 0)>(pv, tag);
            }
            if constexpr (it == 1) wl(IC<0>{});
            sched0();
            if constexpr (i != 0) Sn = mfma32(k16, q16, zero);
            sched0();
            ex(IC<0>{}, IC<8>{});
            if constexpr (i != 0) Sn = mfma32k8(k4, q4, Sn);
            sched0();
            if constexpr (it < 7) { k16 = ldtr2(ka + ((it + 2) & 7) * 2560, kc + ((it + 2) & 7) * 2560); k4 = ldtr(kr + ((it + 2) & 7) * 2560); }
            if constexpr (it == 5) { j16 = ldtr2(ka + 8 * 2560, kc + 8 * 2560); j4 = ldtr(kr + 8 * 2560); }
            if constexpr (it == 7) { v2 = asbf8(lds128(va[0][0] + 4 * 5136)); v3 = asbf8(lds128(va[0][1] + 4 * 5136)); }
            wl(IC<3 * it - 2>{});
            sched0();
            cv(0);
            if constexpr (i == 0) { o = mfma32(v2, asbf8(u32x4{p[0] & mk[0], p[1] & mk[1], p[2] & mk[2], p[3] & mk[3]}), o); sched0(); }
            ex(IC<8>{}, IC<12>{});
            if constexpr (i == 0) o = mfma32(v0, asbf8(u32x4{p[0] & ~mk[0], p[1] & ~mk[1], p[2] & ~mk[2], p[3] & ~mk[3]}), o);
            else o = mfma32(v0, asbf8(u32x4{p[0], p[1], p[2], p[3]}), o);
            sched0();
            wl(IC<3 * it - 1>{});
            if constexpr (it == 6) S8 = mfma32(j16, q16, zero);
            sched0();
            ex(IC<12>{}, IC<16>{});
            cv(4);
            if constexpr (i == 0) {
                o = mfma32(v3, asbf8(u32x4{p[4] & mk[4], p[5] & mk[5], p[6] & mk[6], p[7] & mk[7]}), o);
                o = mfma32(v1, asbf8(u32x4{p[4] & ~mk[4], p[5] & ~mk[5], p[6] & ~mk[6], p[7] & ~mk[7]}), o);
            } else o = mfma32(v1, asbf8(u32x4{p[4], p[5], p[6], p[7]}), o);
            sched0();
            if constexpr (it == 6) S8 = mfma32k8(j4, q4, S8);
            if constexpr (i != 0) { v0 = asbf8(lds128(va[nx & 1][0] + (nx >> 1) * 5136)); v1 = asbf8(lds128(va[nx & 1][1] + (nx >> 1) * 5136)); }
            wl(IC<3 * it>{});
            sched0();
            if constexpr (i != 0) S = Sn;
        });
        if constexpr ((ASM >> 9) & 1) STAMP(44);
        const int qq = (int)l31 - 4 * (int)hf;
        float am = 0.f;
#pragma unroll
        for (int r = 0; r < 12; r += 2) am = fmaxf(fmaxf(am, fabsf(o[r])), fabsf(o[r + 1]));
        const bool fine = o[12] > 0x1p-30f && o[12] < 0x1p30f && am < 0x1p120f;
        if (__builtin_amdgcn_ballot_w64(!fine) != 0ull) {
            const int i0 = rb >= 2 ? 0 : 8 - 4 * (int)rb - (int)wr;
            auto sc = [&](int i) {
                f32x16 s = qk(i * 2560);
#pragma unroll
                for (int r = 0; r < 16; ++r) {
                    const bool le = (r & 3) + 8 * (r >> 2) <= qq;
                    const bool ok = i == 0 ? !le : (i == 8 ? le : true);
                    s[r] = ok ? s[r] : -__builtin_inff();
                }
                return s;
            };
            float m = -__builtin_inff();
#pragma nounroll
            for (int i = i0; i < 9; ++i) {
                const f32x16 s = sc(i);
#pragma unroll
                for (int r = 0; r < 16; ++r) m = fmaxf(m, s[r]);
            }
            auto sw = __builtin_amdgcn_permlane32_swap(f2u(m), f2u(m), false, false);
            m = fmaxf(u2f(sw[0]), u2f(sw[1]));
            o = zero;
#pragma nounroll
            for (int i = i0; i < 9; ++i) {
                const f32x16 s = sc(i);
                u32 p[8];
#pragma unroll
                for (int j = 0; j < 8; ++j) p[j] = pk2(__builtin_amdgcn_exp2f(s[2 * j] - m), __builtin_amdgcn_exp2f(s[2 * j + 1] - m));
                const u32 vo_ = (i >> 1) * 5136;
                o = mfma32(asbf8(lds128(((i & 1) ? va[1][0] : va[0][0]) + vo_)), asbf8(u32x4{p[0], p[1], p[2], p[3]}), o);
                o = mfma32(asbf8(lds128(((i & 1) ? va[1][1] : va[0][1]) + vo_)), asbf8(u32x4{p[4], p[5], p[6], p[7]}), o);
            }
        }
        const float rs = __builtin_amdgcn_rcpf(o[12]) * gt;
        const u32 ob = oprod + 20480 + (l31 >> 4) * 40960;
        u32x4 v = {pk2m(o[0] * rs, o[1] * rs), pk2m(o[2] * rs, o[3] * rs), pk2m(o[4] * rs, o[5] * rs), pk2m(o[6] * rs, o[7] * rs)};
        __builtin_amdgcn_raw_buffer_store_b128(v, wsr, ob + c4 * 1024 + (2 * hh + hf) * 256 + l15 * 16, 0, 16);
        u32x2 vt = {pk2m(o[8] * rs, o[9] * rs), pk2m(o[10] * rs, o[11] * rs)};
        __builtin_amdgcn_raw_buffer_store_b64(vt, wsr, hf ? OOB : ob + 4096 + c4 * 256 + l15 * 16 + hh * 8, 0, 16);
        cfence();
    }
    STAMP(17);
    STAMPC(58);
    if (!rok) spin(f2o, rws, tag);
    if (wid < 4) {
        if (rok) wait_vm<2>(); else { recs(); wait_vm<0>(); }
        float s[8];
#pragma unroll
        for (int i = 0; i < 8; ++i) {
            const u32x4 v = lds128(L_RC + wr * 8192 + i * 1024 + l16);
#pragma unroll
            for (int k = 0; k < 4; ++k) { s[2 * k] = i ? s[2 * k] + blo(v[k]) : blo(v[k]); s[2 * k + 1] = i ? s[2 * k + 1] + bhi(v[k]) : bhi(v[k]); }
        }
        float t[4];
#pragma unroll
        for (int k = 0; k < 4; ++k) {
            auto sw = __builtin_amdgcn_permlane32_swap(f2u(s[k]), f2u(s[4 + k]), false, false);
            t[k] = u2f(sw[0]) + u2f(sw[1]);
        }
        sts64(L_REC + wr * 512 + l31 * 16 + hf * 8, u32x2{pk2(t[0], t[1]), pk2(t[2], t[3])});
    } else {
        ctl_wait(L_MC, 10);
        STAMP(23);
        const u32x4 m = lds128(L_REC + l16), n = lds128(L_REC + 1024 + l16);
        const bool m1 = l15 & 4;
        const bf16x8 a0 = asbf8(u32x4{m[0], m[1], g4 == 0 ? n[2] : 0, g4 == 0 ? n[3] : 0});
        const bf16x8 a1 = asbf8(u32x4{m[2], m[3], g4 == 1 ? n[2] : 0, g4 == 1 ? n[3] : 0});
        const bf16x8 x0 = asbf8(u32x4{m1 ? 0 : n[0], m1 ? 0 : n[1], (g4 == 2 && !m1) ? n[2] : 0, (g4 == 2 && !m1) ? n[3] : 0});
        const bf16x8 x1 = asbf8(u32x4{m1 ? n[0] : 0, m1 ? n[1] : 0, (g4 == 3 && m1) ? n[2] : 0, (g4 == 3 && m1) ? n[3] : 0});
#pragma unroll
        for (int t2 = 0; t2 < 2; ++t2) {
            const bf16x8 b0 = asbf8(u32x4{P[t2][0], P[t2][1], P[2][2 * t2], P[2][2 * t2 + 1]});
            const bf16x8 b1 = asbf8(u32x4{P[t2][2], P[t2][3], P[2][2 * t2], P[2][2 * t2 + 1]});
            const f32x4 z = {0, 0, 0, 0};
            const f32x4 xx = mfma16(x1, b1, mfma16(x0, b0, z));
            const f32x4 n0 = mfma16(a0, b0, z), n1 = mfma16(a1, b1, z);
            const u32 rc = f2u(__builtin_amdgcn_rcpf(xx[0] + 1e-6f) * (1.f - (t2 ? g1 : g0)));
            const u32 rs = __builtin_amdgcn_permlane32_swap(rc, rc, false, false)[1];
            auto sw = __builtin_amdgcn_permlane16_swap(rs, rs, false, false);
            const float r0 = u2f(sw[0]), r1 = u2f(sw[1]), rr = u2f(rs);
            const u32 so = oprod + t2 * 40960;
            st128(u32x4{pk2m(n0[0] * r0, n0[1] * r0), pk2m(n0[2] * r0, n0[3] * r0), pk2m(n1[0] * r1, n1[1] * r1), pk2m(n1[2] * r1, n1[3] * r1)}, l16, rws, so + c4 * 1024);
            __builtin_amdgcn_raw_buffer_store_b64(u32x2{pk2m(xx[0] * rr, xx[1] * rr), pk2m(xx[2] * rr, xx[3] * rr)}, wsr, hf ? OOB : l15 * 16 + g4 * 8, so + 4096 + c4 * 256, 16);
        }
        cfence();
    }
    ctl_set(me, 10);
    STAMP(24);
    {
        u32x4 O[4][5];
        u32 so[4];
#pragma unroll
        for (u32 t = 0; t < 4; ++t) so[t] = WS_O + (r64 * 4 + (wr ^ ((t + 1) & 3))) * 40960 + (wid ^ SWP) * 5120;
        const u32 lr = opaque_v(l16 + 1024);
        auto old = [&](auto T, auto E) { if constexpr (E.v < 4) ul128<E.v * 1024>(O[T.v][E.v], l16, rws, so[T.v]); else ul128<3072>(O[T.v][4], lr, rws, so[T.v]); };
        auto owait = [&](auto T, auto N) { wait5<N.v>(O[T.v]); };
        auto omax = [&](int t) { return mx4(mx4(mx4(mx4(mx4(0, O[t][0]), O[t][1]), O[t][2]), O[t][3]), O[t][4]); };
        auto slow = [&](auto T) {
            constexpr int hj = T.v + NUP < 4 ? T.v + NUP : 4;
            sfor<T.v, hj>([&](auto U) { owait(U, IC<0>{}); });
#pragma nounroll
            for (u32 it = 0; it < (1u << 17); ++it) {
                sfor<T.v, hj>([&](auto U) {
                    sfor<0, 5>([&](auto E) {
                        if (marked(mx4(0, O[U.v][E.v]))) {
                            if constexpr (E.v < 4) rl128<E.v * 1024>(O[U.v][E.v], l16, rws, so[U.v]); else rl128<3072>(O[U.v][4], lr, rws, so[U.v]);
                        }
                    });
                });
                sfor<T.v, hj>([&](auto U) { owait(U, IC<0>{}); });
                if (!marked(omax(T.v))) break;
            }
        };
        f32x4 ac[2][5], sm[5];
        const u32 grp = wid >= 4 ? L_HQ : L_MC, pb = (wid >> 2) * 61440 + l16, pr = opaque_v(pb + wr * 15360), po = opaque_v(61440 - (wid >> 2) * 61440 + wr * 15360 + l16);
        if (OLATE) ctl_wait8(10);
        SPINTO(25);
        STAMP(25);
        if (wid < 4) __builtin_amdgcn_s_setprio(TPM); else __builtin_amdgcn_s_setprio(TPH);
        sfor<0, NUP>([&](auto T) { sfor<0, 5>([&](auto E) { old(T, E); }); });
        STAMP(26);
        owait(IC<0>{}, IC<5 * (NUP - 1)>{});
        bool bad = marked(omax(0));
        STAMP(27);
        u32x4 snap;
        u32 sn1;
        sfor<0, 4>([&](auto T) {
            constexpr int t = decltype(T)::v;
            if (bad) slow(T);
            u32 mx = 0;
            sfor<0, 25>([&](auto N) {
                constexpr int n = decltype(N)::v, e = n / 5, c = n % 5;
                sched0();
                mfma16a<e == 0>(ac[t & 1][c], asbf8(W[n]), asbf8(O[t][e]));
                sched0();
                if constexpr (t > 0 && n >= 1 && n < 6) sts128(pb + (wr ^ t) * 15360 + (t - 1) * 5120 + (n - 1) * 1024, __builtin_bit_cast(u32x4, ac[(t - 1) & 1][n - 1]));
                if constexpr (t > 0 && n == 6) ctl_set(me, 10 + t);
                if constexpr (t + NUP < 4 && n >= LD0 && n < LD0 + 5 * LDS_ && (n - LD0) % LDS_ == 0) old(IC<t + NUP>{}, IC<(n - LD0) / LDS_>{});
                if constexpr (t < 3) {
                    if constexpr (n == CK0) owait(IC<t + 1>{}, IC<5 * ((t + NUP < 4 ? t + NUP : 3) - t - 1)>{});
                    if constexpr (n >= CK0 && n < CK0 + 5) { mx = mx4(mx, O[t + 1][n - CK0]); mx = opaque_v(mx); }
                } else {
                    if constexpr (n == 0) snap = ctl_get(grp);
                    if constexpr (n == 7) { if (!ctl_ge(snap, 12)) ctl_wait(grp, 12); cfence(); }
                    if constexpr (n >= 7 && n < 12) { O[0][n - 7] = lds128(pr + (n - 7) * 1024); O[1][n - 7] = lds128(pr + 5120 + (n - 7) * 1024); }
                    if constexpr (n == 12) snap = ctl_get(grp);
                    if constexpr (n >= 12 && n < 17) { sm[n - 12] = __builtin_bit_cast(f32x4, O[0][n - 12]) + __builtin_bit_cast(f32x4, O[1][n - 12]); tief(sm[n - 12]); }
                    if constexpr (n == 17) {
                        if (!ctl_ge(snap, 13)) ctl_wait(grp, 13);
                        cfence();
#pragma unroll
                        for (int i = 0; i < 5; ++i) O[0][i] = lds128(pr + 10240 + i * 1024);
                    }
                    if constexpr (n == 19) {
                        sn1 = *(volatile AS3 u32*)(me ^ 16);
                        cfence();
#pragma unroll
                        for (int i = 0; i < 5; ++i) O[1][i] = lds128(po + i * 1024);
                    }
                    if constexpr (n == 22) {
#pragma unroll
                        for (int i = 0; i < 5; ++i) { sm[i] += __builtin_bit_cast(f32x4, O[0][i]); tief(sm[i]); }
                    }
                }
                sched0();
            });
            if constexpr (t < 3) bad = marked(mx);
            if constexpr (t < 3) STAMP(t == 0 ? 28 : t == 1 ? 18 : 29);
        });
        mfma_pad();
        STAMP(19);
#pragma unroll
        for (int c = 0; c < 5; ++c) sm[c] += ac[1][c];
        bool fin = rfl(sn1) >= 14;
        if (!fin) {
#pragma unroll
            for (int c = 0; c < 5; ++c) sts128(pr + c * 1024, __builtin_bit_cast(u32x4, sm[c]));
            ctl_set(me, 14);
            sn1 = *(volatile AS3 u32*)(me ^ 16);
            cfence();
#pragma unroll
            for (int i = 0; i < 5; ++i) O[1][i] = lds128(po + i * 1024);
            fin = rfl(sn1) >= 14;
        }
        if (fin) {
#pragma unroll
            for (int c = 0; c < 5; ++c) sm[c] += __builtin_bit_cast(f32x4, O[1][c]);
            u32 r[10];
#pragma unroll
            for (int c = 0; c < 5; ++c) { r[2 * c] = pk2(sm[c][0], sm[c][1]); r[2 * c + 1] = pk2(sm[c][2], sm[c][3]); }
            const __amdgpu_buffer_rsrc_t ors = __builtin_amdgcn_make_buffer_rsrc(out, 0, 2048 * 1280, 0x00020000);
            const u32 vo = l15 * 1280 + g4 * 40 + (r64 * 64 + wr * 16) * 1280 + c80 * 160;
            __builtin_amdgcn_raw_buffer_store_b128(u32x4{r[0], r[1], r[2], r[3]}, ors, vo, 0, OAUX);
            __builtin_amdgcn_raw_buffer_store_b128(u32x4{r[4], r[5], r[6], r[7]}, ors, vo + 16, 0, OAUX);
            __builtin_amdgcn_raw_buffer_store_b64(u32x2{r[8], r[9]}, ors, vo + 32, 0, OAUX);
        }
    }
    SPINTO(22);
    STAMP(22);
    STAMPC(63);
    asm volatile(".Lcode_hi:");
}

extern "C" void kernel_launch(void* const* d_in, const int* in_sizes, int n_in, void* d_out, int out_size, void* d_ws,
                              size_t ws_size, hipStream_t stream) {
    (void)in_sizes; (void)n_in; (void)out_size; (void)ws_size;
    (void)hipFuncSetAttribute((const void*)fused_kernel, hipFuncAttributeMaxDynamicSharedMemorySize, LDS_BYTES);
    fused_kernel<<<256, 512, LDS_BYTES, stream>>>((const u16*)d_in[0], (const u16*)d_in[1], (const u16*)d_in[2], (const u16*)d_in[4],
                                                  (const u16*)d_in[5], (const u16*)d_in[6], (u16*)d_out, (char*)d_ws);
}
```

```cpp
#include <hip/hip_runtime.h>
#include <stdint.h>
#pragma clang diagnostic ignored "-Wint-to-pointer-cast"

#define SPINS 1
#define PRIO 3
typedef unsigned short u16;
typedef unsigned int u32;
typedef unsigned long long u64;
typedef __attribute__((ext_vector_type(8))) __bf16 bf16x8;
typedef __attribute__((ext_vector_type(4))) __bf16 bf16x4;
typedef __attribute__((ext_vector_type(2))) __bf16 bf16x2;
typedef __attribute__((ext_vector_type(2))) float f32x2;
typedef __attribute__((ext_vector_type(4))) float f32x4;
typedef __attribute__((ext_vector_type(16))) float f32x16;
typedef __attribute__((ext_vector_type(4))) u32 u32x4;
typedef __attribute__((ext_vector_type(2))) u32 u32x2;
typedef __attribute__((ext_vector_type(16))) u32 u32x16;
typedef __attribute__((ext_vector_type(4))) short s16x4;
#define AS3 __attribute__((address_space(3)))
#define DEVI __device__ __forceinline__

constexpr u32 WS_E   = 0;
constexpr u32 WS_FS  = 4096;
constexpr u32 WS_F2  = 12288;
constexpr u32 WS_FQ  = 8192;
constexpr u32 WS_FT  = 16384;
constexpr u32 WS_T   = 81920;
constexpr u32 WS_KVP = WS_T + 2 * 16 * 64 * 2560;
constexpr u32 WS_O   = WS_KVP + 16 * 16 * 2048;
constexpr u32 WS_WO  = WS_O + 128 * 40960;
constexpr u32 WS_WQ  = WS_WO + 16 * 102400;
constexpr u32 WS_FM  = WS_WQ + 16 * 102400;
constexpr u32 WS_END = WS_FM + 4096;
constexpr u32 OOB = 0x80000000u;

constexpr u32 WREG  = 5152;
constexpr u32 XO    = 20608;
constexpr u32 SLOT  = 36992;
__device__ constexpr u32 SL[4] = {0, 3 * SLOT, 2 * SLOT, SLOT};
constexpr u32 L_SCR = 4 * SLOT;
constexpr u32 L_GW  = L_SCR + 8192;
constexpr u32 L_DUMP = 163328;
constexpr u32 L_REC = 0;
constexpr u32 L_XQ  = 2048;
constexpr u32 L_QT  = 3 * SLOT;
constexpr u32 L_K   = 101376;
constexpr u32 L_V   = 132096;
constexpr u32 L_RC  = 8192;
constexpr u32 L_HQ  = 162944, L_MC = L_HQ + 16, L_TDW = L_HQ + 32, L_TAG = L_HQ + 48;
constexpr u32 LDS_BYTES = 163840;
static_assert(L_GW + 2048 <= L_HQ && L_DUMP >= L_TAG + 4 && L_DUMP + 256 <= LDS_BYTES && L_V + 6 * 5136 <= L_HQ && L_XQ + 6144 <= L_QT - 102400, "lds");

constexpr float LOG2E = 1.4426950408889634f;
constexpr float QSCALE = 0.22360679774997896f * 1.4426950408889634f;
constexpr u32 TAGC = 0x7F4A7C15u;
constexpr u32 ONES = 0x3F803F80u;

DEVI u32 pk2(float a, float b) { f32x2 v = {a, b}; bf16x2 r = __builtin_convertvector(v, bf16x2); return __builtin_bit_cast(u32, r); }
DEVI u32 f2u(float f) { return __builtin_bit_cast(u32, f); }
DEVI float u2f(u32 u) { return __builtin_bit_cast(float, u); }
DEVI float blo(u32 u) { return u2f(u << 16); }
DEVI float bhi(u32 u) { return u2f(u & 0xFFFF0000u); }
DEVI u32 umin(u32 a, u32 b) { return a < b ? a : b; }
DEVI u32 umax(u32 a, u32 b) { return a > b ? a : b; }
DEVI u32 pk2m(float a, float b) { return umin(pk2(a, b), 0xFFFFFFFEu); }
DEVI bf16x8 asbf8(u32x4 v) { return __builtin_bit_cast(bf16x8, v); }
DEVI f32x4 mfma16(bf16x8 a, bf16x8 b, f32x4 c) { return __builtin_amdgcn_mfma_f32_16x16x32_bf16(a, b, c, 0, 0, 0); }
template <bool FIRST = false> DEVI void mfma16a(f32x4& c, bf16x8 a, bf16x8 b) {
    if constexpr (FIRST) asm volatile("v_mfma_f32_16x16x32_bf16 %0, %1, %2, 0" : "=&v"(c) : "v"(a), "v"(b));
    else asm volatile("v_mfma_f32_16x16x32_bf16 %0, %1, %2, %0" : "+v"(c) : "v"(a), "v"(b));
}
template <bool FIRST> DEVI void mfma16b(f32x4& c, bf16x8 a, bf16x8 b) {
    if constexpr (FIRST) asm volatile("v_mfma_f32_16x16x32_bf16 %0, %1, %2, 0" : "+v"(c) : "v"(a), "v"(b));
    else asm volatile("v_mfma_f32_16x16x32_bf16 %0, %1, %2, %0" : "+v"(c) : "v"(a), "v"(b));
}
DEVI void mfma_pad() { __builtin_amdgcn_sched_barrier(0); asm volatile("s_nop 7\n\ts_nop 3" ::: "memory"); __builtin_amdgcn_sched_barrier(0); }
DEVI f32x16 mfma32(bf16x8 a, bf16x8 b, f32x16 c) { return __builtin_amdgcn_mfma_f32_32x32x16_bf16(a, b, c, 0, 0, 0); }
DEVI f32x16 mfma32k8(bf16x4 a, bf16x4 b, f32x16 c) {
    return __builtin_amdgcn_mfma_f32_32x32x8bf16_1k(__builtin_bit_cast(s16x4, a), __builtin_bit_cast(s16x4, b), c, 0, 0, 0);
}
DEVI bf16x4 ldtr(u32 a) { return __builtin_amdgcn_ds_read_tr16_b64_v4bf16((AS3 bf16x4*)a); }
DEVI bf16x8 cat8(bf16x4 x, bf16x4 y) { return __builtin_shufflevector(x, y, 0, 1, 2, 3, 4, 5, 6, 7); }
DEVI bf16x8 ldtr2(u32 a, u32 b) { bf16x4 x = ldtr(a), y = ldtr(b); return cat8(x, y); }
DEVI u32x4 lds128(u32 a) { return *(AS3 u32x4*)a; }
DEVI u32x2 lds64(u32 a) { return *(AS3 u32x2*)a; }
DEVI u32 lds32(u32 a) { return *(AS3 u32*)a; }
DEVI void sts128(u32 a, u32x4 v) { *(AS3 u32x4*)a = v; }
DEVI void sts64(u32 a, u32x2 v) { *(AS3 u32x2*)a = v; }
DEVI void sts32(u32 a, u32 v) { *(AS3 u32*)a = v; }
DEVI u32 rfl(u32 v) { return __builtin_amdgcn_readfirstlane(v); }
DEVI u32 opaque_s(u32 v) { asm volatile("" : "+s"(v)); return v; }
DEVI u32 opaque_v(u32 v) { asm volatile("" : "+v"(v)); return v; }
DEVI void undef(u32x4& v) { asm volatile("" : "=v"(v)); }
DEVI void cfence() { asm volatile("" ::: "memory"); }

DEVI u32x4 mkrs(const void* p, u32 bytes) { return u32x4{(u32)(uintptr_t)p, (u32)((uintptr_t)p >> 32) & 0xFFFFu, bytes, 0x00020000u}; }
template <int MO, int OFF = 0> DEVI void dma(u32 voff, u32x4 rs, u32 soff, u32 mb) {
    asm volatile("s_add_u32 m0, %3, %5\n\ts_nop 0\n\tbuffer_load_dwordx4 %0, %1, %2 offen offset:%4 lds"
                 :: "v"(voff), "s"(rs), "s"(soff), "s"(mb), "i"(OFF), "i"(MO) : "memory", "scc");
}
template <int MO, int OFF = 0> DEVI void dmac(u32 voff, u32x4 rs, u32 soff, u32 mb) {
    asm volatile("s_add_u32 m0, %3, %5\n\ts_nop 0\n\tbuffer_load_dwordx4 %0, %1, %2 offen offset:%4 sc1 lds"
                 :: "v"(voff), "s"(rs), "s"(soff), "s"(mb), "i"(OFF), "i"(MO) : "memory", "scc");
}
DEVI void pref(u32 voff, u32x4 rs, u32 soff) {
    asm volatile("s_mov_b32 m0, %3\n\ts_nop 0\n\tbuffer_load_dword %0, %1, %2 offen sc1 lds" :: "v"(voff), "s"(rs), "s"(soff), "i"(L_DUMP) : "memory");
}
DEVI u32 ldflag(u32 voff, u32x4 rs) {
    u32 v;
    asm volatile("buffer_load_dword %0, %1, %2, 0 offen sc1" : "=v"(v) : "v"(voff), "s"(rs) : "memory");
    return v;
}
DEVI u32x4 ldflag4(u32 voff, u32x4 rs) {
    u32x4 v;
    asm volatile("buffer_load_dwordx4 %0, %1, %2, 0 offen sc1" : "=v"(v) : "v"(voff), "s"(rs) : "memory");
    return v;
}
DEVI u64 ldflag2(u32 voff, u32x4 rs) {
    u64 v;
    asm volatile("buffer_load_dwordx2 %0, %1, %2, 0 offen sc1" : "=v"(v) : "v"(voff), "s"(rs) : "memory");
    return v;
}
DEVI u32 ldflags(u32 voff, u32x4 rs, u32 soff) {
    u32 v;
    asm volatile("buffer_load_dword %0, %1, %2, %3 offen sc1" : "=v"(v) : "v"(voff), "s"(rs), "s"(soff) : "memory");
    return v;
}
DEVI u32 undef1() { u32 v; asm volatile("" : "=v"(v)); return v; }
DEVI u32 pkneg(u32 a, u32 b) {
    u32 r;
    asm volatile("v_pk_sub_i16 %0, %1, %2\n\tv_pk_ashrrev_i16 %0, 15, %0 op_sel_hi:[0,1]" : "=v"(r) : "s"(a), "v"(b));
    return r;
}
DEVI void stflag(u32 val, u32 soff, u32x4 rs) {
    asm volatile("s_mov_b64 exec, 1\n\tbuffer_store_dword %0, off, %1, %2 sc1\n\ts_mov_b64 exec, -1" :: "v"(val), "s"(rs), "s"(soff) : "memory");
}
DEVI void stflagn(u32 val, u32 soff, u32x4 rs) {
    asm volatile("s_mov_b64 exec, 1\n\ts_nop 4\n\tbuffer_store_dword %0, off, %1, %2 sc1\n\ts_mov_b64 exec, -1" :: "v"(val), "s"(rs), "s"(soff) : "memory");
}
template <int OFF = 0> DEVI void st128(u32x4 v, u32 voff, u32x4 rs, u32 soff) {
    asm volatile("buffer_store_dwordx4 %0, %1, %2, %3 offen offset:%4 sc1\n\ts_nop 1" :: "v"(v), "v"(voff), "s"(rs), "s"(soff), "i"(OFF) : "memory");
}
template <int OFF = 0> DEVI void st128n(u32x4 v, u32 voff, u32x4 rs, u32 soff) {
    asm volatile("buffer_store_dwordx4 %0, %1, %2, %3 offen offset:%4\n\ts_nop 1" :: "v"(v), "v"(voff), "s"(rs), "s"(soff), "i"(OFF) : "memory");
}
template <int N> DEVI void wait_vm() { asm volatile("s_waitcnt vmcnt(%0)" ::"i"(N) : "memory"); }
template <int N> DEVI u32 wait_rfl(u32 v) { u32 r; asm volatile("s_waitcnt vmcnt(%2)\n\tv_readfirstlane_b32 %0, %1" : "=s"(r) : "v"(v), "i"(N) : "memory"); return r; }
template <int N> DEVI bool wait_eq(u32 v, u32 tag) {
    u64 m; asm volatile("s_waitcnt vmcnt(%3)\n\tv_cmp_ne_u32_e64 %0, %2, %1" : "=s"(m) : "v"(v), "s"(tag), "i"(N) : "memory");
    return m == 0ull;
}
template <int N> DEVI bool wait_eq2(u64 v, u32 tag) {
    u64 m; asm volatile("s_waitcnt vmcnt(%3)\n\tv_cmp_ne_u64_e64 %0, %2, %1" : "=s"(m) : "v"(v), "s"(((u64)tag << 32) | tag), "i"(N) : "memory");
    return m == 0ull;
}
template <int N> DEVI bool wait_eq4(u32x4 v, u32 tag) {
    asm volatile("s_waitcnt vmcnt(%1)" : "+v"(v) : "i"(N) : "memory");
    return __builtin_amdgcn_ballot_w64(((v[0] ^ tag) | (v[1] ^ tag) | (v[2] ^ tag) | (v[3] ^ tag)) != 0u) == 0ull;
}
template <int N> DEVI void wait_lgkm() { asm volatile("s_waitcnt lgkmcnt(%0)" ::"i"(N) : "memory"); }
DEVI void bar() { asm volatile("s_barrier" ::: "memory"); }
DEVI void sched0() { __builtin_amdgcn_sched_barrier(0); }
DEVI void spin(u32 voff, u32x4 rs, u32 tag) {
#pragma nounroll
    for (u32 i = 0; i < (1u << 17); ++i) {
        u32 v = ldflag(voff, rs);
        if (wait_eq<0>(v, tag)) break;
        __builtin_amdgcn_s_sleep(SPINS);
    }
}
DEVI void ctl_set(u32 a, u32 v) { asm volatile("s_mov_b64 exec, 1\n\tds_write_b32 %0, %1\n\ts_mov_b64 exec, -1" :: "v"(a), "v"(v) : "memory"); }
DEVI u32x4 ctl_get(u32 a) { return *(volatile AS3 u32x4*)a; }
DEVI bool ctl_ge(u32x4 v, u32 t) { return rfl(umin(umin(v[0], v[1]), umin(v[2], v[3]))) >= t; }
DEVI void ctl_wait(u32 a, u32 t) {
#pragma nounroll
    for (u32 i = 0; i < (1u << 20); ++i) { if (ctl_ge(ctl_get(a), t)) break; __builtin_amdgcn_s_sleep(1); }
    cfence();
}
DEVI void ctl_wait8(u32 t) {
#pragma nounroll
    for (u32 i = 0; i < (1u << 20); ++i) {
        const u32x4 x = ctl_get(L_HQ), y = ctl_get(L_MC);
        if (ctl_ge(u32x4{umin(x[0], y[0]), umin(x[1], y[1]), umin(x[2], y[2]), umin(x[3], y[3])}, t)) break;
        __builtin_amdgcn_s_sleep(1);
    }
    cfence();
}
DEVI void ctl_wait1(u32 a, u32 t) {
#pragma nounroll
    for (u32 i = 0; i < (1u << 20); ++i) { if (rfl(*(volatile AS3 u32*)a) >= t) break; __builtin_amdgcn_s_sleep(1); }
    cfence();
}
DEVI bool marked(u32 m) { return __builtin_amdgcn_ballot_w64(m == 0xFFFFFFFFu) != 0ull; }
DEVI u32 mx4(u32 m, u32x4 v) { return umax(umax(umax(m, v[0]), v[1]), umax(v[2], v[3])); }

DEVI float phi(float t) { return __builtin_amdgcn_fmed3f(t + 1.f, __builtin_amdgcn_exp2f(t * LOG2E), 1.f) + 1e-6f; }
DEVI u32 phi2(float a, float b) { return pk2(phi(a), phi(b)); }
DEVI u32x4 phi8(f32x4 a, f32x4 b) {
    const f32x2 t[4] = {f32x2{a[0], a[1]}, f32x2{a[2], a[3]}, f32x2{b[0], b[1]}, f32x2{b[2], b[3]}};
    f32x2 m[4], s[4];
    u32x4 r;
    __builtin_amdgcn_sched_barrier(0);
#pragma unroll
    for (int i = 0; i < 4; ++i) m[i] = t[i] * LOG2E;
    __builtin_amdgcn_sched_barrier(0);
#pragma unroll
    for (int i = 0; i < 4; ++i) { m[i] = f32x2{__builtin_amdgcn_exp2f(m[i][0]), __builtin_amdgcn_exp2f(m[i][1])}; __builtin_amdgcn_sched_barrier(0); }
#pragma unroll
    for (int i = 0; i < 4; ++i) s[i] = t[i] + 1.f;
    __builtin_amdgcn_sched_barrier(0);
#pragma unroll
    for (int i = 0; i < 4; ++i) { s[i] = f32x2{__builtin_amdgcn_fmed3f(s[i][0], m[i][0], 1.f), __builtin_amdgcn_fmed3f(s[i][1], m[i][1], 1.f)}; __builtin_amdgcn_sched_barrier(0); }
#pragma unroll
    for (int i = 0; i < 4; ++i) { s[i] = s[i] + 1e-6f; __builtin_amdgcn_sched_barrier(0); }
#pragma unroll
    for (int i = 0; i < 4; ++i) { r[i] = pk2(s[i][0], s[i][1]); __builtin_amdgcn_sched_barrier(0); }
    return r;
}
template <int OFF> DEVI void ul128(u32x4& v, u32 voff, u32x4 rs, u32 soff) {
    asm volatile("buffer_load_dwordx4 %0, %1, %2, %3 offen offset:%4 sc1" : "=v"(v) : "v"(voff), "s"(rs), "s"(soff), "i"(OFF) : "memory");
}
template <int OFF> DEVI void rl128(u32x4& v, u32 voff, u32x4 rs, u32 soff) {
    asm volatile("buffer_load_dwordx4 %0, %1, %2, %3 offen offset:%4 sc1" : "+v"(v) : "v"(voff), "s"(rs), "s"(soff), "i"(OFF) : "memory");
}
template <int OFF> DEVI void rl128x(u32x4& v, u32 voff, u32x4 rs, u32 soff, u64 mask) {
    asm volatile("s_mov_b64 exec, %5\n\tbuffer_load_dwordx4 %0, %1, %2, %3 offen offset:%4 sc1\n\ts_mov_b64 exec, -1"
                 : "+v"(v) : "v"(voff), "s"(rs), "s"(soff), "i"(OFF), "s"(mask) : "memory");
}
#define WSC " sc1"
template <int OFF> DEVI void uw128(u32x4& v, u32 voff, u32x4 rs, u32 soff) {
    asm volatile("buffer_load_dwordx4 %0, %1, %2, %3 offen offset:%4" WSC : "=v"(v) : "v"(voff), "s"(rs), "s"(soff), "i"(OFF) : "memory");
}
template <int OFF> DEVI void rw128(u32x4& v, u32 voff, u32x4 rs, u32 soff) {
    asm volatile("buffer_load_dwordx4 %0, %1, %2, %3 offen offset:%4" WSC : "+v"(v) : "v"(voff), "s"(rs), "s"(soff), "i"(OFF) : "memory");
}
DEVI u32x4 ld128(u32 voff, u32x4 rs, u32 soff) {
    u32x4 v;
    asm volatile("buffer_load_dwordx4 %0, %1, %2, %3 offen" : "=v"(v) : "v"(voff), "s"(rs), "s"(soff) : "memory");
    return v;
}
template <int OFF> DEVI void rl64(u32x2& v, u32 voff, u32x4 rs, u32 soff) {
    asm volatile("buffer_load_dwordx2 %0, %1, %2, %3 offen offset:%4 sc1" : "+v"(v) : "v"(voff), "s"(rs), "s"(soff), "i"(OFF) : "memory");
}
template <int N> DEVI void wait5(u32x4 (&o)[5]) {
    asm volatile("s_waitcnt vmcnt(%5)" : "+v"(o[0]), "+v"(o[1]), "+v"(o[2]), "+v"(o[3]), "+v"(o[4]) : "i"(N) : "memory");
}
DEVI void tief(f32x4& v) { asm volatile("" : "+v"(v)); }

#define STAMP(i)
#define STAMPC(i)

#define SPINTO(k)
template <int N> struct IC { static constexpr int v = N; };
template <int I, int N, class F> DEVI void sfor(F&& f) { if constexpr (I < N) { f(IC<I>{}); sfor<I + 1, N>(f); } }

#define ROTA 1
#define ROTB 3
#define UPTO {2, 3, 4, 5, 6, 7, 8, 9, 10, 10, 10}
#define HK1 2
#define HK2 4
#define HA 4
#define HB 6
#define XPM 0
#define HPRIO 3
#define QPRIO 0
#define PFQ -1
#define PFO 0
#define OLATE 1
#define FQS 1
#define CPF 0
#define ASM 0
#define FSB 6
#define HWN 200
#define HWS 1
#define OT 1
#define PFW 0
#define WU 0
#define OAUX 0
#define MKS -1
#define SWP 4
#define SWH 6
#define NUP 2
#define LD0 1
#define LDS_ 2
#define CK0 18
#define TPM 1
#define TPH 0
#define W1L 0
#define QD0 10
#define QDS 2
#define MKA 1
#define EFC 0
constexpr int qcnt(int n) { return QD0 < 0 ? 10 : n < QD0 ? 0 : (n - QD0) / QDS + 1 > 10 ? 10 : (n - QD0) / QDS + 1; }
#define WA 18
#define OTW -1
#define XNOW 0
#define QS 8
#define JBAL 1
#define JQ -1
#define JW 0
#define FD 8
#define KO 1
#define EB 0
#define UX 0
#define JD 0
#define JE 0
#define JST 18
#define FSS 4
#define ED 0
#define F4 1
#define MK1 1
#define WM0 1
#define PH8 3
#define TD 6
#define AP0 0
#define APS 3
#define TDP 1
#define TD0 0
#define IL 0
#define W2 0
#define MK2 2
#define MF 4
#define HM 8
#define HS 1
#define AE 2
#define KS 6
#define APH 0
#define APM 0
#define HWP 0
#define DUP 0
#define XM 1
#define QB 1
constexpr u64 lem(int k) { u64 m = 0; for (int l = 0; l < 64; ++l) if (k + 4 * (l >> 5) <= (l & 31)) m |= 1ull << l; return m; }
__device__ constexpr int UP[11] = UPTO;
__global__ __launch_bounds__(512, 2) void fused_kernel(const u16*, const u16*, const u16*, const u16*, const u16*, const u16*, u16*, char*) {
    const u32 lane = threadIdx.x & 63;
    const u32 wid = rfl(threadIdx.x >> 6);
    const u32 b = blockIdx.x, xcd = b & 7, idx = b >> 3;
    STAMP(0);
    STAMPC(62);
    u32x16 arg;
    asm volatile("s_load_dwordx16 %0, %1, 0" : "=s"(arg) : "s"(__builtin_amdgcn_kernarg_segment_ptr()));
    const u16 *x, *wa, *woa, *wb, *wob, *gw;
    u16* out;
    char* ws;
    u32x4 rws;
    auto args = [&](bool again) __attribute__((always_inline)) {
        if (again) asm volatile("s_load_dwordx16 %0, %1, 0\n\ts_waitcnt lgkmcnt(0)" : "=&s"(arg) : "s"(__builtin_amdgcn_kernarg_segment_ptr()));
        else asm volatile("s_waitcnt lgkmcnt(0)" : "+s"(arg));
        auto P = [&](int i) { return (u16*)(((u64)arg[2 * i + 1] << 32) | arg[2 * i]); };
        x = P(0); wa = P(1); woa = P(2); wb = P(3); wob = P(4); gw = P(5); out = P(6); ws = (char*)P(7);
        rws = mkrs(ws, WS_END);
    };
    if (wid == 3) {
        u64 pc; u32 len;
        asm volatile("s_getpc_b64 %0\n.Lcode_lo:\n\ts_mov_b32 %1, .Lcode_hi-.Lcode_lo\n\ts_max_u32 %1, %1, .Lcode_hm-.Lcode_lo" : "=s"(pc), "=s"(len) :: "scc");
        u32 o = (idx & 15) * 4096 + lane * 64;
        o = o < len ? o : 0;
        asm volatile("s_mov_b32 m0, %2\n\ts_nop 0\n\tglobal_load_lds_dword %0, %1" :: "v"(o), "s"(pc), "i"(L_DUMP) : "memory");
    }
    const u32 rb = (xcd & 1) * 8 + (idx >> 2), c4 = idx & 3, sg = xcd >> 1;
    const u32 ty = c4 >> 1;
    const u32 hg = ty ? 2 * sg + (c4 & 1) : sg + 4 * (c4 & 1);
    const u32 hp = 4 * sg + c4;
    const u32 wr = wid & 3, wq = wid ^ (wid >= 4 ? SWH : SWP);
    const u32 l15 = lane & 15, g4 = lane >> 4, l31 = lane & 31, hf = lane >> 5, l16 = lane * 16;
    const u32 me = (wid >= 4 ? L_HQ : L_MC) + wr * 4;
    const u32 oprod = WS_O + (rb * 8 + 2 * wr) * 40960 + sg * 5120;
    u32x4 qf; u32x2 qr; float gt;
    u32x4 P[3]; float g0, g1;
    u32 tag;
    const u32 r64 = xcd * 4 + (idx >> 3), c80 = idx & 7;
    u32 f2o;
    u32x4 W[25];
    bool wok;
    const u32 fso = WS_FS + (lane >> 2) * 256 + (lane & 3) * 16;
    auto wld = [&](auto I, u32 vw) __attribute__((always_inline)) { uw128<(I.v & 3) * 1024>(W[I.v], vw, rws, WS_WO + c80 * 204800 + wq * 25600 + (I.v >> 2) * 4096); };
    auto wlate = [&]() __attribute__((always_inline)) {
        if (!wok) {
#pragma nounroll
            for (u32 i = 0; i < (1u << 17); ++i) { if (wait_eq4<0>(ldflag4(fso, rws), tag)) break; __builtin_amdgcn_s_sleep(SPINS); }
            sfor<0, WA>([&](auto I) { rw128<(I.v & 3) * 1024>(W[I.v], l16, rws, WS_WO + c80 * 204800 + wq * 25600 + (I.v >> 2) * 4096); });
        }
    };
    u32 ka, kc, kr, va[2][2], mk[8];
    auto masks = [&]() __attribute__((always_inline)) {
        const u32 ln = opaque_v(lane), q2 = ((ln & 31) - 4 * (ln >> 5) + 8) * 0x10001u;
        sfor<0, 8>([&](auto J) {
            constexpr u32 k0 = ((2 * J.v) & 3) + 8 * ((2 * J.v) >> 2);
            mk[J.v] = pkneg((k0 + 7) | ((k0 + 8) << 16), q2);
        });
    };
    auto asetup = [&](u32 hh) __attribute__((always_inline)) {
        const u32 ch = 2 * (g4 & 1) + (lane & 1), kb = L_K + wr * 2560 + ((lane >> 1) & 1) * 8;
        ka = kb + hh * 1024 + ch * 256 + (((8 * hf + (l15 >> 2)) ^ (4 * ch)) * 16); kc = ka ^ 64;
        kr = kb + 2048 + ch * 128 + ((hh ^ (ch >> 1)) * 64) + (l15 >> 2) * 16;
#pragma unroll
        for (int p = 0; p < 2; ++p)
#pragma unroll
            for (int m = 0; m < 2; ++m) {
                const u32 bi = wr + p, c = 2 * m + hf;
                va[p][m] = L_V + (bi >> 1) * 5136 + (l31 < 20 ? (bi & 1) * 2560 + (l31 < 16 ? hh * 1024 + c * 256 + ((l31 ^ (4 * c)) * 16) : 2048 + c * 128 + ((hh ^ m) * 64) + (l31 - 16) * 16) : 5120);
            }
        if constexpr (HS) masks();
        else sfor<0, 8>([&](auto J) {
            constexpr int k0 = ((2 * J.v) & 3) + 8 * ((2 * J.v) >> 2);
            mk[J.v] = (__builtin_amdgcn_inverse_ballot_w64(lem(k0)) ? 0xFFFFu : 0u) | (__builtin_amdgcn_inverse_ballot_w64(lem(k0 + 1)) ? 0xFFFF0000u : 0u);
        });
        f2o = WS_F2 + ((lane & 16) ? (r64 >> 1) * 256 + l15 * (F4 ? 16 : 4) : l15 * 256 + ((hp & 12) + 2 + ((hp >> 1) & 1)) * (F4 ? 16 : 4)) + (F4 ? hf * 8 : 0);
        if constexpr (MK2) f2o = WS_F2 + (lane >> 2) * 256 + ((hp & 12) + 2 + ((hp >> 1) & 1)) * 16 + (lane & 3) * 4;
        asm volatile("" : "+v"(ka), "+v"(kc), "+v"(kr), "+v"(va[0][0]), "+v"(va[0][1]), "+v"(va[1][0]), "+v"(va[1][1]), "+v"(f2o), "+s"(out));
        asm volatile("" : "+v"(mk[0]), "+v"(mk[1]), "+v"(mk[2]), "+v"(mk[3]), "+v"(mk[4]), "+v"(mk[5]), "+v"(mk[6]), "+v"(mk[7]));
    };
    auto apart = [&](auto I) __attribute__((always_inline)) {
        constexpr int i = I.v;
        const u32 ln = opaque_v(lane), a15 = ln & 15, a31 = ln & 31, ah = ln >> 5;
        if constexpr (i < 2) {
            const u32 ch = 2 * ((ln >> 4) & 1) + (ln & 1), kb = L_K + wr * 2560 + ((ln >> 1) & 1) * 8;
            if constexpr (i == 0) { ka = kb + ch * 256 + (((8 * ah + (a15 >> 2)) ^ (4 * ch)) * 16); asm volatile("" : "+v"(ka)); if constexpr (!HS) { kc = ka ^ 64; asm volatile("" : "+v"(kc)); } }
            else { kr = kb + 2048 + ch * 128 + ((ch >> 1) * 64) + (a15 >> 2) * 16; asm volatile("" : "+v"(kr)); }
        } else if constexpr (i < 6) {
            constexpr int p = (i - 2) >> 1, m = (i - 2) & 1;
            const u32 bi = wr + p, c = 2 * m + ah;
            va[p][m] = L_V + (bi >> 1) * 5136 + (a31 < 20 ? (bi & 1) * 2560 + (a31 < 16 ? c * 256 + ((a31 ^ (4 * c)) * 16) : 2048 + c * 128 + m * 64 + (a31 - 16) * 16) : 5120);
            asm volatile("" : "+v"(va[p][m]));
        } else if constexpr (i == 6) {
            f2o = WS_F2 + ((ln & 16) ? (r64 >> 1) * 256 + a15 * (F4 ? 16 : 4) : a15 * 256 + ((hp & 12) + 2 + ((hp >> 1) & 1)) * (F4 ? 16 : 4)) + (F4 ? ah * 8 : 0);
            asm volatile("" : "+v"(f2o), "+s"(out));
        } else {
            constexpr int j = i - 7, k0 = ((2 * j) & 3) + 8 * ((2 * j) >> 2);
            mk[j] = (__builtin_amdgcn_inverse_ballot_w64(lem(k0)) ? 0xFFFFu : 0u) | (__builtin_amdgcn_inverse_ballot_w64(lem(k0 + 1)) ? 0xFFFF0000u : 0u);
            asm volatile("" : "+v"(mk[j]));
        }
    };

    if (__builtin_expect(wid >= 4, 1)) {
        const u32 rot = (ROTA * rb + ROTB * c4) % 10;
        auto kbof = [&](u32 s) { u32 k = rot + s; return k >= 10 ? k - 10 : k; };
        u32x4 rsx, rsw;
        const u32 vx = opaque_v((rb * 128 + 32 * wr + (lane >> 3)) * 1280 + (((lane & 7) ^ ((lane >> 3) & 6)) * 16));
        u32 vo[5];
        auto vcalc = [&]() __attribute__((always_inline)) {
        const u32 cb0 = opaque_s(1280 + (ty ? 160 : 320) * hg), cb1 = opaque_s(cb0 + ty * 1120);
#pragma unroll
        for (int i = 0; i < 5; ++i) {
            const u32 ch = 64 * i + lane, pr = (ch * 205) >> 12, j = ch - pr * 20;
            const u32 k = 32 * (wr >> 1) + 8 * (wr & 1) + (pr & 7) + 2 * (pr & 8);
            vo[i] = k * 3840 + (j >= 10 ? cb1 : cb0) + j * 16;
        }
        asm volatile("" : "+v"(vo[0]), "+v"(vo[1]), "+v"(vo[2]), "+v"(vo[3]), "+v"(vo[4]));
        };
        auto issuep = [&](auto SC, auto A, auto B) {
            constexpr int s = decltype(SC)::v;
            const u32 kb = kbof(s);
            sfor<A.v, B.v>([&](auto I) {
                if constexpr (I.v < 4) dma<SL[s % 4] + XO + I.v * 1024>(vx, rsx, kb * 128 + I.v * 10240, wr * 4096);
                else dma<SL[s % 4] + (I.v - 4) * 1024>(vo[I.v - 4], rsw, kb * 245760, wr * WREG);
            });
        };
        auto issue = [&](auto SC) { issuep(SC, IC<0>{}, IC<9>{}); };
        if constexpr (!KO) vcalc();
        args(false);
        rsx = mkrs(x, 2048 * 1280); rsw = mkrs(ty ? wb : wa, 640 * 3840);
        STAMP(30);
        issuep(IC<0>{}, IC<0>{}, IC<4>{});
        if constexpr (KO) vcalc();
        issuep(IC<0>{}, IC<4>{}, IC<9>{});
        STAMP(31);
        {
            const u32 o = l16 + (wr & 1) * 1024 + rot * 128;
            dma<L_GW>(o >= 1280 ? o - 1280 : o, mkrs(gw, 1280), 0, (wr & 1) * 1024);
        }
        if constexpr (EB) bar();
        sfor<1, UP[0]>(issue);
        if constexpr (UX) issuep(IC<UP[0]>{}, IC<0>{}, IC<HA>{});
        STAMP(1);
        const u32 xb0 = XO + (32 * wr + l15) * 128 + ((g4 ^ (l15 & 6)) * 16), xb1 = xb0 ^ 64;
        const u32 xc0 = xb0 + 2 * SLOT, xc1 = xb1 + 2 * SLOT, ga = L_GW + g4 * 16;
        const u32 qm0 = (wr & 1) * 5120 + (1 - (wr >> 1)) * 10240, qs0 = WS_WQ + hp * 102400 + (wr & 1) * 5120;
        auto qissue = [&](auto JC) {
            constexpr int jj = decltype(JC)::v;
            const u32 so = qs0 + kbof(2 * jj + (wr >> 1)) * 10240;
            sfor<0, 4>([&](auto I) { dmac<L_QT - 20480 * (jj + 1), I.v * 1024>(l16, rws, so, qm0); });
            dmac<L_QT - 20480 * (jj + 1) + 4096>(l16, rws, so + 4096, qm0);
        };
        auto qone = [&](auto KC) {
            constexpr int jj = 3 + decltype(KC)::v / 5, i = decltype(KC)::v % 5;
            const u32 so = qs0 + kbof(2 * jj + (wr >> 1)) * 10240;
            if constexpr (i < 4) dmac<L_QT - 20480 * (jj + 1), i * 1024>(l16, rws, so, qm0);
            else dmac<L_QT - 20480 * (jj + 1) + 4096>(l16, rws, so + 4096, qm0);
        };
        const u32 fqo = WS_FQ + sg * 1024 + g4 * 256 + l15 * 4;
        bf16x8 xf[10][2][2];
        f32x4 gacc[2];
        u32 pv = 0;
        bool qok = true;
        constexpr int XA[10] = {0, 0, 0, 0, 0, 0, QS == 7 || QB, QS == 8 && !QB, 0, 0};
        auto exf = [&](int s) { int j = 0; while (UP[j] <= s) ++j; int e = 0; for (; j < s; ++j) e += XA[j]; return e; };
        sfor<0, 10>([&](auto SC) {
            constexpr int s = decltype(SC)::v;
            wait_lgkm<0>();
            constexpr int ex = exf(s) + (s == 0 && UX ? HA : 0);
            static_assert(!UX || UP[1] > UP[0], "early part");
            static_assert(UP[QS] == 10 && UP[QS - 1] == 9, "schedule");
            if constexpr (s <= QS - (QB > 0)) wait_vm<9 * (UP[s] - s - 1) + ex>();
            if constexpr (s == 8 && QB) { if (qok) wait_vm<14>(); else wait_vm<9>(); }
            if constexpr (s == 9) { if (qok) wait_vm<QS == 7 || QB == 1 ? 15 : 10>(); else wait_vm<0>(); }
            bar();
            if constexpr (s == 0) __builtin_amdgcn_s_setprio(HPRIO);
            if constexpr (s == 0) SPINTO(2);
            if constexpr (s == 4) SPINTO(6);
            STAMP(2 + s);
            if constexpr (s == 0 || s == 2 || s == 4 || s == 7) STAMPC(s == 0 ? 51 : s == 2 ? 52 : s == 4 ? 53 : 54);
            constexpr bool up = SL[s % 4] >= 2 * SLOT, any = UP[s + 1] > UP[s];
            constexpr u32 xo = SL[s % 4] - (up ? 2 * SLOT : 0);
            const u32 a0 = up ? xc0 : xb0, a1 = up ? xc1 : xb1;
            if constexpr (s == 5) tag = rfl(lds32(L_TAG));
            if constexpr (s == QS - 1 - (QB > 0)) pv = ldflag(fqo, rws);
            if constexpr (s == QS && !QB) { qok = wait_eq<9>(pv, tag); if (qok) qissue(IC<0>{}); }
            if constexpr (s == 7 && QB) qok = wait_eq<9>(pv, tag);
            if constexpr (s == 8 && (QS == 7 || QB)) { if (qok) qissue(IC<1>{}); }
            if constexpr (s == 9 && QS == 8 && QB != 1) { if (qok) qissue(IC<2>{}); }
            if constexpr (s == 9 && QD0 < 0) { if (qok) qissue(IC<3>{}); }
            if constexpr (any && !(UX && s == 0)) issuep(IC<UP[s]>{}, IC<0>{}, IC<HA>{});
            xf[s][0][0] = asbf8(lds128(a0 + xo)); xf[s][0][1] = asbf8(lds128(a0 + xo + 2048));
            xf[s][1][0] = asbf8(lds128(a1 + xo)); xf[s][1][1] = asbf8(lds128(a1 + xo + 2048));
            const bf16x8 w0 = asbf8(lds128(ga + s * 128)), w1 = asbf8(lds128(ga + s * 128 + 64));
            if constexpr (any) issuep(IC<UP[s]>{}, IC<HA>{}, IC<HB>{});
            mfma16a<s == 0>(gacc[0], w0, xf[s][0][0]); mfma16a<s == 0>(gacc[1], w0, xf[s][0][1]);
            mfma16a(gacc[0], w1, xf[s][1][0]); mfma16a(gacc[1], w1, xf[s][1][1]);
            if constexpr (any) { issuep(IC<UP[s]>{}, IC<HB>{}, IC<9>{}); sfor<UP[s] + 1, UP[s + 1]>(issue); }
            if constexpr (s == 7 && QB) { if (qok) qissue(IC<0>{}); }
            if constexpr (s == 8 && QB != 2) { if (qok) qissue(IC<QS == 7 || QB ? 2 : 1>{}); }
            if constexpr (s == 9 && QD0 < 0) { if (qok) qissue(IC<4>{}); }
            if constexpr (HS && s >= 1 && s < (MK2 ? 7 : 8)) { sched0(); apart(IC<s - 1>{}); sched0(); }
        });
        __builtin_amdgcn_s_setprio(QPRIO);
        if (!qok) { spin(fqo, rws, tag); sfor<0, (QD0 < 0 ? 5 : 3)>(qissue); }
        asm volatile("s_nop 7\n\ts_nop 3" : "+v"(gacc[0]), "+v"(gacc[1]));
        auto gates = [&]() __attribute__((always_inline)) {
            g0 = __builtin_amdgcn_rcpf(1.f + __builtin_amdgcn_exp2f(-gacc[0][0] * LOG2E));
            g1 = __builtin_amdgcn_rcpf(1.f + __builtin_amdgcn_exp2f(-gacc[1][0] * LOG2E));
            gt = (lane & 16) ? g1 : g0;
        };
        if (TD && ty) {
            sfor<0, 10>(qone);
            if constexpr (TDP) wait_vm<20>();
            else { ctl_wait1(L_TDW + wr * 4, 1); wait_vm<0>(); }
        } else wait_vm<10 + qcnt(-1)>();
        ctl_set(me, 1);
        if constexpr (AP0 || HS) { gates(); asm volatile("" : "+v"(g0), "+v"(g1), "+v"(gt)); }
        const u32 lhi = opaque_v(l16 + L_QT - 51200);
        ctl_wait(L_HQ, 1);
        SPINTO(12);
        STAMP(12);
        STAMPC(55);
        constexpr int PD = 4;
        bf16x8 wf[PD + 1];
        f32x4 qa[2][5];
        u32x4 snap, fs;
        u32 vw;
        auto wadr = [&](int n) {
            const int j = n / 10;
            return (j < 5 ? lhi + 40960 - 10240 * j : l16 + L_QT - 10240 * (j + 1)) + (n % 10) * 1024;
        };
        sfor<0, PD>([&](auto NC) { wf[NC.v] = asbf8(lds128(wadr(NC.v))); });
        if constexpr (TD && TDP) { sched0(); if (ty || TD0) ctl_wait1(L_TDW + wr * 4, 1); STAMP(43); sched0(); }
        sfor<0, 100>([&](auto NC) {
            constexpr int n = decltype(NC)::v, m = n + PD;
            constexpr int st = n / 10, ks = (n % 10) / 5, a = n % 5;
            const bf16x8 w = wf[n % (PD + 1)];
            if constexpr (m < 100 && m % 20 == 0) { if (!ctl_ge(snap, m / 20 + 1)) ctl_wait(L_HQ, m / 20 + 1); cfence(); }
            sched0();
            mfma16a<(n < 5)>(qa[0][a], w, xf[st][ks][0]);
            sched0();
            if constexpr (m < 100) wf[m % (PD + 1)] = asbf8(lds128(wadr(m)));
            sched0();
            mfma16a<(n < 5)>(qa[1][a], w, xf[st][ks][1]);
            sched0();
            if constexpr (QD0 >= 0 && qcnt(n) > qcnt(n - 1)) { if (!(TD && ty)) qone(IC<qcnt(n) - 1>{}); }
            if constexpr (n < 80 && n % 20 == 8) {
                static_assert(5 + qcnt(n) - 5 * (n / 20) >= 0, "q schedule");
                wait_vm<5 + qcnt(n) - 5 * (n / 20)>();
                ctl_set(me, n / 20 + 2);
            }
            if constexpr (AP0 && n >= AP0 && (n - AP0) % APS == 0 && (n - AP0) / APS < 15) apart(IC<(n - AP0) / APS>{});
            if constexpr (n < 80 && n % 20 == 12) snap = ctl_get(L_HQ);
            if constexpr (n == 50 && HWN < 200) fs = ldflag4(fso, rws);
            if constexpr (n == 69 && HWN < 200) { wok = wait_eq4<0>(fs, tag); vw = wok ? l16 : OOB; }
            if constexpr (n >= HWN && (n - HWN) % HWS == 0 && (n - HWN) / HWS < 25) wld(IC<(n - HWN) / HWS>{}, vw);
            sched0();
        });
        constexpr int HW0 = HWN >= 100 ? 0 : (100 - HWN + HWS - 1) / HWS;
        if constexpr (HWN < 200) sfor<HW0, 25>([&](auto I) { wld(I, vw); });
        else sfor<0, 25>([&](auto I) { undef(W[I.v]); });
        mfma_pad();
        STAMP(13);
        STAMPC(56);
        if constexpr (!AP0 && !HS) gates();
        {
            auto cvt = [&](int a, u32x4& o) {
                auto s0 = __builtin_amdgcn_permlane16_swap(pk2(qa[0][a][0] * QSCALE, qa[0][a][1] * QSCALE), pk2(qa[1][a][0] * QSCALE, qa[1][a][1] * QSCALE), false, false);
                auto s1 = __builtin_amdgcn_permlane16_swap(pk2(qa[0][a][2] * QSCALE, qa[0][a][3] * QSCALE), pk2(qa[1][a][2] * QSCALE, qa[1][a][3] * QSCALE), false, false);
                o = u32x4{s0[0], s1[0], s0[1], s1[1]};
            };
            u32x4 r, q1;
            cvt(4, q1); cvt(2, r);
            sts128(L_XQ + wr * 1536 + l16, q1);
            sts64(L_XQ + wr * 1536 + 1024 + lane * 8, u32x2{hf ? f2u(gt) : r[2], r[3]});
            ctl_set(me, 6);
            cvt(3, qf);
            qr = hf ? u32x2{0, 0} : u32x2{r[0], r[1]};
        }
#pragma unroll
        for (int t = 0; t < 2; ++t) {
            if constexpr (PH8 & 2) {
                P[t] = phi8(qa[t][0], qa[t][1]);
                if (t) {
                    const u32x4 w = phi8(qa[0][2], qa[1][2]);
#pragma unroll
                    for (int i = 0; i < 4; ++i) P[2][i] = __builtin_amdgcn_permlane32_swap(w[i], w[i], false, false)[1];
                }
                continue;
            }
            P[t] = u32x4{phi2(qa[t][0][0], qa[t][0][1]), phi2(qa[t][0][2], qa[t][0][3]), phi2(qa[t][1][0], qa[t][1][1]), phi2(qa[t][1][2], qa[t][1][3])};
            const u32 u = phi2(qa[t][2][0], qa[t][2][1]), v = phi2(qa[t][2][2], qa[t][2][3]);
            P[2][2 * t] = __builtin_amdgcn_permlane32_swap(u, u, false, false)[1];
            P[2][2 * t + 1] = __builtin_amdgcn_permlane32_swap(v, v, false, false)[1];
        }
        asm volatile("" : "+v"(P[0]), "+v"(P[1]), "+v"(P[2]), "+v"(qf), "+v"(qr));
        static_assert(AP0 + 14 * APS < 100, "set-up");
        if constexpr (HS) {
            masks(); kc = opaque_v(ka) ^ 64;
            if constexpr (MK2) asm volatile("" : "=v"(f2o));
            asm volatile("" : "+v"(kc), "+s"(out));
        } else if constexpr (!AP0) asetup(0);
        if constexpr (HWN < 200) wlate();
        STAMP(14);
        __builtin_amdgcn_s_setprio(APH);
        if constexpr (HWP) {
            ctl_wait1(L_MC + wr * 4, 2);
            sfor<0, HWP>([&](auto I) { rw128<(I.v & 3) * 1024>(W[I.v], l16, rws, WS_WO + c80 * 204800 + wq * 25600 + (I.v >> 2) * 4096); });
        }
        ctl_wait(L_MC, 3 + ED);
        SPINTO(16);
        STAMP(16);
        STAMPC(57);
    } else {
        const u32 wm = wr >> 1, wn = wr & 1;
        args(true);
        const u32 ev = ldflag(0, rws);
        const u32 ju = JBAL ? ty * 16 + (idx >> 2) * 2 + (c4 & 1) : idx;
        const u32 pw0 = (ju * 2 + (xcd & 1)) * 4 + wr;
        constexpr u32 LQ = JQ >= 0 ? SL[3] : L_SCR;
        static_assert(JQ < 0 || (JQ <= 8 && UP[1] < 4), "scratch");
        u32 qd[2];
#pragma unroll
        for (u32 j = 0; j < 2; ++j) {
            const u32 p = pw0 + 256 * j, isb = p >= 200, q = isb ? p - 200 : p, rg = (q * 205) >> 10;
            const u32 so = p < 400 ? rg * 61440 + sg * 320 + (q - rg * 5) * 64 : 0;
            dma<LQ>(p < 400 ? (lane >> 2) * 3840 + (lane & 3) * 16 : OOB, mkrs(isb ? wb : wa, 640 * 3840), so, wr * 2048 + j * 1024);
        }
        STAMP(37);
        if constexpr (EB) bar();
#pragma unroll
        for (u32 j = 0; j < 2; ++j) {
            const u32 p = opaque_s(pw0) + 256 * j, isb = p >= 200, q = isb ? p - 200 : p, rg = (q * 205) >> 10;
            const u32 n = (q - rg * 5) * 32 + l31, h = (n * 205) >> 12, d = n - 20 * h, o8 = 2 * rg + hf;
            const u32 t = d < 16 ? (h & 1) * 1024 + (isb ? 0 : 3072) + d * 16 : 2048 - 256 + (8 * isb + 4 * (h & 1) + d) * 16;
            qd[j] = p < 400 ? WS_WQ + (4 * sg + (h >> 1)) * 102400 + (o8 >> 2) * 5120 + (o8 & 3) * 256 + t : OOB;
        }
        u32 ov[2], os[2], od[2];
        u32x4 ors[2];
#pragma unroll
        for (u32 j = 0; j < 2; ++j) {
            const u32 p = j * 1024 + (opaque_s(ju) * 8 + xcd) * 4 + wr;
            const bool br = p >= 800;
            const u32 q = br ? p - 800 : p, oct = q / 10, cl = q - oct * 10;
            const u32 kx = oct >> 2, go = oct & 3, G = kx / 5, e = kx - G * 5;
            const u32 r0 = 160 * G + (e == 4 ? 40 * go + 16 : 40 * e + (br ? 20 * (go >> 1) + 4 * (go & 1) : 4 * go));
            const u32 step = e != 4 && br ? 8 : 20;
            ov[j] = opaque_v(p < 1600 ? (((lane >> 3) & 3) + step * hf) * 1280 + (lane & 7) * 16 : OOB);
            os[j] = opaque_s(p < 1600 ? r0 * 1280 + cl * 128 : 0);
            ors[j] = mkrs(br ? woa : wob, 640 * 1280);
            asm volatile("" : "+s"(ors[j]));
            const u32 n = 64 * cl + lane, h = (n * 205) >> 12, d = n - 20 * h;
            od[j] = opaque_v(p < 1600 ? WS_WO + ((h >> 2) * 2 + br) * 102400 + (kx * 5 + (d >> 2)) * 1024 + (go * 16 + 4 * (h & 3) + (d & 3)) * 16 : OOB);
        }
        const u32 osrc = opaque_v(L_SCR + wr * 2048 + (l15 >> 2) * 128 + (4 * g4 + (lane & 3)) * 8);
        const u32 krec = WS_KVP + ((2 * hg + wn) * 16 + rb) * 2048 + wm * 1024;
        if (lane < 3) sts32(L_HQ + lane * 16 + wr * 4, 0);
        u32 wb_[2][3], xb_[2][2];
        {
            const u32 q3 = lane & 3, wrow = (g4 & 1) * WREG + (8 * (g4 >> 1) + (l15 >> 2)) * 320;
            wb_[0][0] = wrow + (ty ? 80 : 160) * wn + q3 * 8;
            wb_[0][1] = wb_[0][0] + (ty ? 160 : 80);
            wb_[0][2] = wrow + 32 + (ty ? 80 * wn + ((q3 & 2) ? 0 : 160) + 40 * (q3 & 1) : 160 * wn + 40 * q3);
            xb_[0][0] = XO + (64 * wm + 16 * (l15 >> 3) + (l15 & 7)) * 128 + ((g4 ^ (l15 & 6)) * 16);
            xb_[0][1] = xb_[0][0] ^ 64;
#pragma unroll
            for (int i = 0; i < 3; ++i) wb_[1][i] = opaque_v(wb_[0][i] + 2 * SLOT);
#pragma unroll
            for (int i = 0; i < 2; ++i) xb_[1][i] = opaque_v(xb_[0][i] + 2 * SLOT);
        }
        const u32 qsrc = LQ + wr * 2048 + (8 * hf + (l15 >> 2)) * 64 + (g4 & 1) * 32 + (lane & 3) * 8;
        auto wad = [&](int s, int m) { return wb_[SL[s % 4] >= 2 * SLOT][(m % 5) >> 1] + SL[s % 4] % (2 * SLOT) + (m / 5) * (2 * WREG) + ((m % 5) & 1) * 40; };
        auto xad = [&](int s, int ks, int t) { return xb_[SL[s % 4] >= 2 * SLOT][ks] + SL[s % 4] % (2 * SLOT) + (t >> 1) * 4096 + (t & 1) * 1024; };
        f32x4 acc[4][5];
        STAMP(30);
        auto oload = [&]() __attribute__((always_inline)) { dma<L_SCR>(ov[0], ors[0], os[0], wr * 2048); dma<L_SCR + 1024>(ov[1], ors[1], os[1], wr * 2048); };
        auto qjob = [&]() __attribute__((always_inline)) {
            tag = wait_rfl<JQ >= 0 ? 2 : 0>(ev) + TAGC;
            STAMP(31);
            const bf16x8 j0 = ldtr2(qsrc, qsrc + 256), j1 = ldtr2(qsrc + 1024, qsrc + 1280);
            if constexpr (JQ < 0) { wait_lgkm<0>(); oload(); }
            st128(__builtin_bit_cast(u32x4, j0), qd[0], rws, 0);
            st128(__builtin_bit_cast(u32x4, j1), qd[1], rws, 0);
            if (wr == 0 && lane == 0) sts32(L_TAG, tag);
            STAMP(38);
        };
        if constexpr (JQ < 0) qjob();
        else {
            asm volatile("" : "+v"(qd[0]), "+v"(qd[1]));
            if constexpr (JW) wait_vm<JW - 1>();
            oload();
        }
        auto marker = [&](int i) {
            const u32x4 mk4 = {~0u, ~0u, ~0u, ~0u};
            if (i < 4) st128(mk4, l16, rws, oprod + c4 * 1024 + i * 20480);
            else st128(mk4, (g4 >> 1) * 40960 + (g4 & 1) * 20480 + l15 * 16, rws, oprod + 4096 + c4 * 256);
        };
        auto markers = [&]() { if constexpr (MKS < 0) sfor<0, 5>([&](auto I) { marker(I.v); }); };
        constexpr int MKN = MKS < 0 ? 5 : 0;
        auto pk8 = [&](int u, int c) {
            return u32x4{pk2(acc[2 * u][c][0], acc[2 * u][c][1]), pk2(acc[2 * u][c][2], acc[2 * u][c][3]),
                         pk2(acc[2 * u + 1][c][0], acc[2 * u + 1][c][1]), pk2(acc[2 * u + 1][c][2], acc[2 * u + 1][c][3])};
        };
        const u32 tso = WS_T + ((4 * hg + 2 * wn) * 64 + 4 * rb + 2 * wm) * 2560;
        u32 tvm, tvr;
        auto tadr = [&]() __attribute__((always_inline)) {
            const u32 ln = IL ? opaque_v(lane) : lane, a15 = ln & 15, a4 = ln >> 4;
            tvm = a4 * 256 + ((a15 ^ (4 * a4)) * 16);
            tvr = 2048 + a4 * 128 + ((((a15 >> 2) ^ (a4 >> 1)) & 1) * 64) + (a15 & 3) * 16 + (a15 >> 3) * 163840;
            if constexpr (IL) asm volatile("" : "+v"(tvm), "+v"(tvr));
        };
        auto tst = [&](auto I) __attribute__((always_inline)) {
            constexpr int c = decltype(I)::v >> 1, u = decltype(I)::v & 1;
            st128<(c < 4 ? (c & 1) * 1024 : 0) + u * 2560>(pk8(u, c), c < 4 ? tvm : tvr, rws, tso + (c == 2 || c == 3 ? 163840 : 0));
            if constexpr (TD0 == I.v + 1) { ctl_set(L_TDW + wr * 4, 1); STAMP(42); }
        };
        {
            bf16x8 wf[5], xa[2][4], j0, j1;
            STAMP(1);
            wait_lgkm<0>();
            bar();
            SPINTO(2);
            STAMP(2);
            sfor<0, 4>([&](auto I) { xa[0][I.v] = asbf8(lds128(xad(0, 0, I.v))); });
            wf[0] = ldtr2(wad(0, 0), wad(0, 0) + 1280);
            wf[1] = ldtr2(wad(0, 1), wad(0, 1) + 1280);
            sfor<0, 100>([&](auto NC) {
                constexpr int s = decltype(NC)::v / 10, n = decltype(NC)::v % 10, ks = n / 5, c = n % 5;
                constexpr int s2 = n >= 8 ? s + 1 : s, m = (n + 2) % 10;
                constexpr bool more = s2 < 10, first = s == 0 && ks == 0, rdw = more && (!(XPM & 1) || s == 0), rdx = !(XPM & 2) || s == 0;
                bf16x4 lo, hi;
                bf16x8 t0, t1;
                auto mm = [&](auto I) { sched0(); if constexpr (!(XPM & 4) || s == 0) mfma16a<first>(acc[I.v][c], xa[ks][I.v], wf[c]); else asm volatile("" :: "v"(xa[ks][I.v]), "v"(wf[c])); sched0(); };
                mm(IC<0>{});
                if constexpr (NC.v == JQ) qjob();
                constexpr bool JL = JST > FQS * 10 + 8;
                if constexpr (NC.v == JST) {
                    if constexpr (!JL) wait_vm<JQ >= 0 ? 2 : 0>();
                    STAMP(36);
                    j0 = ldtr2(osrc, osrc + 512); j1 = ldtr2(osrc + 1024, osrc + 1536);
                }
                if constexpr (NC.v == JST + JD) st128(__builtin_bit_cast(u32x4, j0), od[0], rws, 0);
                if constexpr (NC.v == JST + JD + JE) { st128(__builtin_bit_cast(u32x4, j1), od[1], rws, 0); STAMP(35); }
                if constexpr (n == 8 && more) {
                    wait_lgkm<0>();
                    static_assert(JST + JD + JE <= FSS * 10 + 8 && FSS > FQS && (!JL || JQ < 0), "flags");
                    if constexpr (s == FQS && JL) wait_vm<0>();
                    if constexpr (s == FQS && JQ >= 0) wait_vm<2>();
                    if constexpr (s == FSS) wait_vm<0>();
                    bar();
                    sched0();
                }
                if constexpr (rdw) lo = ldtr(wad(s2, m));
                mm(IC<1>{});
                if constexpr (rdw) hi = ldtr(wad(s2, m) + 1280);
                mm(IC<2>{});
                if constexpr (n >= 8 && more && rdx) t0 = asbf8(lds128(xad(s2, 0, 2 * (n - 8))));
                if constexpr (n < 4 && rdx) xa[1][n] = asbf8(lds128(xad(s, 1, n)));
                mm(IC<3>{});
                if constexpr (n >= 8 && more && rdx) { t1 = asbf8(lds128(xad(s2, 0, 2 * (n - 8) + 1))); xa[0][2 * (n - 8)] = t0; xa[0][2 * (n - 8) + 1] = t1; }
                if constexpr (s == MKS && n % 2 == 0) marker(n / 2);
                if constexpr (s == OTW && n == 2) {
                    const __amdgpu_buffer_rsrc_t ors = __builtin_amdgcn_make_buffer_rsrc(out, 0, 2048 * 1280, 0x00020000);
                    __builtin_amdgcn_raw_buffer_store_b32(0, ors, l15 * 1280 + g4 * 40 + (r64 * 64 + wr * 16) * 1280 + c80 * 160, 0, OAUX);
                }
                if constexpr (NC.v == FQS * 10 + 8 + FD) { if (wr == 0) stflag(tag, WS_FQ + sg * 1024 + (idx >> 3) * 256 + ((idx & 7) * 2 + (xcd & 1)) * 4, rws); }
                if constexpr (NC.v == FSS * 10 + 8 + FD) { if (wr == 1) stflag(tag, WS_FS + (b >> 4) * 256 + (b & 15) * 4, rws); }
                if constexpr (IL && NC.v == 93) tadr();
                if constexpr (IL && NC.v >= 100 - IL) { if (!ty) sfor<2 * (NC.v - 100 + IL), 2 * (NC.v - 100 + IL) + 2>(tst); }
                if constexpr (rdw) wf[m % 5] = cat8(lo, hi);
                sched0();
            });
        }
        mfma_pad();
        __builtin_amdgcn_s_setprio(PRIO);
        SPINTO(12);
        STAMP(12);
        auto ph8 = [&](int u, int c) {
            if constexpr (PH8 & 1) return phi8(acc[2 * u][c], acc[2 * u + 1][c]);
            return u32x4{phi2(acc[2 * u][c][0], acc[2 * u][c][1]), phi2(acc[2 * u][c][2], acc[2 * u][c][3]),
                         phi2(acc[2 * u + 1][c][0], acc[2 * u + 1][c][1]), phi2(acc[2 * u + 1][c][2], acc[2 * u + 1][c][3])};
        };
        if (!ty) {
            const u32x4 fs = ldflag4(fso, rws);
            if constexpr (!IL) tadr();
            sfor<2 * IL, 10>(tst);
            STAMP(32);
            if constexpr (!MKA) markers();
            STAMP(33);
            wok = wait_eq4<MKA ? 0 : MKN>(fs, tag);
            stflag(tag, WS_FT + ((hg * 2 + wn) * 32 + 2 * rb + wm) * 128, rws);
            STAMP(40);
            if constexpr (MKA && !MK2) markers();
            STAMP(34);
            const u32 vw = wok ? l16 : OOB;
            if constexpr (!W2) sfor<0, WA>([&](auto I) { wld(I, vw); });
        } else {
            const u32x4 fs = ldflag4(fso, rws);
            if constexpr (!MK1) markers();
            STAMP(33);
            if constexpr (PFW) {
                const u32 po = WS_WO + (((idx >> 2) * 2 + (idx & 1)) * 4 + wr) * 25600;
                pref(lane * 128, rws, po); pref(lane * 128, rws, po + 8192); pref(lane * 128, rws, po + 16384); pref(lane < 8 ? lane * 128 : OOB, rws, po + 24576);
            }
            f32x4 Q0 = {0, 0, 0, 0}, Q1 = Q0, X0 = Q0, X1 = Q0, Y0 = Q0, Y1 = Q0, U = Q0;
#pragma unroll
            for (int u = 0; u < 2; ++u) {
                const bf16x8 v0 = asbf8(pk8(u, 2)), v1 = asbf8(pk8(u, 3));
                const u32x4 rr = pk8(u, 4);
                const bf16x8 rbv = asbf8(l15 < 8 ? rr : u32x4{ONES, ONES, ONES, ONES});
                auto sig = [&](int k) { if (TD && 3 * u + k == TD) { sched0(); ctl_set(L_TDW + wr * 4, 1); STAMP(42); sched0(); } };
                const bf16x8 k0 = asbf8(ph8(u, 0));
                sig(1);
                Q0 = mfma16(k0, v0, Q0); X0 = mfma16(k0, rbv, X0);
                const bf16x8 k1 = asbf8(ph8(u, 1));
                sig(2);
                Q1 = mfma16(k1, v1, Q1); X1 = mfma16(k1, rbv, X1);
                const bf16x8 ra = asbf8(ph8(u, 4));
                sig(3);
                Y0 = mfma16(ra, v0, Y0); Y1 = mfma16(ra, v1, Y1); U = mfma16(ra, rbv, U);
                if (u == WU && !MK1) {
                    sched0();
                    STAMP(34);
                    wok = wait_eq4<MKN + 4 * PFW>(fs, tag);
                    const u32 vw = wok ? l16 : OOB;
                    sfor<0, WA>([&](auto I) { wld(I, vw); });
                    STAMP(32);
                    sched0();
                }
            }
            f32x4 X = (l15 & 4) ? X1 : X0, Z;
#pragma unroll
            for (int r = 0; r < 4; ++r) Z[r] = u2f(__builtin_amdgcn_permlane32_swap(f2u((g4 & 1) ? Y1[r] : Y0[r]), f2u(U[r]), false, false)[1]);
            sts128(L_SCR + wr * 2048 + l16, __builtin_bit_cast(u32x4, wm ? Q0 : X));
            sts128(L_SCR + wr * 2048 + 1024 + l16, __builtin_bit_cast(u32x4, wm ? Q1 : Z));
            ctl_set(me, 1);
            ctl_wait1(L_MC + (wr ^ 2) * 4, 1);
            const f32x4 a = (wm ? X : Q0) + __builtin_bit_cast(f32x4, lds128(L_SCR + (wr ^ 2) * 2048 + l16));
            const f32x4 c = (wm ? Z : Q1) + __builtin_bit_cast(f32x4, lds128(L_SCR + (wr ^ 2) * 2048 + 1024 + l16));
            st128(u32x4{pk2(a[0], a[1]), pk2(a[2], a[3]), pk2(c[0], c[1]), pk2(c[2], c[3])}, l16, rws, krec);
            if constexpr (MK1) {
                STAMP(34);
                wok = wait_eq4<1>(fs, tag);
                if constexpr (!MK2) markers();
                STAMP(32);
                const u32 vw = wok ? l16 : OOB;
                if constexpr (!W2) sfor<0, WA>([&](auto I) { wld(I, vw); });
            }
        }
        STAMP(13);
        if constexpr (CPF) {
            u64 pc; u32 len; int rel;
            asm volatile("s_getpc_b64 %0\n.Lcode_p2:\n\ts_mov_b32 %2, .Lcode_at-.Lcode_p2\n\ts_mov_b32 %1, .Lcode_hi-.Lcode_at" : "=s"(pc), "=s"(len), "=s"(rel));
            pc += (long long)rel;
            u32 o = wr * 8192 + lane * 128;
            o = o < len ? o : 0;
            asm volatile("s_mov_b32 m0, %2\n\ts_nop 0\n\tglobal_load_lds_dword %0, %1" :: "v"(o), "s"(pc), "i"(L_DUMP) : "memory");
        }
        {
            const int g0 = 2 * (int)rb - 4 + 3 * (int)wn;
            int gl = g0 + (int)(lane < 2 ? lane : 2);
            gl = gl < 0 ? 0 : gl;
            const u32 fto = WS_FT + ((wm * 8 + (hp >> 1)) * 32 + gl) * 128;
            if constexpr (F4) {
                static_assert(!F4 || (MK1 && MKA && !ED), "f4");
                if constexpr (HWP) wlate();
                ctl_set(me, 2);
                const u32 v = ldflag(fto, rws);
                if constexpr (AE == 1) { sched0(); asetup(1); sched0(); }
                wait_vm<(W2 ? 0 : WA) + 1>();
                stflagn(tag, rfl(WS_F2 + rb * 256 + hp * 16 + wr * 4), rws);
                STAMP(41);
                if constexpr (AE == 2) { sched0(); asetup(1); sched0(); }
                if (!wait_eq<1>(v, tag)) spin(fto, rws, tag);
                STAMP(39);
                ctl_wait8(2);
            } else {
            if constexpr (ED) ctl_set(me, 2);
            spin(fto, rws, tag);
            STAMP(39);
            ctl_set(me, 2 + ED);
            ctl_wait8(2);
            if constexpr (!ED) { if (wr == 0) stflag(tag, WS_F2 + rb * 256 + hp * 4, rws); }
            }
            STAMP(14);
#pragma unroll
            for (int i = 0; i < 3; ++i) {
                const u32 mb = wm ? L_V + (3 * wn + i) * 5136 : L_K + (3 * wn + i) * 5120;
                if (g0 + i >= 0) {
                    const u32 so = WS_T + ((wm * 16 + hp) * 32 + g0 + i) * 5120;
                    dmac<0, 0>(l16, rws, so, mb); dmac<0, 1024>(l16, rws, so, mb); dmac<0, 2048>(l16, rws, so, mb); dmac<0, 3072>(l16, rws, so, mb);
                    dmac<4096, 0>(l16, rws, so + 4096, mb);
                } else {
#pragma unroll
                    for (int k = 0; k < 5; ++k) sts128(mb + k * 1024 + l16, u32x4{0, 0, 0, 0});
                }
            }
            if (wm && lane < 12) sts32(L_V + (3 * wn + (lane >> 2)) * 5136 + 5120 + (lane & 3) * 4, g0 + (int)(lane >> 2) >= 0 ? ONES : 0);
            if constexpr (W2) {
                static_assert(!W2 || (F4 && WM0), "w2");
                if (!wok) {
#pragma nounroll
                    for (u32 i = 0; i < (1u << 17); ++i) { if (wait_eq4<0>(ldflag4(fso, rws), tag)) break; __builtin_amdgcn_s_sleep(SPINS); }
                }
                sfor<0, WA>([&](auto I) { wld(I, l16); });
            } else if constexpr (!HWP) wlate();
            static_assert(!MK2 || (F4 && MK1 && MKS < 0 && WM0 && !W2 && MF <= HK2 && HM > HK2), "mk2");
            if constexpr (MK2 == 1) markers();
            sfor<WA, 25>([&](auto I) { if constexpr (WM0) undef(W[I.v]); else wld(I, l16); });
            if constexpr (!AE) asetup(1);
            asm volatile("" : "=v"(P[0]), "=v"(P[1]), "=v"(P[2]));
            if (ED && wr == 0) { ctl_wait(L_MC, 3); stflag(tag, WS_F2 + rb * 256 + hp * 4, rws); wait_vm<WM0 ? 1 : 26 - WA>(); }
            else wait_vm<MK2 == 1 ? 5 : W2 ? WA : WM0 ? 0 : 25 - WA>();
            ctl_set(me, 3 + ED);
            STAMP(15);
            if constexpr (MK2 == 2) markers();
        }
        ctl_wait(L_HQ, 6);
        qf = lds128(L_XQ + wr * 1536 + l16);
        const u32x2 r = lds64(L_XQ + wr * 1536 + 1024 + lane * 8);
        qr = hf ? u32x2{0, 0} : r;
        gt = u2f(lds32(L_XQ + wr * 1536 + 1280 + l31 * 8));
        ctl_wait(L_MC, 3 + ED);
        if (b == 0 && wr == 0) stflag(tag, WS_E, rws);
        __builtin_amdgcn_s_setprio(APM);
        SPINTO(16);
        STAMP(16);
        asm volatile(".Lcode_hm:");
    }
    asm volatile(".Lcode_at:");
    ws = (char*)(((u64)rfl((u32)((u64)ws >> 32)) << 32) | rfl((u32)(u64)ws));
    rws = mkrs(ws, WS_END);
    const __amdgpu_buffer_rsrc_t wsr = __builtin_amdgcn_make_buffer_rsrc(ws, 0, WS_END, 0x00020000);
    auto recs = [&]() {
        const u32 vo = hf * 2048 + l31 * 16, so = WS_KVP + hp * 32768 + wr * 512;
        sfor<0, 8>([&](auto I) { dmac<L_RC + I.v * 1024>(vo, rws, so + I.v * 4096, wr * 8192); });
    };
    auto kvsum = [&]() __attribute__((always_inline)) {
        float s[8];
#pragma unroll
        for (int i = 0; i < 8; ++i) {
            const u32x4 v = lds128(L_RC + wr * 8192 + i * 1024 + l16);
#pragma unroll
            for (int k = 0; k < 4; ++k) { s[2 * k] = i ? s[2 * k] + blo(v[k]) : blo(v[k]); s[2 * k + 1] = i ? s[2 * k + 1] + bhi(v[k]) : bhi(v[k]); }
        }
        float t[4];
#pragma unroll
        for (int k = 0; k < 4; ++k) {
            auto sw = __builtin_amdgcn_permlane32_swap(f2u(s[k]), f2u(s[4 + k]), false, false);
            t[k] = u2f(sw[0]) + u2f(sw[1]);
        }
        sts64(L_REC + wr * 512 + l31 * 16 + hf * 8, u32x2{pk2(t[0], t[1]), pk2(t[2], t[3])});
    };
    u32 pv;
    u64 pv4;
    u32 pm, sm4;
    const u32 fmo = WS_FM + (r64 >> 1) * 256;
    bool rok = false;
    {
        const u32 hh = wid < 4;
        f32x16 zero;
#pragma unroll
        for (int i = 0; i < 16; ++i) zero[i] = 0.f;
        const bf16x8 q16 = asbf8(qf);
        const bf16x4 q4 = __builtin_bit_cast(bf16x4, qr);
        auto qk = [&](u32 o) { return mfma32k8(ldtr(kr + o), q4, mfma32(ldtr2(ka + o, kc + o), q16, zero)); };
        f32x16 o = zero;
        u64 hmask;
        asm volatile("s_cmp_ge_u32 %1, 4\n\ts_cselect_b64 %0, -1, 0" : "=s"(hmask) : "s"(wid) : "scc");
        auto attn = [&](auto HC) __attribute__((always_inline)) {
        const bool isH = HC.v < 2 ? HC.v == 1 : wid >= 4;
        sched0();
        bf16x8 j16 = ldtr2(ka + 2560, kc + 2560), v2, v3;
        bf16x4 j4 = ldtr(kr + 2560);
        bf16x8 k16 = ldtr2(ka + 5120, kc + 5120);
        bf16x4 k4 = ldtr(kr + 5120);
        bf16x8 v0 = asbf8(lds128(va[1][0])), v1 = asbf8(lds128(va[1][1]));
        sched0();
        f32x16 S = mfma32k8(j4, q4, mfma32(j16, q16, zero)), Sn, S8;
        sfor<1, 9>([&](auto I) {
            constexpr int it = decltype(I)::v, i = it & 7, nx = (it + 1) & 7;
            auto wl = [&](auto J) {
                constexpr int k = 24 - J.v;
                if constexpr (WM0 && k >= WA) rw128<(k & 3) * 1024>(W[k], l16, rws, WS_WO + c80 * 204800 + wq * 25600 + (k >> 2) * 4096);
                else if constexpr (XM && k >= HWP) rl128x<(k & 3) * 1024>(W[k], l16, rws, WS_WO + c80 * 204800 + wq * 25600 + (k >> 2) * 4096, hmask);
                else if constexpr (HWN >= 200 && k >= HWP) { if (isH) rw128<(k & 3) * 1024>(W[k], l16, rws, WS_WO + c80 * 204800 + wq * 25600 + (k >> 2) * 4096); }
            };
            float e[16];
            u32 p[8];
            auto ex = [&](auto A, auto B) {
                sfor<A.v, B.v>([&](auto R) {
                    if constexpr (i == 0) e[R.v] = __builtin_amdgcn_exp2f(__builtin_amdgcn_inverse_ballot_w64(lem((R.v & 3) + 8 * (R.v >> 2))) ? S8[R.v] : S[R.v]);
                    else e[R.v] = __builtin_amdgcn_exp2f(S[R.v]);
                });
                sched0();
            };
            auto cv = [&](int a) {
#pragma unroll
                for (int j = a; j < a + 4; ++j) p[j] = pk2(e[2 * j], e[2 * j + 1]);
                sched0();
            };
            sched0();
            if constexpr ((ASM >> it) & 1) STAMP(35 + it);
            if constexpr (MK2) {
                constexpr int lo = 3 * HK1 - 2, hi = 3 * HK2 - 2, nm = (25 - WA < hi ? (25 - WA > lo ? 25 - WA - lo : 0) : hi - lo) + (MF > HK1);
                static_assert(25 - WA <= hi && HK1 > 1 && MF > 1, "hk");
                if constexpr (it == MF) {
                    if (!isH) { wait_vm<(25 - WA < 3 * MF - 2 ? 25 - WA : 3 * MF - 2) + (MF > HK1)>(); stflagn(tag, rfl(WS_FM + rb * 256 + hp * 16 + wr * 4), rws); ctl_set(me, 4); STAMP(45); }
                }
                if constexpr (it == HK1) { if (!isH) pv = ldflag(f2o, rws); else pv = undef1(); }
                if constexpr (it == HK2) { if (!isH) { rok = wait_eq<nm>(pv, tag); if (rok) recs(); } }
                static_assert(!KS || (KS > HK2 && KS <= HM), "ks");
                if constexpr (it == KS) { if (rok) { wait_vm<0>(); kvsum(); ctl_set(me, 5); STAMP(46); } }
                if constexpr (it == HM) {
                    pm = ldflags(lane * 4, rws, fmo);
                    sm4 = *(volatile AS3 u32*)(L_MC + wr * 4);
                }
            } else {
            if constexpr (it == HK1) { if constexpr (F4) pv4 = ldflag2(f2o, rws); else pv = ldflag(f2o, rws); }
            if constexpr (it == HK2) {
                constexpr int lo = 3 * HK1 - 2, hi = 3 * HK2 - 2, nm = !WM0 ? 0 : (25 - WA < hi ? (25 - WA > lo ? 25 - WA - lo : 0) : hi - lo);
                static_assert(HK1 > 1 && (!WM0 || 25 - WA <= hi), "hk");
                if constexpr (F4) {
                    if (!isH) { rok = wait_eq2<nm>(pv4, tag); if (rok) recs(); }
                    else rok = wait_eq2<(HWN >= 200 ? 3 * (HK2 - HK1) : 0)>(pv4, tag);
                } else {
                    if (!isH) { rok = wait_eq<nm>(pv, tag); if (rok) recs(); }
                    else rok = wait_eq<(HWN >= 200 ? 3 * (HK2 - HK1) : 0)>(pv, tag);
                }
            }
            }
            if constexpr (it == 1) wl(IC<0>{});
            sched0();
            if constexpr (i != 0) Sn = mfma32(k16, q16, zero);
            sched0();
            ex(IC<0>{}, IC<8>{});
            if constexpr (i != 0) Sn = mfma32k8(k4, q4, Sn);
            sched0();
            if constexpr (it < 7) { k16 = ldtr2(ka + ((it + 2) & 7) * 2560, kc + ((it + 2) & 7) * 2560); k4 = ldtr(kr + ((it + 2) & 7) * 2560); }
            if constexpr (it == 5) { j16 = ldtr2(ka + 8 * 2560, kc + 8 * 2560); j4 = ldtr(kr + 8 * 2560); }
            if constexpr (it == 7) { v2 = asbf8(lds128(va[0][0] + 4 * 5136)); v3 = asbf8(lds128(va[0][1] + 4 * 5136)); }
            wl(IC<3 * it - 2>{});
            sched0();
            cv(0);
            if constexpr (i == 0) { o = mfma32(v2, asbf8(u32x4{p[0] & mk[0], p[1] & mk[1], p[2] & mk[2], p[3] & mk[3]}), o); sched0(); }
            ex(IC<8>{}, IC<12>{});
            if constexpr (i == 0) o = mfma32(v0, asbf8(u32x4{p[0] & ~mk[0], p[1] & ~mk[1], p[2] & ~mk[2], p[3] & ~mk[3]}), o);
            else o = mfma32(v0, asbf8(u32x4{p[0], p[1], p[2], p[3]}), o);
            sched0();
            wl(IC<3 * it - 1>{});
            if constexpr (it == 6) S8 = mfma32(j16, q16, zero);
            sched0();
            ex(IC<12>{}, IC<16>{});
            cv(4);
            if constexpr (i == 0) {
                o = mfma32(v3, asbf8(u32x4{p[4] & mk[4], p[5] & mk[5], p[6] & mk[6], p[7] & mk[7]}), o);
                o = mfma32(v1, asbf8(u32x4{p[4] & ~mk[4], p[5] & ~mk[5], p[6] & ~mk[6], p[7] & ~mk[7]}), o);
            } else o = mfma32(v1, asbf8(u32x4{p[4], p[5], p[6], p[7]}), o);
            sched0();
            if constexpr (it == 6) S8 = mfma32k8(j4, q4, S8);
            if constexpr (i != 0) { v0 = asbf8(lds128(va[nx & 1][0] + (nx >> 1) * 5136)); v1 = asbf8(lds128(va[nx & 1][1] + (nx >> 1) * 5136)); }
            wl(IC<3 * it>{});
            sched0();
            if constexpr (i != 0) S = Sn;
        });
        };
        if constexpr (DUP) { if (wid >= 4) attn(IC<1>{}); else attn(IC<0>{}); } else attn(IC<2>{});
        if constexpr ((ASM >> 9) & 1) STAMP(44);
        const int qq = (int)l31 - 4 * (int)hf;
        float am = 0.f;
#pragma unroll
        for (int r = 0; r < 12; r += 2) am = fmaxf(fmaxf(am, fabsf(o[r])), fabsf(o[r + 1]));
        const bool fine = o[12] > 0x1p-30f && o[12] < 0x1p30f && am < 0x1p120f;
        if (__builtin_amdgcn_ballot_w64(!fine) != 0ull) {
            const int i0 = rb >= 2 ? 0 : 8 - 4 * (int)rb - (int)wr;
            auto sc = [&](int i) {
                f32x16 s = qk(i * 2560);
#pragma unroll
                for (int r = 0; r < 16; ++r) {
                    const bool le = (r & 3) + 8 * (r >> 2) <= qq;
                    const bool ok = i == 0 ? !le : (i == 8 ? le : true);
                    s[r] = ok ? s[r] : -__builtin_inff();
                }
                return s;
            };
            float m = -__builtin_inff();
#pragma nounroll
            for (int i = i0; i < 9; ++i) {
                const f32x16 s = sc(i);
#pragma unroll
                for (int r = 0; r < 16; ++r) m = fmaxf(m, s[r]);
            }
            auto sw = __builtin_amdgcn_permlane32_swap(f2u(m), f2u(m), false, false);
            m = fmaxf(u2f(sw[0]), u2f(sw[1]));
            o = zero;
#pragma nounroll
            for (int i = i0; i < 9; ++i) {
                const f32x16 s = sc(i);
                u32 p[8];
#pragma unroll
                for (int j = 0; j < 8; ++j) p[j] = pk2(__builtin_amdgcn_exp2f(s[2 * j] - m), __builtin_amdgcn_exp2f(s[2 * j + 1] - m));
                const u32 vo_ = (i >> 1) * 5136;
                o = mfma32(asbf8(lds128(((i & 1) ? va[1][0] : va[0][0]) + vo_)), asbf8(u32x4{p[0], p[1], p[2], p[3]}), o);
                o = mfma32(asbf8(lds128(((i & 1) ? va[1][1] : va[0][1]) + vo_)), asbf8(u32x4{p[4], p[5], p[6], p[7]}), o);
            }
        }
        if constexpr (MK2) { if (rfl(sm4) < 4) ctl_wait1(L_MC + wr * 4, 4); cfence(); }
        const float rs = __builtin_amdgcn_rcpf(o[12]) * gt;
        const u32 ob = oprod + 20480 + (l31 >> 4) * 40960;
        u32x4 v = {pk2m(o[0] * rs, o[1] * rs), pk2m(o[2] * rs, o[3] * rs), pk2m(o[4] * rs, o[5] * rs), pk2m(o[6] * rs, o[7] * rs)};
        __builtin_amdgcn_raw_buffer_store_b128(v, wsr, ob + c4 * 1024 + (2 * hh + hf) * 256 + l15 * 16, 0, 16);
        u32x2 vt = {pk2m(o[8] * rs, o[9] * rs), pk2m(o[10] * rs, o[11] * rs)};
        __builtin_amdgcn_raw_buffer_store_b64(vt, wsr, hf ? OOB : ob + 4096 + c4 * 256 + l15 * 16 + hh * 8, 0, 16);
        cfence();
    }
    STAMP(17);
    STAMPC(58);
    bool mok;
    if constexpr (MK2) {
        if (wid < 4) { mok = wait_eq<2>(pm, tag); if (!rok) spin(f2o, rws, tag); }
        else mok = wait_eq<2 + (25 - HWP > 3 * HM - 2 ? 25 - HWP - (3 * HM - 2) : 0)>(pm, tag);
    } else
    if (!rok) {
        if constexpr (F4) {
#pragma nounroll
            for (u32 i = 0; i < (1u << 17); ++i) { if (wait_eq2<0>(ldflag2(f2o, rws), tag)) break; __builtin_amdgcn_s_sleep(SPINS); }
        } else spin(f2o, rws, tag);
    }
    if (wid < 4) {
        if (rok) wait_vm<2>(); else { recs(); wait_vm<0>(); }
        if (!KS || !rok) kvsum();
    } else {
        ctl_wait(L_MC, KS ? 5 : 10);
        STAMP(23);
        const u32x4 m = lds128(L_REC + l16), n = lds128(L_REC + 1024 + l16);
        const bool m1 = l15 & 4;
        const bf16x8 a0 = asbf8(u32x4{m[0], m[1], g4 == 0 ? n[2] : 0, g4 == 0 ? n[3] : 0});
        const bf16x8 a1 = asbf8(u32x4{m[2], m[3], g4 == 1 ? n[2] : 0, g4 == 1 ? n[3] : 0});
        const bf16x8 x0 = asbf8(u32x4{m1 ? 0 : n[0], m1 ? 0 : n[1], (g4 == 2 && !m1) ? n[2] : 0, (g4 == 2 && !m1) ? n[3] : 0});
        const bf16x8 x1 = asbf8(u32x4{m1 ? n[0] : 0, m1 ? n[1] : 0, (g4 == 3 && m1) ? n[2] : 0, (g4 == 3 && m1) ? n[3] : 0});
#pragma unroll
        for (int t2 = 0; t2 < 2; ++t2) {
            const bf16x8 b0 = asbf8(u32x4{P[t2][0], P[t2][1], P[2][2 * t2], P[2][2 * t2 + 1]});
            const bf16x8 b1 = asbf8(u32x4{P[t2][2], P[t2][3], P[2][2 * t2], P[2][2 * t2 + 1]});
            const f32x4 z = {0, 0, 0, 0};
            const f32x4 xx = mfma16(x1, b1, mfma16(x0, b0, z));
            const f32x4 n0 = mfma16(a0, b0, z), n1 = mfma16(a1, b1, z);
            const u32 rc = f2u(__builtin_amdgcn_rcpf(xx[0] + 1e-6f) * (1.f - (t2 ? g1 : g0)));
            const u32 rs = __builtin_amdgcn_permlane32_swap(rc, rc, false, false)[1];
            auto sw = __builtin_amdgcn_permlane16_swap(rs, rs, false, false);
            const float r0 = u2f(sw[0]), r1 = u2f(sw[1]), rr = u2f(rs);
            const u32 so = oprod + t2 * 40960;
            st128(u32x4{pk2m(n0[0] * r0, n0[1] * r0), pk2m(n0[2] * r0, n0[3] * r0), pk2m(n1[0] * r1, n1[1] * r1), pk2m(n1[2] * r1, n1[3] * r1)}, l16, rws, so + c4 * 1024);
            __builtin_amdgcn_raw_buffer_store_b64(u32x2{pk2m(xx[0] * rr, xx[1] * rr), pk2m(xx[2] * rr, xx[3] * rr)}, wsr, hf ? OOB : l15 * 16 + g4 * 8, so + 4096 + c4 * 256, 16);
        }
        cfence();
    }
    ctl_set(me, 10);
    STAMP(24);
    {
        u32x4 O[4][5];
        u32 so[4];
#pragma unroll
        for (u32 t = 0; t < 4; ++t) so[t] = WS_O + (r64 * 4 + (wr ^ ((t + 1) & 3))) * 40960 + wq * 5120;
        const u32 lr = opaque_v(l16 + 1024);
        auto old = [&](auto T, auto E) { if constexpr (E.v < 4) ul128<E.v * 1024>(O[T.v][E.v], l16, rws, so[T.v]); else ul128<3072>(O[T.v][4], lr, rws, so[T.v]); };
        auto owait = [&](auto T, auto N) { wait5<N.v>(O[T.v]); };
        auto omax = [&](int t) { return mx4(mx4(mx4(mx4(mx4(0, O[t][0]), O[t][1]), O[t][2]), O[t][3]), O[t][4]); };
        auto slow = [&](auto T) {
            constexpr int hj = T.v + NUP < 4 ? T.v + NUP : 4;
            sfor<T.v, hj>([&](auto U) { owait(U, IC<0>{}); });
#pragma nounroll
            for (u32 it = 0; it < (1u << 17); ++it) {
                sfor<T.v, hj>([&](auto U) {
                    sfor<0, 5>([&](auto E) {
                        if (marked(mx4(0, O[U.v][E.v]))) {
                            if constexpr (E.v < 4) rl128<E.v * 1024>(O[U.v][E.v], l16, rws, so[U.v]); else rl128<3072>(O[U.v][4], lr, rws, so[U.v]);
                        }
                    });
                });
                sfor<T.v, hj>([&](auto U) { owait(U, IC<0>{}); });
                if (!marked(omax(T.v))) break;
            }
        };
        f32x4 ac[2][5], sm[5];
        const u32 grp = wid >= 4 ? L_HQ : L_MC, pb = (wid >> 2) * 61440 + l16, pr = opaque_v(pb + wr * 15360), po = opaque_v(61440 - (wid >> 2) * 61440 + wr * 15360 + l16);
        u32 otw = 0, otv[3] = {0, 0, 0};
        if constexpr (OT) {
            otw = opaque_v((wid >> 2) * 61440 + wr * 15360 + 5120 + l15 * 160 + g4 * 40);
#pragma unroll
            for (int i = 0; i < 3; ++i) {
                const u32 pp = lane + 64 * i, v = pp * 16 + ((pp * 205) >> 11) * 1120 + (r64 * 64 + wr * 16) * 1280 + c80 * 160;
                otv[i] = opaque_v(pp < 160 ? v : OOB);
            }
        }
        if constexpr (MK2) { if (!mok) spin(fmo + lane * 4, rws, tag); }
        if (OLATE) ctl_wait8(10);
        SPINTO(25);
        STAMP(25);
        if (wid < 4) __builtin_amdgcn_s_setprio(TPM); else __builtin_amdgcn_s_setprio(TPH);
        sfor<0, NUP>([&](auto T) { sfor<0, 5>([&](auto E) { old(T, E); }); });
        STAMP(26);
        owait(IC<0>{}, IC<5 * (NUP - 1)>{});
        bool bad = marked(omax(0));
        STAMP(27);
        u32x4 snap;
        u32 sn1;
        sfor<0, 4>([&](auto T) {
            constexpr int t = decltype(T)::v;
            if (bad) slow(T);
            u32 mx = 0;
            sfor<0, 25>([&](auto N) {
                constexpr int n = decltype(N)::v, e = n / 5, c = n % 5;
                sched0();
                mfma16a<e == 0>(ac[t & 1][c], asbf8(W[n]), asbf8(O[t][e]));
                sched0();
                if constexpr (t > 0 && n >= 1 && n < 6) sts128(pb + (wr ^ t) * 15360 + (t - 1) * 5120 + (n - 1) * 1024, __builtin_bit_cast(u32x4, ac[(t - 1) & 1][n - 1]));
                if constexpr (t > 0 && n == 6) ctl_set(me, 10 + t);
                if constexpr (t + NUP < 4 && n >= LD0 && n < LD0 + 5 * LDS_ && (n - LD0) % LDS_ == 0) old(IC<t + NUP>{}, IC<(n - LD0) / LDS_>{});
                if constexpr (t < 3) {
                    if constexpr (n == CK0) owait(IC<t + 1>{}, IC<5 * ((t + NUP < 4 ? t + NUP : 3) - t - 1)>{});
                    if constexpr (n >= CK0 && n < CK0 + 5) { mx = mx4(mx, O[t + 1][n - CK0]); mx = opaque_v(mx); }
                } else {
                    if constexpr (n == 0) snap = ctl_get(grp);
                    if constexpr (n == 7) { if (!ctl_ge(snap, 12)) ctl_wait(grp, 12); cfence(); }
                    if constexpr (n >= 7 && n < 12) { O[0][n - 7] = lds128(pr + (n - 7) * 1024); O[1][n - 7] = lds128(pr + 5120 + (n - 7) * 1024); }
                    if constexpr (n == 12) snap = ctl_get(grp);
                    if constexpr (n >= 12 && n < 17) { sm[n - 12] = __builtin_bit_cast(f32x4, O[0][n - 12]) + __builtin_bit_cast(f32x4, O[1][n - 12]); tief(sm[n - 12]); }
                    if constexpr (n == 17) {
                        if (!ctl_ge(snap, 13)) ctl_wait(grp, 13);
                        cfence();
#pragma unroll
                        for (int i = 0; i < 5; ++i) O[0][i] = lds128(pr + 10240 + i * 1024);
                    }
                    if constexpr (n == 19) {
                        sn1 = *(volatile AS3 u32*)(me ^ 16);
                        cfence();
#pragma unroll
                        for (int i = 0; i < 5; ++i) O[1][i] = lds128(po + i * 1024);
                    }
                    if constexpr (n == 22) {
#pragma unroll
                        for (int i = 0; i < 5; ++i) { sm[i] += __builtin_bit_cast(f32x4, O[0][i]); tief(sm[i]); }
                    }
                }
                sched0();
            });
            if constexpr (t < 3) bad = marked(mx);
            if constexpr (t < 3) STAMP(t == 0 ? 28 : t == 1 ? 18 : 29);
        });
        mfma_pad();
        STAMP(19);
#pragma unroll
        for (int c = 0; c < 5; ++c) sm[c] += ac[1][c];
        bool fin = rfl(sn1) >= 14;
        if (!fin) {
#pragma unroll
            for (int c = 0; c < 5; ++c) sts128(pr + c * 1024, __builtin_bit_cast(u32x4, sm[c]));
            ctl_set(me, 14);
            sn1 = *(volatile AS3 u32*)(me ^ 16);
            cfence();
#pragma unroll
            for (int i = 0; i < 5; ++i) O[1][i] = lds128(po + i * 1024);
            fin = rfl(sn1) >= 14;
        }
        if (fin) {
#pragma unroll
            for (int c = 0; c < 5; ++c) sm[c] += __builtin_bit_cast(f32x4, O[1][c]);
            u32 r[10];
#pragma unroll
            for (int c = 0; c < 5; ++c) { r[2 * c] = pk2(sm[c][0], sm[c][1]); r[2 * c + 1] = pk2(sm[c][2], sm[c][3]); }
            const __amdgpu_buffer_rsrc_t ors = __builtin_amdgcn_make_buffer_rsrc(out, 0, 2048 * 1280, 0x00020000);
            if constexpr (OT) {
#pragma unroll
                for (int c = 0; c < 5; ++c) *(volatile AS3 u32x2*)(otw + 8 * c) = u32x2{r[2 * c], r[2 * c + 1]};
                cfence();
                u32x4 t[3];
#pragma unroll
                for (int i = 0; i < 3; ++i) t[i] = lds128(pr + 5120 + i * 1024);
#pragma unroll
                for (int i = 0; i < 3; ++i) __builtin_amdgcn_raw_buffer_store_b128(t[i], ors, otv[i], 0, OAUX);
            } else {
            const u32 vo = l15 * 1280 + g4 * 40 + (r64 * 64 + wr * 16) * 1280 + c80 * 160;
            __builtin_amdgcn_raw_buffer_store_b128(u32x4{r[0], r[1], r[2], r[3]}, ors, vo, 0, OAUX);
            __builtin_amdgcn_raw_buffer_store_b128(u32x4{r[4], r[5], r[6], r[7]}, ors, vo + 16, 0, OAUX);
            __builtin_amdgcn_raw_buffer_store_b64(u32x2{r[8], r[9]}, ors, vo + 32, 0, OAUX);
            }
        }
    }
    SPINTO(22);
    STAMP(22);
    STAMPC(63);
    asm volatile(".Lcode_hi:");
}

extern "C" void kernel_launch(void* const* d_in, const int* in_sizes, int n_in, void* d_out, int out_size, void* d_ws,
                              size_t ws_size, hipStream_t stream) {
    (void)in_sizes; (void)n_in; (void)out_size; (void)ws_size;
    (void)hipFuncSetAttribute((const void*)fused_kernel, hipFuncAttributeMaxDynamicSharedMemorySize, LDS_BYTES);
    fused_kernel<<<256, 512, LDS_BYTES, stream>>>((const u16*)d_in[0], (const u16*)d_in[1], (const u16*)d_in[2], (const u16*)d_in[4],
                                                  (const u16*)d_in[5], (const u16*)d_in[6], (u16*)d_out, (char*)d_ws);
}
```
